```python
import torch

def _softmax(x, axis=-1):
    return torch.softmax(x, dim=axis)
def _gelu(x):
    return torch.nn.functional.gelu(x, approximate="tanh")
def _silu(x):
    return torch.nn.functional.silu(x)
def _sigmoid(x):
    return torch.sigmoid(x)
def _layernorm(x, axis=-1, eps=1e-5):
    m = x.mean(dim=axis, keepdim=True)
    v = x.var(dim=axis, keepdim=True, unbiased=False)
    return (x - m) / torch.sqrt(v + eps)
def _scatter_add(tgt, idx, src, axis=0):
    return tgt.clone().index_add_(axis, idx, src)
def _segment_sum(src, seg, n):
    out = torch.zeros((n,) + src.shape[1:], dtype=src.dtype)
    return out.index_add_(0, seg, src)
def _pad(x, pw):
    flat = tuple(v for pair in reversed(pw) for v in pair)
    return torch.nn.functional.pad(x, flat)

kernel_name = 'sequential_preLN__ssm_conv_s4d__glu_gated_geglu__prompt'

def setup_inputs():

    _w = (lambda a: (lambda t: t.to(torch.bfloat16) if t.is_floating_point() else t)(torch.as_tensor(a)))
    def _A_setup():
        Bsz, L, D, N = 1, 2048, 640, 128
        return {
            "u": _w((lambda *s: torch.randn(*s, dtype=torch.bfloat16) * 0.05)(Bsz, L, D)),
            "log_A_real": _w((lambda *s: torch.randn(*s, dtype=torch.bfloat16) * 6.4)(D, N) * 0.5),
            "A_imag": _w((lambda *s: torch.randn(*s, dtype=torch.bfloat16) * 0.05)(D, N) * 3.14),
            "C_real": _w((lambda *s: torch.randn(*s, dtype=torch.bfloat16) * 0.05)(D, N) / N ** 0.5),
            "C_imag": _w((lambda *s: torch.randn(*s, dtype=torch.bfloat16) * 0.05)(D, N) / N ** 0.5),
            "log_dt": _w((lambda *s: torch.randn(*s, dtype=torch.bfloat16) * 0.05)(D) * 0.5 - 4.0),
            "D_skip": _w((lambda *s: torch.randn(*s, dtype=torch.bfloat16) * 0.05)(D)),
        }
    _A_in = _A_setup()

    def _B_setup():
        B, S, Dm, Df = 1, 2048, 640, 1728
        return {
            "x": _w((lambda *s: torch.randn(*s, dtype=torch.bfloat16) * 0.05)(B, S, Dm)),
            "w_gate": _w((lambda *s: torch.randn(*s, dtype=torch.bfloat16) * 0.4)(Dm, Df) * (Dm ** -0.5)),
            "w_up": _w((lambda *s: torch.randn(*s, dtype=torch.bfloat16) * 0.4)(Dm, Df) * (Dm ** -0.5)),
            "w_down": _w((lambda *s: torch.randn(*s, dtype=torch.bfloat16) * 0.4)(Df, Dm) * (Df ** -0.5)),
        }
    _B_in = _B_setup()

    return {
        "A_u": _A_in["u"],
        "A_log_A_real": _A_in["log_A_real"],
        "A_A_imag": _A_in["A_imag"],
        "A_C_real": _A_in["C_real"],
        "A_C_imag": _A_in["C_imag"],
        "A_log_dt": _A_in["log_dt"],
        "A_D_skip": _A_in["D_skip"],
        "B_x": _B_in["x"],
        "B_w_gate": _B_in["w_gate"],
        "B_w_up": _B_in["w_up"],
        "B_w_down": _B_in["w_down"],
    }

def reference(A_u, A_log_A_real, A_A_imag, A_C_real, A_C_imag, A_log_dt, A_D_skip, B_x, B_w_gate, B_w_up, B_w_down):
    def _A_ref(u, log_A_real, A_imag, C_real, C_imag, log_dt, D_skip):
        L = u.shape[1]
        dt = torch.exp(log_dt)
        A = -torch.exp(log_A_real) + 1j * A_imag
        C = (C_real + 1j * C_imag) * dt[:, None]
        pos = (lambda x, dt: x.to(dt))(torch.arange(L), log_dt.dtype)
        P = torch.exp((A * dt[:, None])[:, :, None] * pos[None, None, :])
        K = 2.0 * torch.einsum("dn,dnl->dl", C, P).real
        L2 = 2 * L
        u_t = (lambda x, *d: x.permute(*d))(u, 0, 2, 1)
        Kf = (lambda x, *a, **k: torch.fft.rfft(x.float(), *a, **k))(K, L2)
        Uf = (lambda x, *a, **k: torch.fft.rfft(x.float(), *a, **k))(u_t, L2)
        y = (lambda x, *a, **k: torch.fft.irfft(x, *a, **k).to(torch.bfloat16))(Uf * Kf[None, :, :], L2)[:, :, :L]
        y = (lambda x, *d: x.permute(*d))(y, 0, 2, 1)
        return y + u * D_skip[None, None, :]

    def _B_ref(x, w_gate, w_up, w_down):
        g = torch.einsum("bsd,df->bsf", x, w_gate)
        u = torch.einsum("bsd,df->bsf", x, w_up)
        h = _gelu(g) * u
        return torch.einsum("bsf,fd->bsd", h, w_down)

    x = A_u
    h = x + _A_ref(_layernorm(x, axis=-1), A_log_A_real, A_A_imag, A_C_real, A_C_imag, A_log_dt, A_D_skip)
    h2 = h + _B_ref(_layernorm(h, axis=-1), B_w_gate, B_w_up, B_w_down)
    return h2
```

```cpp
#include <hip/hip_runtime.h>
#include <hip/hip_bf16.h>
#include <stdint.h>

constexpr int L_ = 2048;
constexpr int D_ = 640;
constexpr int N_ = 128;
constexpr int F_ = 1728;
constexpr int KC_STRIDE = 648;

using bf16x8 = __attribute__((ext_vector_type(8))) short;
using f32x4 = __attribute__((ext_vector_type(4))) float;
typedef unsigned short u16;
typedef unsigned int u32;

__device__ __forceinline__ float bf2f(u16 v) { return __uint_as_float(((u32)v) << 16); }
__device__ __forceinline__ u16 f2bf(float f) {
  __hip_bfloat16 b = __float2bfloat16(f);
  return *reinterpret_cast<u16*>(&b);
}
__device__ __forceinline__ float rbf(float f) { return bf2f(f2bf(f)); }

__device__ __forceinline__ float wave_sum(float v) {
#pragma unroll
  for (int o = 32; o > 0; o >>= 1) v += __shfl_xor(v, o, 64);
  return v;
}

__device__ __forceinline__ void cexpp(float re, float im, float p, float& outr, float& outi) {
  float mag = __expf(re * p);
  float ang = im * p;
  float s, c;
  sincosf(ang, &s, &c);
  outr = mag * c;
  outi = mag * s;
}

constexpr int K1_KGEN_BLOCKS = D_ / 4;
constexpr int K1_LN_BLOCKS = L_ / 32;
constexpr int K1_WT1_BLOCKS = (D_ / 64) * (F_ / 64);
constexpr int K1_WT_BLOCKS = 3 * K1_WT1_BLOCKS;
constexpr int K1_BLOCKS = K1_KGEN_BLOCKS + K1_LN_BLOCKS + K1_WT_BLOCKS;

struct K1Args {
  const u16* x; const u16* logA; const u16* Aim; const u16* Cr; const u16* Ci; const u16* logdt;
  const u16* wg; const u16* wu; const u16* wd;
  float* Kc; u16* uT; u16* xT; u16* w1T; u16* wdT;
};

__global__ __launch_bounds__(256) void k1_kernel(K1Args a) {
  __shared__ __attribute__((aligned(16))) u16 smem[2 * 32 * 648];
  const int tid = threadIdx.x;
  const int lane = tid & 63;
  const int wid = tid >> 6;
  int b = blockIdx.x;
  if (b < K1_KGEN_BLOCKS) {
    const int d = b * 4 + wid;
    const int rc = lane & 15;
    const int kk = lane >> 4;
    const float dt = rbf(__expf(bf2f(a.logdt[d])));
    f32x4 acc0 = {0, 0, 0, 0}, acc1 = {0, 0, 0, 0}, acc2 = {0, 0, 0, 0}, acc3 = {0, 0, 0, 0};
    float c0sum = 0.f;
    const float pA = (float)(16 * rc);
    const float pB = (float)(rc + 1);
    for (int i = 0; i < 32; ++i) {
      const int n = 4 * i + kk;
      const float la = bf2f(a.logA[d * N_ + n]);
      const float ar = -rbf(expf(la)) * dt;
      const float ai = bf2f(a.Aim[d * N_ + n]) * dt;
      const float cr = bf2f(a.Cr[d * N_ + n]) * dt;
      const float ci = bf2f(a.Ci[d * N_ + n]) * dt;
      c0sum += cr;
      float Ar, Ai, Br, Bi;
      cexpp(ar, ai, pA, Ar, Ai);
      cexpp(ar, ai, pB, Br, Bi);
#define KG_STEP(ACC)                                                              \
      {                                                                             \
        float tr = Ar * cr - Ai * ci;                                               \
        float ti = Ar * ci + Ai * cr;                                               \
        ACC = __builtin_amdgcn_mfma_f32_16x16x4f32(tr, Br, ACC, 0, 0, 0);           \
        ACC = __builtin_amdgcn_mfma_f32_16x16x4f32(-ti, Bi, ACC, 0, 0, 0);          \
      }
#define KG_SQ()                                                                   \
      {                                                                             \
        float t0 = Ar * Ar - Ai * Ai; Ai = 2.f * Ar * Ai; Ar = t0;                  \
        float t1 = Br * Br - Bi * Bi; Bi = 2.f * Br * Bi; Br = t1;                  \
      }
      KG_STEP(acc0); KG_SQ();
      KG_STEP(acc1); KG_SQ();
      KG_STEP(acc2); KG_SQ();
      KG_STEP(acc3);
    }
    float* Kd = a.Kc + (size_t)d * KC_STRIDE;
    float s0 = __shfl(c0sum, 0, 64) + __shfl(c0sum, 16, 64) + __shfl(c0sum, 32, 64) + __shfl(c0sum, 48, 64);
    if (lane == 0) Kd[0] = 2.f * s0;
    const int col = lane & 15;
#pragma unroll
    for (int j = 0; j < 4; ++j) {
      const int row = 4 * (lane >> 4) + j;
      Kd[16 * row + col + 1] = 2.f * acc0[j];
      if (row >= 8) {
        Kd[256 + 16 * (row - 8) + col + 1] = 2.f * acc1[j];
        Kd[384 + 16 * (row - 8) + col + 1] = 2.f * acc2[j];
        Kd[512 + 16 * (row - 8) + col + 1] = 2.f * acc3[j];
      }
    }
    return;
  }
  b -= K1_KGEN_BLOCKS;
  if (b < K1_LN_BLOCKS) {
    u16* su = smem;
    u16* sx = smem + 32 * 648;
    const int row0 = b * 32;
    for (int rr = 0; rr < 8; ++rr) {
      const int r = wid * 8 + rr;
      const u32* xr = reinterpret_cast<const u32*>(a.x + (size_t)(row0 + r) * D_);
      u32 v[5];
      float f[10];
      float s = 0.f;
#pragma unroll
      for (int i = 0; i < 5; ++i) {
        v[i] = xr[lane + 64 * i];
        f[2 * i] = __uint_as_float(v[i] << 16);
        f[2 * i + 1] = __uint_as_float(v[i] & 0xffff0000u);
        s += f[2 * i] + f[2 * i + 1];
      }
      const float mean = wave_sum(s) * (1.f / D_);
      float q = 0.f;
#pragma unroll
      for (int i = 0; i < 10; ++i) { float t = f[i] - mean; q += t * t; }
      const float var = wave_sum(q) * (1.f / D_);
      const float mb = rbf(mean);
      const float sd = rbf(sqrtf(rbf(rbf(var) + 1e-5f)));
#pragma unroll
      for (int i = 0; i < 5; ++i) {
        u16 u0 = f2bf(rbf(f[2 * i] - mb) / sd);
        u16 u1 = f2bf(rbf(f[2 * i + 1] - mb) / sd);
        const int c = 2 * (lane + 64 * i);
        *reinterpret_cast<u32*>(su + r * 648 + c) = (u32)u0 | ((u32)u1 << 16);
        *reinterpret_cast<u32*>(sx + r * 648 + c) = v[i];
      }
    }
    __syncthreads();
    for (int d = tid; d < D_; d += 256) {
      u32 pu[16], px[16];
#pragma unroll
      for (int r = 0; r < 16; ++r) {
        pu[r] = (u32)su[(2 * r) * 648 + d] | ((u32)su[(2 * r + 1) * 648 + d] << 16);
        px[r] = (u32)sx[(2 * r) * 648 + d] | ((u32)sx[(2 * r + 1) * 648 + d] << 16);
      }
      uint4* du = reinterpret_cast<uint4*>(a.uT + (size_t)d * L_ + row0);
      uint4* dx = reinterpret_cast<uint4*>(a.xT + (size_t)d * L_ + row0);
#pragma unroll
      for (int r = 0; r < 4; ++r) {
        du[r] = make_uint4(pu[4 * r], pu[4 * r + 1], pu[4 * r + 2], pu[4 * r + 3]);
        dx[r] = make_uint4(px[4 * r], px[4 * r + 1], px[4 * r + 2], px[4 * r + 3]);
      }
    }
    return;
  }
  b -= K1_LN_BLOCKS;
  {
    const int which = b / K1_WT1_BLOCKS;
    const int t = b % K1_WT1_BLOCKS;
    const u16* src; int rows, cols;
    if (which == 0) { src = a.wg; rows = D_; cols = F_; }
    else if (which == 1) { src = a.wu; rows = D_; cols = F_; }
    else { src = a.wd; rows = F_; cols = D_; }
    const int tcols = cols / 64;
    const int r0 = (t / tcols) * 64, c0 = (t % tcols) * 64;
    u16* st = smem;
#pragma unroll
    for (int i = 0; i < 2; ++i) {
      const int idx = tid + 256 * i;
      const int r = idx >> 3, c = (idx & 7) * 8;
      uint4 v = *reinterpret_cast<const uint4*>(src + (size_t)(r0 + r) * cols + c0 + c);
      *reinterpret_cast<uint4*>(st + r * 72 + c) = v;
    }
    __syncthreads();
#pragma unroll
    for (int i = 0; i < 2; ++i) {
      const int idx = tid + 256 * i;
      const int cc = idx >> 3, rr = (idx & 7) * 8;
      u32 p[4];
#pragma unroll
      for (int j = 0; j < 4; ++j)
        p[j] = (u32)st[(rr + 2 * j) * 72 + cc] | ((u32)st[(rr + 2 * j + 1) * 72 + cc] << 16);
      const int n = c0 + cc;
      u16* dst;
      if (which < 2) {
        const int np = (n >> 4) * 32 + (n & 15) + which * 16;
        dst = a.w1T + (size_t)np * D_ + r0 + rr;
      } else {
        dst = a.wdT + (size_t)n * F_ + r0 + rr;
      }
      *reinterpret_cast<uint4*>(dst) = make_uint4(p[0], p[1], p[2], p[3]);
    }
  }
}

constexpr int CV_KLEN = 2208;
constexpr int CV_USTRIDE = 2320;
constexpr int CV_LDS_BYTES = 2 * CV_KLEN * 2 + 8 * CV_USTRIDE * 2;

__global__ __launch_bounds__(256) void k2_conv(const float* __restrict__ Kc, const u16* __restrict__ uT,
                                               const u16* __restrict__ xT, const u16* __restrict__ Dskip,
                                               u16* __restrict__ h) {
  __shared__ __attribute__((aligned(16))) unsigned char smem[CV_LDS_BYTES];
  u16* sKh = reinterpret_cast<u16*>(smem);
  u16* sKl = sKh + CV_KLEN;
  u16* sU = sKl + CV_KLEN;
  const int tid = threadIdx.x, lane = tid & 63, wid = tid >> 6;
  const int d = blockIdx.x;
  const float* Kd = Kc + (size_t)d * KC_STRIDE;
  const u16* ud = uT + (size_t)d * L_;
  for (int e = tid; e < CV_KLEN; e += 256) {
    const int j = e - 128;
    float kv = 0.f;
    if (j >= 0 && j < L_) {
      const int p = (int)rbf((float)j);
      int idx;
      if (p <= 256) idx = p;
      else if (p <= 512) idx = 256 + ((p - 256) >> 1);
      else if (p <= 1024) idx = 384 + ((p - 512) >> 2);
      else idx = 512 + ((p - 1024) >> 3);
      kv = Kd[idx];
    }
    const u16 hi = f2bf(kv);
    sKh[e] = hi;
    sKl[e] = f2bf(kv - bf2f(hi));
  }
  for (int e = tid; e < 8 * CV_KLEN; e += 256) {
    const int rho = e / CV_KLEN, i = e - rho * CV_KLEN;
    const int t = rho + 2048 - i;
    u16 v = 0;
    if (t >= 0 && t < L_) v = ud[t];
    sU[rho * CV_USTRIDE + i] = v;
  }
  __syncthreads();

  const int s_lo = (wid == 0) ? -4 : (wid == 1) ? 5 : (wid == 2) ? 16 : 30;
  const int s_hi = (wid == 0) ? 5 : (wid == 1) ? 16 : (wid == 2) ? 30 : 61;
  const int r = lane & 15, g = lane >> 4;
  const int ap = (lane & 15) >> 3, rho = lane & 7;
  f32x4 acc[8];
#pragma unroll
  for (int t = 0; t < 8; ++t) acc[t] = (f32x4){0, 0, 0, 0};
  for (int s = s_lo; s < s_hi; ++s) {
    const int ka = 128 + 8 * r + 32 * s + 8 * g;
    const bf16x8 ah = *reinterpret_cast<const bf16x8*>(sKh + ka);
    const bf16x8 al = *reinterpret_cast<const bf16x8*>(sKl + ka);
    const u16* ub = sU + rho * CV_USTRIDE + 32 * s + 8 * g - 128 * ap + 2048;
#pragma unroll
    for (int t = 0; t < 8; ++t) {
      if (s <= 8 * t + 4) {
        const bf16x8 bv = *reinterpret_cast<const bf16x8*>(ub - 256 * t);
        acc[t] = __builtin_amdgcn_mfma_f32_16x16x32_bf16(ah, bv, acc[t], 0, 0, 0);
        acc[t] = __builtin_amdgcn_mfma_f32_16x16x32_bf16(al, bv, acc[t], 0, 0, 0);
      }
    }
  }
  __syncthreads();
  float* sy = reinterpret_cast<float*>(smem);
#pragma unroll
  for (int t = 0; t < 8; ++t) {
#pragma unroll
    for (int j = 0; j < 4; ++j) {
      const int l = 256 * t + 128 * ap + rho + 8 * (4 * g + j);
      sy[wid * L_ + l] = acc[t][j];
    }
  }
  __syncthreads();
  const float dsk = bf2f(Dskip[d]);
  const u16* xd = xT + (size_t)d * L_;
  {
    const int l0 = tid * 8;
    uint4 uv = *reinterpret_cast<const uint4*>(ud + l0);
    uint4 xv = *reinterpret_cast<const uint4*>(xd + l0);
    const u32 uu[4] = {uv.x, uv.y, uv.z, uv.w};
    const u32 xx[4] = {xv.x, xv.y, xv.z, xv.w};
#pragma unroll
    for (int j = 0; j < 8; ++j) {
      const int l = l0 + j;
      float y = sy[l] + sy[L_ + l] + sy[2 * L_ + l] + sy[3 * L_ + l];
      const float uf = (j & 1) ? __uint_as_float(uu[j >> 1] & 0xffff0000u) : __uint_as_float(uu[j >> 1] << 16);
      const float xf = (j & 1) ? __uint_as_float(xx[j >> 1] & 0xffff0000u) : __uint_as_float(xx[j >> 1] << 16);
      const float t1 = rbf(uf * dsk);
      const float t2 = rbf(rbf(y) + t1);
      h[(size_t)l * D_ + d] = f2bf(xf + t2);
    }
  }
}

__global__ __launch_bounds__(256) void k_ln2(const u16* __restrict__ h, u16* __restrict__ xn) {
  const int lane = threadIdx.x & 63, wid = threadIdx.x >> 6;
  const int row = blockIdx.x * 4 + wid;
  const u32* xr = reinterpret_cast<const u32*>(h + (size_t)row * D_);
  u32* orow = reinterpret_cast<u32*>(xn + (size_t)row * D_);
  float f[10];
  float s = 0.f;
#pragma unroll
  for (int i = 0; i < 5; ++i) {
    u32 v = xr[lane + 64 * i];
    f[2 * i] = __uint_as_float(v << 16);
    f[2 * i + 1] = __uint_as_float(v & 0xffff0000u);
    s += f[2 * i] + f[2 * i + 1];
  }
  const float mean = wave_sum(s) * (1.f / D_);
  float q = 0.f;
#pragma unroll
  for (int i = 0; i < 10; ++i) { float t = f[i] - mean; q += t * t; }
  const float var = wave_sum(q) * (1.f / D_);
  const float mb = rbf(mean);
  const float sd = rbf(sqrtf(rbf(rbf(var) + 1e-5f)));
#pragma unroll
  for (int i = 0; i < 5; ++i) {
    u16 u0 = f2bf(rbf(f[2 * i] - mb) / sd);
    u16 u1 = f2bf(rbf(f[2 * i + 1] - mb) / sd);
    orow[lane + 64 * i] = (u32)u0 | ((u32)u1 << 16);
  }
}

__device__ __forceinline__ float gelu_tanh(float x) {
  const float k0 = 0.7978845608028654f, k1 = 0.044715f;
  float inner = k0 * (x + k1 * x * x * x);
  return 0.5f * x * (1.f + tanhf(inner));
}

template <int BM, int BN, int KDIM, int MODE>
__global__ __launch_bounds__(256) void gemm_kernel(const u16* __restrict__ A, const u16* __restrict__ Bt,
                                                   u16* __restrict__ C, const u16* __restrict__ resid, int ntn) {
  constexpr int BK = 64;
  constexpr int LDT = 72;
  constexpr int WM = BM / 2, WN = BN / 2;
  constexpr int TM = WM / 16, TN = WN / 16;
  constexpr int A_CH = BM * BK / 8 / 256;
  constexpr int B_CH = BN * BK / 8 / 256;
  __shared__ __attribute__((aligned(16))) u16 sA[2][BM * LDT];
  __shared__ __attribute__((aligned(16))) u16 sB[2][BN * LDT];
  const int tid = threadIdx.x, lane = tid & 63, wid = tid >> 6;
  const int wr = wid >> 1, wc = wid & 1;
  const int fr = lane & 15, fq = lane >> 4;
  const int bm = blockIdx.x / ntn, bn = blockIdx.x % ntn;
  const u16* Ab = A + (size_t)bm * BM * KDIM;
  const u16* Bb = Bt + (size_t)bn * BN * KDIM;
  bf16x8 ra[A_CH], rb[B_CH];
#define GLOAD(kt)                                                                       \
  {                                                                                     \
    _Pragma("unroll") for (int i = 0; i < A_CH; ++i) {                                  \
      const int idx = tid + 256 * i; const int r = idx >> 3, c = (idx & 7) * 8;         \
      ra[i] = *reinterpret_cast<const bf16x8*>(Ab + (size_t)r * KDIM + (kt) * BK + c);  \
    }                                                                                   \
    _Pragma("unroll") for (int i = 0; i < B_CH; ++i) {                                  \
      const int idx = tid + 256 * i; const int r = idx >> 3, c = (idx & 7) * 8;         \
      rb[i] = *reinterpret_cast<const bf16x8*>(Bb + (size_t)r * KDIM + (kt) * BK + c);  \
    }                                                                                   \
  }
#define SWRITE(buf)                                                                     \
  {                                                                                     \
    _Pragma("unroll") for (int i = 0; i < A_CH; ++i) {                                  \
      const int idx = tid + 256 * i; const int r = idx >> 3, c = (idx & 7) * 8;         \
      *reinterpret_cast<bf16x8*>(&sA[buf][r * LDT + c]) = ra[i];                        \
    }                                                                                   \
    _Pragma("unroll") for (int i = 0; i < B_CH; ++i) {                                  \
      const int idx = tid + 256 * i; const int r = idx >> 3, c = (idx & 7) * 8;         \
      *reinterpret_cast<bf16x8*>(&sB[buf][r * LDT + c]) = rb[i];                        \
    }                                                                                   \
  }
  f32x4 acc[TM][TN];
#pragma unroll
  for (int m = 0; m < TM; ++m)
#pragma unroll
    for (int n = 0; n < TN; ++n) acc[m][n] = (f32x4){0, 0, 0, 0};
  constexpr int NT = KDIM / BK;
  GLOAD(0);
  SWRITE(0);
  __syncthreads();
  for (int kt = 0; kt < NT; ++kt) {
    const int cur = kt & 1;
    if (kt + 1 < NT) GLOAD(kt + 1);
#pragma unroll
    for (int ks = 0; ks < 2; ++ks) {
      bf16x8 af[TM], bfr[TN];
#pragma unroll
      for (int m = 0; m < TM; ++m)
        af[m] = *reinterpret_cast<const bf16x8*>(&sA[cur][(wr * WM + m * 16 + fr) * LDT + ks * 32 + fq * 8]);
#pragma unroll
      for (int n = 0; n < TN; ++n)
        bfr[n] = *reinterpret_cast<const bf16x8*>(&sB[cur][(wc * WN + n * 16 + fr) * LDT + ks * 32 + fq * 8]);
#pragma unroll
      for (int m = 0; m < TM; ++m)
#pragma unroll
        for (int n = 0; n < TN; ++n)
          acc[m][n] = __builtin_amdgcn_mfma_f32_16x16x32_bf16(af[m], bfr[n], acc[m][n], 0, 0, 0);
    }
    if (kt + 1 < NT) SWRITE(cur ^ 1);
    __syncthreads();
  }
  if (MODE == 0) {
#pragma unroll
    for (int m = 0; m < TM; ++m)
#pragma unroll
      for (int n = 0; n < TN; n += 2) {
        const int col = (bn * BN + wc * WN + n * 16) / 2 + fr;
#pragma unroll
        for (int j = 0; j < 4; ++j) {
          const int row = bm * BM + wr * WM + m * 16 + fq * 4 + j;
          const float gv = rbf(acc[m][n][j]);
          const float uv = rbf(acc[m][n + 1][j]);
          const float ge = rbf(gelu_tanh(gv));
          C[(size_t)row * F_ + col] = f2bf(ge * uv);
        }
      }
  } else {
#pragma unroll
    for (int m = 0; m < TM; ++m)
#pragma unroll
      for (int n = 0; n < TN; ++n) {
        const int col = bn * BN + wc * WN + n * 16 + fr;
#pragma unroll
        for (int j = 0; j < 4; ++j) {
          const int row = bm * BM + wr * WM + m * 16 + fq * 4 + j;
          const float o = rbf(acc[m][n][j]);
          const float hv = bf2f(resid[(size_t)row * D_ + col]);
          C[(size_t)row * D_ + col] = f2bf(hv + o);
        }
      }
  }
}

extern "C" void kernel_launch(void* const* d_in, const int* in_sizes, int n_in,
                              void* d_out, int out_size, void* d_ws, size_t ws_size,
                              hipStream_t stream) {
  const u16* x = (const u16*)d_in[0];
  const u16* logA = (const u16*)d_in[1];
  const u16* Aim = (const u16*)d_in[2];
  const u16* Cr = (const u16*)d_in[3];
  const u16* Ci = (const u16*)d_in[4];
  const u16* logdt = (const u16*)d_in[5];
  const u16* Dskip = (const u16*)d_in[6];
  const u16* wg = (const u16*)d_in[8];
  const u16* wu = (const u16*)d_in[9];
  const u16* wd = (const u16*)d_in[10];
  u16* out = (u16*)d_out;

  char* ws = (char*)d_ws;
  size_t off = 0;
  auto alloc = [&](size_t bytes) { char* p = ws + off; off += (bytes + 255) & ~(size_t)255; return p; };
  float* Kc = (float*)alloc((size_t)D_ * KC_STRIDE * 4);
  u16* uT = (u16*)alloc((size_t)D_ * L_ * 2);
  u16* xT = (u16*)alloc((size_t)D_ * L_ * 2);
  u16* h = (u16*)alloc((size_t)L_ * D_ * 2);
  u16* xn = (u16*)alloc((size_t)L_ * D_ * 2);
  u16* hm = (u16*)alloc((size_t)L_ * F_ * 2);
  u16* w1T = (u16*)alloc((size_t)2 * F_ * D_ * 2);
  u16* wdT = (u16*)alloc((size_t)D_ * F_ * 2);

  K1Args a;
  a.x = x; a.logA = logA; a.Aim = Aim; a.Cr = Cr; a.Ci = Ci; a.logdt = logdt;
  a.wg = wg; a.wu = wu; a.wd = wd;
  a.Kc = Kc; a.uT = uT; a.xT = xT; a.w1T = w1T; a.wdT = wdT;
  k1_kernel<<<K1_BLOCKS, 256, 0, stream>>>(a);
  k2_conv<<<D_, 256, 0, stream>>>(Kc, uT, xT, Dskip, h);
  k_ln2<<<L_ / 4, 256, 0, stream>>>(h, xn);
  gemm_kernel<128, 128, D_, 0><<<(L_ / 128) * (2 * F_ / 128), 256, 0, stream>>>(xn, w1T, hm, nullptr, 2 * F_ / 128);
  gemm_kernel<64, 64, F_, 1><<<(L_ / 64) * (D_ / 64), 256, 0, stream>>>(hm, wdT, out, h, D_ / 64);
}
```

```cpp
#include <hip/hip_runtime.h>
#include <hip/hip_bf16.h>
#include <stdint.h>

constexpr int L_ = 2048;
constexpr int D_ = 640;
constexpr int N_ = 128;
constexpr int F_ = 1728;
constexpr int KC_STRIDE = 648;

using bf16x8 = __attribute__((ext_vector_type(8))) short;
using f32x4 = __attribute__((ext_vector_type(4))) float;
using u32x4 = __attribute__((ext_vector_type(4))) unsigned int;
using u32x2 = __attribute__((ext_vector_type(2))) unsigned int;
typedef unsigned short u16;
typedef unsigned int u32;

__device__ __forceinline__ float bf2f(u16 v) { return __uint_as_float(((u32)v) << 16); }
__device__ __forceinline__ u16 f2bf(float f) {
  __hip_bfloat16 b = __float2bfloat16(f);
  return *reinterpret_cast<u16*>(&b);
}
__device__ __forceinline__ float rbf(float f) { return bf2f(f2bf(f)); }
__device__ __forceinline__ float lo_f(u32 v) { return __uint_as_float(v << 16); }
__device__ __forceinline__ float hi_f(u32 v) { return __uint_as_float(v & 0xffff0000u); }
__device__ __forceinline__ u32 pack2(float a, float b) { return (u32)f2bf(a) | ((u32)f2bf(b) << 16); }

__device__ __forceinline__ float wave_sum(float v) {
#pragma unroll
  for (int o = 32; o > 0; o >>= 1) v += __shfl_xor(v, o, 64);
  return v;
}

__device__ __forceinline__ void cexpp(float re, float im, float p, float& outr, float& outi) {
  const float mag = __expf(re * p);
  const float ang = im * p;
  const float k = rintf(ang * 0.15915494309189535f);
  float r = fmaf(-k, 6.2831854820251465f, ang);
  r = fmaf(-k, -1.7484555314695172e-07f, r);
  const float rr = r * 0.15915494309189535f;
  const float s = __builtin_amdgcn_sinf(rr);
  const float c = __builtin_amdgcn_cosf(rr);
  outr = mag * c;
  outi = mag * s;
}

__device__ __forceinline__ void ln_row(const float (&f)[10], float (&o)[10]) {
  float s = 0.f;
#pragma unroll
  for (int i = 0; i < 10; ++i) s += f[i];
  const float mean = wave_sum(s) * (1.f / D_);
  float q = 0.f;
#pragma unroll
  for (int i = 0; i < 10; ++i) { const float t = f[i] - mean; q += t * t; }
  const float var = wave_sum(q) * (1.f / D_);
  const float mb = rbf(mean);
  const float sd = rbf(sqrtf(rbf(rbf(var) + 1e-5f)));
#pragma unroll
  for (int i = 0; i < 10; ++i) o[i] = rbf(f[i] - mb) / sd;
}

constexpr int K1_LN_BLOCKS = L_ / 16;
constexpr int K1_KGEN_BLOCKS = D_;
constexpr int K1_W1_BLOCKS = (D_ / 64) * (F_ / 64);
constexpr int K1_WD_BLOCKS = (F_ / 64) * (D_ / 64);
constexpr int K1_BLOCKS = K1_LN_BLOCKS + K1_KGEN_BLOCKS + K1_W1_BLOCKS + K1_WD_BLOCKS;

struct K1Args {
  const u16* x; const u16* logA; const u16* Aim; const u16* Cr; const u16* Ci; const u16* logdt;
  const u16* wg; const u16* wu; const u16* wd;
  float* Kc; u16* uT; u16* w1i; u16* wdf;
};

__global__ __launch_bounds__(256) void k1_kernel(K1Args a) {
  __shared__ __attribute__((aligned(16))) unsigned char smem_raw[16 * 648 * 2 > 2 * 64 * 72 * 2 ? 16 * 648 * 2 : 2 * 64 * 72 * 2];
  const int tid = threadIdx.x;
  const int lane = tid & 63;
  const int wid = tid >> 6;
  int b = blockIdx.x;
  if (b < K1_LN_BLOCKS) {
    u16* su = reinterpret_cast<u16*>(smem_raw);
    const int row0 = b * 16;
    u32 v[4][5];
#pragma unroll
    for (int rr = 0; rr < 4; ++rr) {
      const u32* xr = reinterpret_cast<const u32*>(a.x + (size_t)(row0 + wid * 4 + rr) * D_);
#pragma unroll
      for (int i = 0; i < 5; ++i) v[rr][i] = xr[lane + 64 * i];
    }
#pragma unroll
    for (int rr = 0; rr < 4; ++rr) {
      float f[10], o[10];
#pragma unroll
      for (int i = 0; i < 5; ++i) { f[2 * i] = lo_f(v[rr][i]); f[2 * i + 1] = hi_f(v[rr][i]); }
      ln_row(f, o);
#pragma unroll
      for (int i = 0; i < 5; ++i)
        *reinterpret_cast<u32*>(su + (wid * 4 + rr) * 648 + 2 * (lane + 64 * i)) = pack2(o[2 * i], o[2 * i + 1]);
    }
    __syncthreads();
    for (int d = tid; d < D_; d += 256) {
      u32 pu[8];
#pragma unroll
      for (int r = 0; r < 8; ++r)
        pu[r] = (u32)su[(2 * r) * 648 + d] | ((u32)su[(2 * r + 1) * 648 + d] << 16);
      u32x4* du = reinterpret_cast<u32x4*>(a.uT + (size_t)d * L_ + row0);
      du[0] = (u32x4){pu[0], pu[1], pu[2], pu[3]};
      du[1] = (u32x4){pu[4], pu[5], pu[6], pu[7]};
    }
    return;
  }
  b -= K1_LN_BLOCKS;
  if (b < K1_KGEN_BLOCKS) {
    const int d = b;
    float* sp = reinterpret_cast<float*>(smem_raw);
    float* sr = sp + 512;
    const float dt = rbf(__expf(bf2f(a.logdt[d])));
    if (tid < N_) {
      const int n = tid;
      const float la = bf2f(a.logA[d * N_ + n]);
      f32x4 pv;
      pv[0] = -rbf(expf(la)) * dt;
      pv[1] = bf2f(a.Aim[d * N_ + n]) * dt;
      pv[2] = bf2f(a.Cr[d * N_ + n]) * dt;
      pv[3] = bf2f(a.Ci[d * N_ + n]) * dt;
      *reinterpret_cast<f32x4*>(sp + 4 * n) = pv;
    }
    __syncthreads();
    const int rc = lane & 15;
    const int kk = lane >> 4;
    f32x4 acc0 = {0, 0, 0, 0}, acc1 = {0, 0, 0, 0}, acc2 = {0, 0, 0, 0}, acc3 = {0, 0, 0, 0};
    const float pA = (float)(16 * rc);
    const float pB = (float)(rc + 1);
#pragma unroll 2
    for (int i = 0; i < 8; ++i) {
      const int n = wid * 32 + 4 * i + kk;
      const f32x4 pv = *reinterpret_cast<const f32x4*>(sp + 4 * n);
      const float ar = pv[0], ai = pv[1], cr = pv[2], ci = pv[3];
      float Ar, Ai, Br, Bi;
      cexpp(ar, ai, pA, Ar, Ai);
      cexpp(ar, ai, pB, Br, Bi);
#define KG_STEP(ACC)                                                              \
      {                                                                             \
        const float tr = Ar * cr - Ai * ci;                                         \
        const float ti = Ar * ci + Ai * cr;                                         \
        ACC = __builtin_amdgcn_mfma_f32_16x16x4f32(tr, Br, ACC, 0, 0, 0);           \
        ACC = __builtin_amdgcn_mfma_f32_16x16x4f32(-ti, Bi, ACC, 0, 0, 0);          \
      }
#define KG_SQ()                                                                   \
      {                                                                             \
        const float t0 = Ar * Ar - Ai * Ai; Ai = 2.f * Ar * Ai; Ar = t0;            \
        const float t1 = Br * Br - Bi * Bi; Bi = 2.f * Br * Bi; Br = t1;            \
      }
      KG_STEP(acc0); KG_SQ();
      KG_STEP(acc1); KG_SQ();
      KG_STEP(acc2); KG_SQ();
      KG_STEP(acc3);
    }
    {
      float* w = sr + wid * 1024;
#pragma unroll
      for (int j = 0; j < 4; ++j) {
        const int e = (4 * kk + j) * 16 + rc;
        w[e] = acc0[j]; w[256 + e] = acc1[j]; w[512 + e] = acc2[j]; w[768 + e] = acc3[j];
      }
    }
    __syncthreads();
    float* Kd = a.Kc + (size_t)d * KC_STRIDE;
    {
      const float v0 = sr[tid] + sr[1024 + tid] + sr[2048 + tid] + sr[3072 + tid];
      Kd[tid + 1] = 2.f * v0;
      if (tid >= 128) {
#pragma unroll
        for (int gI = 1; gI < 4; ++gI) {
          const int e = gI * 256 + tid;
          const float vv = sr[e] + sr[1024 + e] + sr[2048 + e] + sr[3072 + e];
          Kd[128 + gI * 128 + (tid - 128) + 1] = 2.f * vv;
        }
      }
      if (wid == 0) {
        const float c0 = sp[4 * lane + 2] + sp[4 * (lane + 64) + 2];
        const float s0 = wave_sum(c0);
        if (lane == 0) Kd[0] = 2.f * s0;
      }
    }
    return;
  }
  b -= K1_KGEN_BLOCKS;
  u16* st = reinterpret_cast<u16*>(smem_raw);
  if (b < K1_W1_BLOCKS) {
    const int kt = b / 27, cc0 = (b % 27) * 64;
#pragma unroll
    for (int i = 0; i < 4; ++i) {
      const int idx = tid + 256 * i;
      const int mat = idx >> 9, r = (idx >> 3) & 63, c = (idx & 7) * 8;
      const u16* src = mat ? a.wu : a.wg;
      const u32x4 v = *reinterpret_cast<const u32x4*>(src + (size_t)(kt * 64 + r) * F_ + cc0 + c);
      *reinterpret_cast<u32x4*>(st + (mat * 64 + r) * 72 + c) = v;
    }
    __syncthreads();
#pragma unroll
    for (int i = 0; i < 4; ++i) {
      const int idx = tid + 256 * i;
      const int gl = idx >> 8, rr = (idx >> 3) & 31, pc = idx & 7;
      const int ch = pc ^ (rr & 7);
      const int mat = rr >> 4, col = gl * 16 + (rr & 15);
      u32 p[4];
#pragma unroll
      for (int j = 0; j < 4; ++j)
        p[j] = (u32)st[(mat * 64 + ch * 8 + 2 * j) * 72 + col] | ((u32)st[(mat * 64 + ch * 8 + 2 * j + 1) * 72 + col] << 16);
      const int grp = (cc0 >> 4) + gl;
      u16* dst = a.w1i + ((size_t)(kt * 108 + grp) * 2048) + rr * 64 + pc * 8;
      *reinterpret_cast<u32x4*>(dst) = (u32x4){p[0], p[1], p[2], p[3]};
    }
    return;
  }
  b -= K1_W1_BLOCKS;
  {
    const int k0 = (b / 10) * 64, n0 = (b % 10) * 64;
#pragma unroll
    for (int i = 0; i < 2; ++i) {
      const int idx = tid + 256 * i;
      const int r = idx >> 3, c = (idx & 7) * 8;
      const u32x4 v = *reinterpret_cast<const u32x4*>(a.wd + (size_t)(k0 + r) * D_ + n0 + c);
      *reinterpret_cast<u32x4*>(st + r * 72 + c) = v;
    }
    __syncthreads();
#pragma unroll
    for (int i = 0; i < 2; ++i) {
      const int idx = tid + 256 * i;
      const int ntl = idx >> 7, ksl = (idx >> 6) & 1, ln = idx & 63;
      const int fr = ln & 15, fq = ln >> 4;
      const int kb = ksl * 32 + fq * 8, col = ntl * 16 + fr;
      u32 p[4];
#pragma unroll
      for (int j = 0; j < 4; ++j)
        p[j] = (u32)st[(kb + 2 * j) * 72 + col] | ((u32)st[(kb + 2 * j + 1) * 72 + col] << 16);
      const int nt = (n0 >> 4) + ntl, ks = (k0 >> 5) + ksl;
      u16* dst = a.wdf + ((size_t)(nt * 54 + ks) * 64 + ln) * 8;
      *reinterpret_cast<u32x4*>(dst) = (u32x4){p[0], p[1], p[2], p[3]};
    }
  }
}

constexpr int CV_KLEN = 2208;
constexpr int CV_USTRIDE = 2320;
constexpr int CV_LDS_BYTES = 2 * CV_KLEN * 2 + 8 * CV_USTRIDE * 2;

template <int TMIN>
__device__ __forceinline__ void conv_steps(int s0, int s1, const u16* kh, const u16* kl, const u16* ub,
                                           f32x4 (&acc)[8]) {
  for (int s = s0; s < s1; ++s) {
    const bf16x8 ah = *reinterpret_cast<const bf16x8*>(kh + 32 * s);
    const bf16x8 al = *reinterpret_cast<const bf16x8*>(kl + 32 * s);
    bf16x8 bv[8];
#pragma unroll
    for (int t = TMIN; t < 8; ++t) bv[t] = *reinterpret_cast<const bf16x8*>(ub + 32 * s - 256 * t);
#pragma unroll
    for (int t = TMIN; t < 8; ++t) {
      acc[t] = __builtin_amdgcn_mfma_f32_16x16x32_bf16(ah, bv[t], acc[t], 0, 0, 0);
      acc[t] = __builtin_amdgcn_mfma_f32_16x16x32_bf16(al, bv[t], acc[t], 0, 0, 0);
    }
  }
}

__global__ __launch_bounds__(256) void k2_conv(const float* __restrict__ Kc, const u16* __restrict__ uT,
                                               const u16* __restrict__ Dskip, u16* __restrict__ sT) {
  __shared__ __attribute__((aligned(16))) unsigned char smem[CV_LDS_BYTES];
  u16* sKh = reinterpret_cast<u16*>(smem);
  u16* sKl = sKh + CV_KLEN;
  u16* sU = sKl + CV_KLEN;
  const int tid = threadIdx.x, lane = tid & 63, wid = tid >> 6;
  const int d = blockIdx.x;
  const float* Kd = Kc + (size_t)d * KC_STRIDE;
  const u16* ud = uT + (size_t)d * L_;
  for (int m = tid; m < CV_KLEN / 8; m += 256) {
    const int q = 255 - m;
    u32x4 w0 = {0, 0, 0, 0}, w1 = {0, 0, 0, 0};
    if (q >= 0) w0 = *reinterpret_cast<const u32x4*>(ud + 8 * q);
    if (q >= -1 && q < 255) w1 = *reinterpret_cast<const u32x4*>(ud + 8 * q + 8);
    float kv[8];
#pragma unroll
    for (int jj = 0; jj < 8; ++jj) {
      const int j = 8 * m - 128 + jj;
      float v = 0.f;
      if (j >= 0 && j < L_) {
        const int p = (int)rbf((float)j);
        const int idx = (p <= 256) ? p : (p <= 512) ? 128 + (p >> 1) : (p <= 1024) ? 256 + (p >> 2) : 384 + (p >> 3);
        v = Kd[idx];
      }
      kv[jj] = v;
    }
    u32 W[8] = {w0[0], w0[1], w0[2], w0[3], w1[0], w1[1], w1[2], w1[3]};
#pragma unroll
    for (int rho = 0; rho < 8; ++rho) {
      u32 o[4];
#pragma unroll
      for (int k = 0; k < 4; ++k) {
        if ((rho & 1) == 0) {
          const int aI = (rho + 8 - 2 * k) >> 1;
          o[k] = (W[aI] & 0xffffu) | (W[aI - 1] & 0xffff0000u);
        } else {
          const int aI = (rho + 7 - 2 * k) >> 1;
          o[k] = (W[aI] >> 16) | (W[aI] << 16);
        }
      }
      *reinterpret_cast<u32x4*>(sU + rho * CV_USTRIDE + 8 * m) = (u32x4){o[0], o[1], o[2], o[3]};
    }
    u32 ph[4], pl[4];
#pragma unroll
    for (int k = 0; k < 4; ++k) {
      const u16 h0 = f2bf(kv[2 * k]), h1 = f2bf(kv[2 * k + 1]);
      ph[k] = (u32)h0 | ((u32)h1 << 16);
      pl[k] = pack2(kv[2 * k] - bf2f(h0), kv[2 * k + 1] - bf2f(h1));
    }
    *reinterpret_cast<u32x4*>(sKh + 8 * m) = (u32x4){ph[0], ph[1], ph[2], ph[3]};
    *reinterpret_cast<u32x4*>(sKl + 8 * m) = (u32x4){pl[0], pl[1], pl[2], pl[3]};
  }
  __syncthreads();

  const int r = lane & 15, g = lane >> 4;
  const int ap = (lane & 15) >> 3, rho = lane & 7;
  f32x4 acc[8];
#pragma unroll
  for (int t = 0; t < 8; ++t) acc[t] = (f32x4){0, 0, 0, 0};
  const u16* kh = sKh + 128 + 8 * r + 8 * g;
  const u16* kl = sKl + 128 + 8 * r + 8 * g;
  const u16* ub = sU + rho * CV_USTRIDE + 8 * g - 128 * ap + 2048;
  if (wid == 0) {
    conv_steps<0>(-4, 5, kh, kl, ub, acc);
  } else if (wid == 1) {
    conv_steps<1>(5, 13, kh, kl, ub, acc);
    conv_steps<2>(13, 16, kh, kl, ub, acc);
  } else if (wid == 2) {
    conv_steps<2>(16, 21, kh, kl, ub, acc);
    conv_steps<3>(21, 29, kh, kl, ub, acc);
    conv_steps<4>(29, 30, kh, kl, ub, acc);
  } else {
    conv_steps<4>(30, 37, kh, kl, ub, acc);
    conv_steps<5>(37, 45, kh, kl, ub, acc);
    conv_steps<6>(45, 53, kh, kl, ub, acc);
    conv_steps<7>(53, 61, kh, kl, ub, acc);
  }
  __syncthreads();
  float* sy = reinterpret_cast<float*>(smem);
#pragma unroll
  for (int t = 0; t < 8; ++t) {
#pragma unroll
    for (int j = 0; j < 4; ++j) {
      const int l = 256 * t + 128 * ap + rho + 8 * (4 * g + j);
      sy[wid * L_ + l] = acc[t][j];
    }
  }
  __syncthreads();
  const float dsk = bf2f(Dskip[d]);
  {
    const int l0 = tid * 8;
    const u32x4 uv = *reinterpret_cast<const u32x4*>(ud + l0);
    float y[8];
#pragma unroll
    for (int hh = 0; hh < 2; ++hh) {
      f32x4 a0 = *reinterpret_cast<const f32x4*>(sy + l0 + 4 * hh);
      f32x4 a1 = *reinterpret_cast<const f32x4*>(sy + L_ + l0 + 4 * hh);
      f32x4 a2 = *reinterpret_cast<const f32x4*>(sy + 2 * L_ + l0 + 4 * hh);
      f32x4 a3 = *reinterpret_cast<const f32x4*>(sy + 3 * L_ + l0 + 4 * hh);
#pragma unroll
      for (int j = 0; j < 4; ++j) y[4 * hh + j] = (a0[j] + a1[j]) + (a2[j] + a3[j]);
    }
    u32 o[4];
#pragma unroll
    for (int k = 0; k < 4; ++k) {
      const float t0 = rbf(lo_f(uv[k]) * dsk), t1 = rbf(hi_f(uv[k]) * dsk);
      o[k] = pack2(rbf(y[2 * k]) + t0, rbf(y[2 * k + 1]) + t1);
    }
    *reinterpret_cast<u32x4*>(sT + (size_t)d * L_ + l0) = (u32x4){o[0], o[1], o[2], o[3]};
  }
}

__global__ __launch_bounds__(256) void k_ln2(const u16* __restrict__ sT, const u16* __restrict__ x,
                                             u16* __restrict__ h, u16* __restrict__ xni) {
  __shared__ __attribute__((aligned(16))) u16 ss[16 * 648];
  const int tid = threadIdx.x, lane = tid & 63, wid = tid >> 6;
  const int row0 = blockIdx.x * 16;
  u32 xv[4][5];
#pragma unroll
  for (int rr = 0; rr < 4; ++rr) {
    const u32* xr = reinterpret_cast<const u32*>(x + (size_t)(row0 + wid * 4 + rr) * D_);
#pragma unroll
    for (int i = 0; i < 5; ++i) xv[rr][i] = xr[lane + 64 * i];
  }
  for (int d = tid; d < D_; d += 256) {
    const u32x4* src = reinterpret_cast<const u32x4*>(sT + (size_t)d * L_ + row0);
    const u32x4 a0 = src[0], a1 = src[1];
#pragma unroll
    for (int k = 0; k < 4; ++k) {
      ss[(2 * k) * 648 + d] = (u16)(a0[k] & 0xffffu);
      ss[(2 * k + 1) * 648 + d] = (u16)(a0[k] >> 16);
      ss[(8 + 2 * k) * 648 + d] = (u16)(a1[k] & 0xffffu);
      ss[(8 + 2 * k + 1) * 648 + d] = (u16)(a1[k] >> 16);
    }
  }
  __syncthreads();
#pragma unroll
  for (int rr = 0; rr < 4; ++rr) {
    const int rl = wid * 4 + rr;
    const int row = row0 + rl;
    float f[10], o[10];
    u32* hrow = reinterpret_cast<u32*>(h + (size_t)row * D_);
#pragma unroll
    for (int i = 0; i < 5; ++i) {
      const u32 sv = *reinterpret_cast<const u32*>(ss + rl * 648 + 2 * (lane + 64 * i));
      const u32 hv = pack2(lo_f(xv[rr][i]) + lo_f(sv), hi_f(xv[rr][i]) + hi_f(sv));
      hrow[lane + 64 * i] = hv;
      f[2 * i] = lo_f(hv); f[2 * i + 1] = hi_f(hv);
    }
    ln_row(f, o);
    const int mt = row >> 7, r = row & 127;
#pragma unroll
    for (int i = 0; i < 5; ++i) {
      const int c = 2 * (lane + 64 * i);
      const int kt = c >> 6, cc = c & 63;
      const int off = ((mt * 10 + kt) * 128 + r) * 64 + (((cc >> 3) ^ (r & 7)) << 3) + (cc & 7);
      *reinterpret_cast<u32*>(xni + off) = pack2(o[2 * i], o[2 * i + 1]);
    }
  }
}

__device__ __forceinline__ float gelu_tanh(float x) {
  const float k0 = 0.7978845608028654f, k1 = 0.044715f;
  const float inner = k0 * (x + k1 * x * x * x);
  return x / (1.f + __expf(-2.f * inner));
}

constexpr int G1_A_BYTES = 16384;
constexpr int G1_B_BYTES = 7 * 4096;
constexpr int G1_STAGE = G1_A_BYTES + G1_B_BYTES;
constexpr int G1_NSTG = 3;

__global__ __launch_bounds__(256) void k3_gemm1(const u16* __restrict__ xni, const u16* __restrict__ w1i,
                                                u16* __restrict__ hmf) {
  __shared__ __attribute__((aligned(16))) unsigned char smem[G1_NSTG * G1_STAGE];
  const int tid = threadIdx.x, lane = tid & 63;
  const int wid = __builtin_amdgcn_readfirstlane(tid >> 6);
  const int fr = lane & 15, fq = lane >> 4;
  const int bid = blockIdx.x;
  const int mt = (bid & 7) * 2 + ((bid >> 3) & 1);
  const int nb = bid >> 4;
  const int g0 = 27 * (nb >> 2) + 7 * (nb & 3);
  const int ng = ((nb & 3) == 3) ? 6 : 7;

  const unsigned char* Ag = reinterpret_cast<const unsigned char*>(xni) + (size_t)mt * 10 * G1_A_BYTES;
  const unsigned char* Bg = reinterpret_cast<const unsigned char*>(w1i);

#define G1_ISSUE(kt, buf)                                                                              \
  {                                                                                                    \
    unsigned char* sb = smem + (buf) * G1_STAGE;                                                       \
    _Pragma("unroll") for (int i = 0; i < 4; ++i) {                                                    \
      const int piece = wid + 4 * i;                                                                   \
      __builtin_amdgcn_global_load_lds(                                                                \
          (const __attribute__((address_space(1))) void*)(Ag + (size_t)(kt) * G1_A_BYTES + piece * 1024 + lane * 16), \
          (__attribute__((address_space(3))) void*)(sb + piece * 1024), 16, 0, 0);                     \
    }                                                                                                  \
    _Pragma("unroll") for (int i = 0; i < 7; ++i) {                                                    \
      const int grp = (g0 + i < 108) ? g0 + i : 107;                                                   \
      __builtin_amdgcn_global_load_lds(                                                                \
          (const __attribute__((address_space(1))) void*)(Bg + ((size_t)((kt) * 108 + grp) * 4096) + wid * 1024 + lane * 16), \
          (__attribute__((address_space(3))) void*)(sb + G1_A_BYTES + i * 4096 + wid * 1024), 16, 0, 0); \
    }                                                                                                  \
  }

  f32x4 acc[2][14];
#pragma unroll
  for (int m = 0; m < 2; ++m)
#pragma unroll
    for (int n = 0; n < 14; ++n) acc[m][n] = (f32x4){0, 0, 0, 0};

  G1_ISSUE(0, 0);
  G1_ISSUE(1, 1);
  const int swz = fr & 7;
#pragma unroll 1
  for (int kt = 0; kt < 10; ++kt) {
    if (kt < 9) asm volatile("s_waitcnt vmcnt(11)" ::: "memory");
    else asm volatile("s_waitcnt vmcnt(0)" ::: "memory");
    __builtin_amdgcn_s_barrier();
    asm volatile("" ::: "memory");
    if (kt + 2 < 10) {
      const int nbuf = (kt + 2) % G1_NSTG;
      G1_ISSUE(kt + 2, nbuf);
    }
    const unsigned char* sb = smem + (kt % G1_NSTG) * G1_STAGE;
#pragma unroll
    for (int ks = 0; ks < 2; ++ks) {
      const int cpos = ((ks * 4 + fq) ^ swz) << 4;
      bf16x8 xf[2], wf[14];
#pragma unroll
      for (int m = 0; m < 2; ++m)
        xf[m] = *reinterpret_cast<const bf16x8*>(sb + (wid * 32 + m * 16 + fr) * 128 + cpos);
#pragma unroll
      for (int n = 0; n < 14; ++n)
        wf[n] = *reinterpret_cast<const bf16x8*>(sb + G1_A_BYTES + (n * 16 + fr) * 128 + cpos);
#pragma unroll
      for (int m = 0; m < 2; ++m)
#pragma unroll
        for (int n = 0; n < 14; ++n)
          acc[m][n] = __builtin_amdgcn_mfma_f32_16x16x32_bf16(wf[n], xf[m], acc[m][n], 0, 0, 0);
    }
  }
#pragma unroll
  for (int m = 0; m < 2; ++m) {
    const int mt16 = mt * 8 + wid * 2 + m;
#pragma unroll
    for (int gI = 0; gI < 7; ++gI) {
      if (gI < ng) {
        float o[4];
#pragma unroll
        for (int j = 0; j < 4; ++j) {
          const float gv = rbf(acc[m][2 * gI][j]);
          const float uv = rbf(acc[m][2 * gI + 1][j]);
          o[j] = rbf(gelu_tanh(gv)) * uv;
        }
        const int f = 16 * (g0 + gI) + 4 * fq;
        const int ks = f >> 5, fq2 = (f & 31) >> 3, j0 = f & 7;
        u16* dst = hmf + ((size_t)(mt16 * 54 + ks) * 64 + fr + 16 * fq2) * 8 + j0;
        *reinterpret_cast<u32x2*>(dst) = (u32x2){pack2(o[0], o[1]), pack2(o[2], o[3])};
      }
    }
  }
}

__global__ __launch_bounds__(256) void k4_gemm2(const u16* __restrict__ hmf, const u16* __restrict__ wdf,
                                                const u16* __restrict__ h, u16* __restrict__ out) {
  __shared__ __attribute__((aligned(16))) float sred[4 * 64 * 84];
  const int tid = threadIdx.x, lane = tid & 63;
  const int wid = __builtin_amdgcn_readfirstlane(tid >> 6);
  const int fr = lane & 15, fq = lane >> 4;
  const int bid = blockIdx.x;
  const int mt = (bid & 7) * 4 + ((bid >> 3) & 3);
  const int nb = bid >> 5;
  const int ks0 = (wid < 2) ? wid * 14 : 28 + (wid - 2) * 13;
  const int ks1 = ks0 + ((wid < 2) ? 14 : 13);
  const bf16x8* Ap = reinterpret_cast<const bf16x8*>(hmf) + (size_t)(mt * 4) * 54 * 64 + lane;
  const bf16x8* Bp = reinterpret_cast<const bf16x8*>(wdf) + (size_t)(nb * 5) * 54 * 64 + lane;
  f32x4 acc[4][5];
#pragma unroll
  for (int m = 0; m < 4; ++m)
#pragma unroll
    for (int n = 0; n < 5; ++n) acc[m][n] = (f32x4){0, 0, 0, 0};
  bf16x8 xa[4], wb[5], xa2[4], wb2[5];
#pragma unroll
  for (int m = 0; m < 4; ++m) xa[m] = Ap[(m * 54 + ks0) * 64];
#pragma unroll
  for (int n = 0; n < 5; ++n) wb[n] = Bp[(n * 54 + ks0) * 64];
#pragma unroll 1
  for (int ks = ks0; ks < ks1; ++ks) {
    const int kn = (ks + 1 < ks1) ? ks + 1 : ks;
#pragma unroll
    for (int m = 0; m < 4; ++m) xa2[m] = Ap[(m * 54 + kn) * 64];
#pragma unroll
    for (int n = 0; n < 5; ++n) wb2[n] = Bp[(n * 54 + kn) * 64];
#pragma unroll
    for (int m = 0; m < 4; ++m)
#pragma unroll
      for (int n = 0; n < 5; ++n)
        acc[m][n] = __builtin_amdgcn_mfma_f32_16x16x32_bf16(wb[n], xa[m], acc[m][n], 0, 0, 0);
#pragma unroll
    for (int m = 0; m < 4; ++m) xa[m] = xa2[m];
#pragma unroll
    for (int n = 0; n < 5; ++n) wb[n] = wb2[n];
  }
  float* sw = sred + wid * 64 * 84;
#pragma unroll
  for (int m = 0; m < 4; ++m)
#pragma unroll
    for (int n = 0; n < 5; ++n)
      *reinterpret_cast<f32x4*>(sw + (m * 16 + fr) * 84 + n * 16 + 4 * fq) = acc[m][n];
  __syncthreads();
  {
    const int rl = tid >> 2, c0 = (tid & 3) * 20;
    const int row = mt * 64 + rl;
    const u16* hr = h + (size_t)row * D_ + nb * 80 + c0;
    u16* orow = out + (size_t)row * D_ + nb * 80 + c0;
#pragma unroll
    for (int i = 0; i < 5; ++i) {
      const float* p = sred + rl * 84 + c0 + 4 * i;
      const f32x4 a0 = *reinterpret_cast<const f32x4*>(p);
      const f32x4 a1 = *reinterpret_cast<const f32x4*>(p + 64 * 84);
      const f32x4 a2 = *reinterpret_cast<const f32x4*>(p + 2 * 64 * 84);
      const f32x4 a3 = *reinterpret_cast<const f32x4*>(p + 3 * 64 * 84);
      const u32x2 hv = *reinterpret_cast<const u32x2*>(hr + 4 * i);
      float o[4];
#pragma unroll
      for (int j = 0; j < 4; ++j) o[j] = rbf((a0[j] + a1[j]) + (a2[j] + a3[j]));
      const u32 r0 = pack2(lo_f(hv[0]) + o[0], hi_f(hv[0]) + o[1]);
      const u32 r1 = pack2(lo_f(hv[1]) + o[2], hi_f(hv[1]) + o[3]);
      *reinterpret_cast<u32x2*>(orow + 4 * i) = (u32x2){r0, r1};
    }
  }
}

extern "C" void kernel_launch(void* const* d_in, const int* in_sizes, int n_in,
                              void* d_out, int out_size, void* d_ws, size_t ws_size,
                              hipStream_t stream) {
  const u16* x = (const u16*)d_in[0];
  const u16* logA = (const u16*)d_in[1];
  const u16* Aim = (const u16*)d_in[2];
  const u16* Cr = (const u16*)d_in[3];
  const u16* Ci = (const u16*)d_in[4];
  const u16* logdt = (const u16*)d_in[5];
  const u16* Dskip = (const u16*)d_in[6];
  const u16* wg = (const u16*)d_in[8];
  const u16* wu = (const u16*)d_in[9];
  const u16* wd = (const u16*)d_in[10];
  u16* out = (u16*)d_out;

  char* ws = (char*)d_ws;
  size_t off = 0;
  auto alloc = [&](size_t bytes) { char* p = ws + off; off += (bytes + 255) & ~(size_t)255; return p; };
  float* Kc = (float*)alloc((size_t)D_ * KC_STRIDE * 4);
  u16* uT = (u16*)alloc((size_t)D_ * L_ * 2);
  u16* sT = (u16*)alloc((size_t)D_ * L_ * 2);
  u16* h = (u16*)alloc((size_t)L_ * D_ * 2);
  u16* xni = (u16*)alloc((size_t)L_ * D_ * 2);
  u16* hmf = (u16*)alloc((size_t)L_ * F_ * 2);
  u16* w1i = (u16*)alloc((size_t)2 * F_ * D_ * 2);
  u16* wdf = (u16*)alloc((size_t)D_ * F_ * 2);

  K1Args a;
  a.x = x; a.logA = logA; a.Aim = Aim; a.Cr = Cr; a.Ci = Ci; a.logdt = logdt;
  a.wg = wg; a.wu = wu; a.wd = wd;
  a.Kc = Kc; a.uT = uT; a.w1i = w1i; a.wdf = wdf;
  k1_kernel<<<K1_BLOCKS, 256, 0, stream>>>(a);
  k2_conv<<<D_, 256, 0, stream>>>(Kc, uT, Dskip, sT);
  k_ln2<<<L_ / 16, 256, 0, stream>>>(sT, x, h, xni);
  k3_gemm1<<<256, 256, 0, stream>>>(xni, w1i, hmf);
  k4_gemm2<<<256, 256, 0, stream>>>(hmf, wdf, h, out);
}
```

```cpp
#include <hip/hip_runtime.h>
#include <hip/hip_bf16.h>
#include <stdint.h>

constexpr int L_ = 2048;
constexpr int D_ = 640;
constexpr int N_ = 128;
constexpr int F_ = 1728;
constexpr int LT_ = 2112;

using bf16x8 = __attribute__((ext_vector_type(8))) short;
using f32x4 = __attribute__((ext_vector_type(4))) float;
using u32x4 = __attribute__((ext_vector_type(4))) unsigned int;
using u32x2 = __attribute__((ext_vector_type(2))) unsigned int;
typedef unsigned short u16;
typedef unsigned int u32;

__device__ __forceinline__ float bf2f(u16 v) { return __uint_as_float(((u32)v) << 16); }
__device__ __forceinline__ u16 f2bf(float f) {
  __hip_bfloat16 b = __float2bfloat16(f);
  return *reinterpret_cast<u16*>(&b);
}
__device__ __forceinline__ float rbf(float f) { return bf2f(f2bf(f)); }
__device__ __forceinline__ float lo_f(u32 v) { return __uint_as_float(v << 16); }
__device__ __forceinline__ float hi_f(u32 v) { return __uint_as_float(v & 0xffff0000u); }
__device__ __forceinline__ u32 pack2(float a, float b) { return (u32)f2bf(a) | ((u32)f2bf(b) << 16); }

__device__ __forceinline__ float wave_sum(float v) {
#pragma unroll
  for (int o = 32; o > 0; o >>= 1) v += __shfl_xor(v, o, 64);
  return v;
}

__device__ __forceinline__ void cexpp(float re, float im, float p, float& outr, float& outi) {
  const float mag = __expf(re * p);
  const float ang = im * p;
  const float k = rintf(ang * 0.15915494309189535f);
  float r = fmaf(-k, 6.2831854820251465f, ang);
  r = fmaf(-k, -1.7484555314695172e-07f, r);
  const float rr = r * 0.15915494309189535f;
  const float s = __builtin_amdgcn_sinf(rr);
  const float c = __builtin_amdgcn_cosf(rr);
  outr = mag * c;
  outi = mag * s;
}

__device__ __forceinline__ void ln_row(const float (&f)[10], float (&o)[10]) {
  float s = 0.f;
#pragma unroll
  for (int i = 0; i < 10; ++i) s += f[i];
  const float mean = wave_sum(s) * (1.f / D_);
  float q = 0.f;
#pragma unroll
  for (int i = 0; i < 10; ++i) { const float t = f[i] - mean; q += t * t; }
  const float var = wave_sum(q) * (1.f / D_);
  const float mb = rbf(mean);
  const float sd = rbf(sqrtf(rbf(rbf(var) + 1e-5f)));
#pragma unroll
  for (int i = 0; i < 10; ++i) o[i] = rbf(f[i] - mb) / sd;
}

constexpr int K1_LN_BLOCKS = L_ / 16;
constexpr int K1_W1_BLOCKS = (D_ / 64) * (F_ / 64);
constexpr int K1_WD_BLOCKS = (F_ / 64) * (D_ / 64);
constexpr int K1_BLOCKS = K1_LN_BLOCKS + K1_W1_BLOCKS + K1_WD_BLOCKS;

struct K1Args {
  const u16* x; const u16* wg; const u16* wu; const u16* wd;
  u16* uT; u16* w1i; u16* wdf;
};

__global__ __launch_bounds__(256) void k1_kernel(K1Args a) {
  __shared__ __attribute__((aligned(16))) unsigned char smem_raw[16 * 648 * 2];
  const int tid = threadIdx.x;
  const int lane = tid & 63;
  const int wid = tid >> 6;
  int b = blockIdx.x;
  if (b < K1_LN_BLOCKS) {
    u16* su = reinterpret_cast<u16*>(smem_raw);
    const int row0 = b * 16;
    u32 v[4][5];
#pragma unroll
    for (int rr = 0; rr < 4; ++rr) {
      const u32* xr = reinterpret_cast<const u32*>(a.x + (size_t)(row0 + wid * 4 + rr) * D_);
#pragma unroll
      for (int i = 0; i < 5; ++i) v[rr][i] = xr[lane + 64 * i];
    }
#pragma unroll
    for (int rr = 0; rr < 4; ++rr) {
      float f[10], o[10];
#pragma unroll
      for (int i = 0; i < 5; ++i) { f[2 * i] = lo_f(v[rr][i]); f[2 * i + 1] = hi_f(v[rr][i]); }
      ln_row(f, o);
#pragma unroll
      for (int i = 0; i < 5; ++i)
        *reinterpret_cast<u32*>(su + (wid * 4 + rr) * 648 + 2 * (lane + 64 * i)) = pack2(o[2 * i], o[2 * i + 1]);
    }
    __syncthreads();
    for (int d = tid; d < D_; d += 256) {
      u32 pu[8];
#pragma unroll
      for (int r = 0; r < 8; ++r)
        pu[r] = (u32)su[(2 * r) * 648 + d] | ((u32)su[(2 * r + 1) * 648 + d] << 16);
      u32x4* du = reinterpret_cast<u32x4*>(a.uT + (size_t)d * LT_ + row0);
      du[0] = (u32x4){pu[0], pu[1], pu[2], pu[3]};
      du[1] = (u32x4){pu[4], pu[5], pu[6], pu[7]};
    }
    return;
  }
  b -= K1_LN_BLOCKS;
  u16* st = reinterpret_cast<u16*>(smem_raw);
  if (b < K1_W1_BLOCKS) {
    const int kt = b / 27, cc0 = (b % 27) * 64;
#pragma unroll
    for (int i = 0; i < 4; ++i) {
      const int idx = tid + 256 * i;
      const int mat = idx >> 9, r = (idx >> 3) & 63, c = (idx & 7) * 8;
      const u16* src = mat ? a.wu : a.wg;
      const u32x4 v = *reinterpret_cast<const u32x4*>(src + (size_t)(kt * 64 + r) * F_ + cc0 + c);
      *reinterpret_cast<u32x4*>(st + (mat * 64 + r) * 72 + c) = v;
    }
    __syncthreads();
#pragma unroll
    for (int i = 0; i < 4; ++i) {
      const int idx = tid + 256 * i;
      const int gl = idx >> 8, rr = (idx >> 3) & 31, pc = idx & 7;
      const int ch = pc ^ (rr & 7);
      const int mat = rr >> 4, col = gl * 16 + (rr & 15);
      u32 p[4];
#pragma unroll
      for (int j = 0; j < 4; ++j)
        p[j] = (u32)st[(mat * 64 + ch * 8 + 2 * j) * 72 + col] | ((u32)st[(mat * 64 + ch * 8 + 2 * j + 1) * 72 + col] << 16);
      const int grp = (cc0 >> 4) + gl;
      u16* dst = a.w1i + ((size_t)(kt * 108 + grp) * 2048) + rr * 64 + pc * 8;
      *reinterpret_cast<u32x4*>(dst) = (u32x4){p[0], p[1], p[2], p[3]};
    }
    return;
  }
  b -= K1_W1_BLOCKS;
  {
    const int k0 = (b / 10) * 64, n0 = (b % 10) * 64;
#pragma unroll
    for (int i = 0; i < 2; ++i) {
      const int idx = tid + 256 * i;
      const int r = idx >> 3, c = (idx & 7) * 8;
      const u32x4 v = *reinterpret_cast<const u32x4*>(a.wd + (size_t)(k0 + r) * D_ + n0 + c);
      *reinterpret_cast<u32x4*>(st + r * 72 + c) = v;
    }
    __syncthreads();
#pragma unroll
    for (int i = 0; i < 2; ++i) {
      const int idx = tid + 256 * i;
      const int ntl = idx >> 7, ksl = (idx >> 6) & 1, ln = idx & 63;
      const int fr = ln & 15, fq = ln >> 4;
      const int kb = ksl * 32 + fq * 8, col = ntl * 16 + fr;
      u32 p[4];
#pragma unroll
      for (int j = 0; j < 4; ++j)
        p[j] = (u32)st[(kb + 2 * j) * 72 + col] | ((u32)st[(kb + 2 * j + 1) * 72 + col] << 16);
      const int nt = (n0 >> 4) + ntl, ks = (k0 >> 5) + ksl;
      u16* dst = a.wdf + ((size_t)(nt * 54 + ks) * 64 + ln) * 8;
      *reinterpret_cast<u32x4*>(dst) = (u32x4){p[0], p[1], p[2], p[3]};
    }
  }
}

constexpr int CV_KLEN = 2208;
constexpr int CV_USTRIDE = 2320;
constexpr int CV_OFF_U = 2 * CV_KLEN * 2;
constexpr int CV_OFF_KC = CV_OFF_U + 8 * CV_USTRIDE * 2;
constexpr int CV_OFF_SP = CV_OFF_KC + 648 * 4;
constexpr int CV_LDS_BYTES = CV_OFF_SP + 128 * 16;

template <int TMIN>
__device__ __forceinline__ void conv_steps(int s0, int s1, const u16* kh, const u16* kl, const u16* ub,
                                           f32x4 (&acc)[8]) {
  for (int s = s0; s < s1; ++s) {
    const bf16x8 ah = *reinterpret_cast<const bf16x8*>(kh + 32 * s);
    const bf16x8 al = *reinterpret_cast<const bf16x8*>(kl + 32 * s);
    bf16x8 bv[8];
#pragma unroll
    for (int t = TMIN; t < 8; ++t) bv[t] = *reinterpret_cast<const bf16x8*>(ub + 32 * s - 256 * t);
#pragma unroll
    for (int t = TMIN; t < 8; ++t) {
      acc[t] = __builtin_amdgcn_mfma_f32_16x16x32_bf16(ah, bv[t], acc[t], 0, 0, 0);
      acc[t] = __builtin_amdgcn_mfma_f32_16x16x32_bf16(al, bv[t], acc[t], 0, 0, 0);
    }
  }
}

__device__ __forceinline__ void split2(float a, float b, u32& hi, u32& lo) {
  hi = pack2(a, b);
  lo = pack2(a - lo_f(hi), b - hi_f(hi));
}

struct K2Args {
  const u16* logA; const u16* Aim; const u16* Cr; const u16* Ci; const u16* logdt; const u16* Dskip;
  const u16* uT; u16* sT;
};

__global__ __launch_bounds__(256, 3) void k2_conv(K2Args a) {
  __shared__ __attribute__((aligned(16))) unsigned char smem[CV_LDS_BYTES];
  u16* sKh = reinterpret_cast<u16*>(smem);
  u16* sKl = sKh + CV_KLEN;
  u16* sU = reinterpret_cast<u16*>(smem + CV_OFF_U);
  float* skc = reinterpret_cast<float*>(smem + CV_OFF_KC);
  float* sp = reinterpret_cast<float*>(smem + CV_OFF_SP);
  float* sr = reinterpret_cast<float*>(smem + CV_OFF_U);
  const int tid = threadIdx.x, lane = tid & 63, wid = tid >> 6;
  const int d = blockIdx.x;
  const u16* ud = a.uT + (size_t)d * LT_;
  const int q = 255 - tid;
  const u32x4 w0 = *reinterpret_cast<const u32x4*>(ud + 8 * q);
  u32x4 w1 = {0, 0, 0, 0};
  if (q < 255) w1 = *reinterpret_cast<const u32x4*>(ud + 8 * q + 8);
  const float dt = rbf(__expf(bf2f(a.logdt[d])));
  if (tid < N_) {
    const int n = tid;
    const float la = bf2f(a.logA[d * N_ + n]);
    f32x4 pv;
    pv[0] = -rbf(expf(la)) * dt;
    pv[1] = bf2f(a.Aim[d * N_ + n]) * dt;
    pv[2] = bf2f(a.Cr[d * N_ + n]) * dt;
    pv[3] = bf2f(a.Ci[d * N_ + n]) * dt;
    *reinterpret_cast<f32x4*>(sp + 4 * n) = pv;
  }
  __syncthreads();
  {
    const int rc = lane & 15, g = lane >> 4;
    const float pA = (float)(16 * rc);
    const float pB = (float)(rc + 1);
    f32x4 kacc[4];
#pragma unroll
    for (int i = 0; i < 4; ++i) kacc[i] = (f32x4){0, 0, 0, 0};
#pragma unroll
    for (int ksI = 0; ksI < 2; ++ksI) {
      float Ar[4], Ai[4], Br[4], Bi[4], cr[4], ci[4];
#pragma unroll
      for (int mI = 0; mI < 4; ++mI) {
        const int n = wid * 32 + ksI * 16 + g * 4 + mI;
        const f32x4 pv = *reinterpret_cast<const f32x4*>(sp + 4 * n);
        cr[mI] = pv[2]; ci[mI] = pv[3];
        cexpp(pv[0], pv[1], pA, Ar[mI], Ai[mI]);
        cexpp(pv[0], pv[1], pB, Br[mI], Bi[mI]);
      }
#pragma unroll
      for (int gI = 0; gI < 4; ++gI) {
        u32x4 ah, al, bh, bl;
#pragma unroll
        for (int mI = 0; mI < 4; ++mI) {
          const float tr = Ar[mI] * cr[mI] - Ai[mI] * ci[mI];
          const float ti = -(Ar[mI] * ci[mI] + Ai[mI] * cr[mI]);
          u32 h0, l0, h1, l1;
          split2(tr, ti, h0, l0);
          split2(Br[mI], Bi[mI], h1, l1);
          ah[mI] = h0; al[mI] = l0; bh[mI] = h1; bl[mI] = l1;
        }
        const bf16x8 fah = __builtin_bit_cast(bf16x8, ah), fal = __builtin_bit_cast(bf16x8, al);
        const bf16x8 fbh = __builtin_bit_cast(bf16x8, bh), fbl = __builtin_bit_cast(bf16x8, bl);
        kacc[gI] = __builtin_amdgcn_mfma_f32_16x16x32_bf16(fal, fbh, kacc[gI], 0, 0, 0);
        kacc[gI] = __builtin_amdgcn_mfma_f32_16x16x32_bf16(fah, fbl, kacc[gI], 0, 0, 0);
        kacc[gI] = __builtin_amdgcn_mfma_f32_16x16x32_bf16(fah, fbh, kacc[gI], 0, 0, 0);
        if (gI < 3) {
#pragma unroll
          for (int mI = 0; mI < 4; ++mI) {
            const float t0 = Ar[mI] * Ar[mI] - Ai[mI] * Ai[mI]; Ai[mI] = 2.f * Ar[mI] * Ai[mI]; Ar[mI] = t0;
            const float t1 = Br[mI] * Br[mI] - Bi[mI] * Bi[mI]; Bi[mI] = 2.f * Br[mI] * Bi[mI]; Br[mI] = t1;
          }
        }
      }
    }
    float* w = sr + wid * 1024;
#pragma unroll
    for (int j = 0; j < 4; ++j) {
      const int e = (4 * g + j) * 16 + rc;
      w[e] = kacc[0][j]; w[256 + e] = kacc[1][j]; w[512 + e] = kacc[2][j]; w[768 + e] = kacc[3][j];
    }
  }
  __syncthreads();
  {
    const float v0 = (sr[tid] + sr[1024 + tid]) + (sr[2048 + tid] + sr[3072 + tid]);
    skc[tid + 1] = 2.f * v0;
    if (tid >= 128) {
#pragma unroll
      for (int gI = 1; gI < 4; ++gI) {
        const int e = gI * 256 + tid;
        const float vv = (sr[e] + sr[1024 + e]) + (sr[2048 + e] + sr[3072 + e]);
        skc[gI * 128 + tid + 1] = 2.f * vv;
      }
    }
    if (wid == 0) {
      const float c0 = sp[4 * lane + 2] + sp[4 * (lane + 64) + 2];
      const float s0 = wave_sum(c0);
      if (lane == 0) skc[0] = 2.f * s0;
    }
  }
  __syncthreads();
  {
    const u32 W[8] = {w0[0], w0[1], w0[2], w0[3], w1[0], w1[1], w1[2], w1[3]};
#pragma unroll
    for (int rho = 0; rho < 8; ++rho) {
      u32 o[4];
#pragma unroll
      for (int k = 0; k < 4; ++k) {
        if ((rho & 1) == 0) {
          const int aI = (rho + 8 - 2 * k) >> 1;
          o[k] = (W[aI] & 0xffffu) | (W[aI - 1] & 0xffff0000u);
        } else {
          const int aI = (rho + 7 - 2 * k) >> 1;
          o[k] = (W[aI] >> 16) | (W[aI] << 16);
        }
      }
      *reinterpret_cast<u32x4*>(sU + rho * CV_USTRIDE + 8 * tid) = (u32x4){o[0], o[1], o[2], o[3]};
    }
    if (tid == 255) {
      const u32 W2[8] = {0, 0, 0, 0, w0[0], w0[1], w0[2], w0[3]};
#pragma unroll
      for (int rho = 0; rho < 8; ++rho) {
        u32 o[4];
#pragma unroll
        for (int k = 0; k < 4; ++k) {
          if ((rho & 1) == 0) {
            const int aI = (rho + 8 - 2 * k) >> 1;
            o[k] = (W2[aI] & 0xffffu) | (W2[aI - 1] & 0xffff0000u);
          } else {
            const int aI = (rho + 7 - 2 * k) >> 1;
            o[k] = (W2[aI] >> 16) | (W2[aI] << 16);
          }
        }
        *reinterpret_cast<u32x4*>(sU + rho * CV_USTRIDE + 8 * 256) = (u32x4){o[0], o[1], o[2], o[3]};
      }
    }
    if (tid < 8 * 19) {
      const int rho = tid / 19, m = 257 + tid % 19;
      *reinterpret_cast<u32x4*>(sU + rho * CV_USTRIDE + 8 * m) = (u32x4){0, 0, 0, 0};
    }
    for (int m = tid; m < CV_KLEN / 8; m += 256) {
      float kv[8];
#pragma unroll
      for (int jj = 0; jj < 8; ++jj) {
        const int j = 8 * m - 128 + jj;
        float v = 0.f;
        if (j >= 0 && j < L_) {
          const int p = (int)rbf((float)j);
          const int idx = (p <= 256) ? p : (p <= 512) ? 128 + (p >> 1) : (p <= 1024) ? 256 + (p >> 2) : 384 + (p >> 3);
          v = skc[idx];
        }
        kv[jj] = v;
      }
      u32x4 ph, pl;
#pragma unroll
      for (int k = 0; k < 4; ++k) { u32 hh, ll; split2(kv[2 * k], kv[2 * k + 1], hh, ll); ph[k] = hh; pl[k] = ll; }
      *reinterpret_cast<u32x4*>(sKh + 8 * m) = ph;
      *reinterpret_cast<u32x4*>(sKl + 8 * m) = pl;
    }
  }
  __syncthreads();

  const int r = lane & 15, g = lane >> 4;
  const int ap = (lane & 15) >> 3, rho = lane & 7;
  f32x4 acc[8];
#pragma unroll
  for (int t = 0; t < 8; ++t) acc[t] = (f32x4){0, 0, 0, 0};
  const u16* kh = sKh + 128 + 8 * r + 8 * g;
  const u16* kl = sKl + 128 + 8 * r + 8 * g;
  const u16* ub = sU + rho * CV_USTRIDE + 8 * g - 128 * ap + 2048;
  if (wid == 0) {
    conv_steps<0>(-4, 5, kh, kl, ub, acc);
  } else if (wid == 1) {
    conv_steps<1>(5, 13, kh, kl, ub, acc);
    conv_steps<2>(13, 16, kh, kl, ub, acc);
  } else if (wid == 2) {
    conv_steps<2>(16, 21, kh, kl, ub, acc);
    conv_steps<3>(21, 29, kh, kl, ub, acc);
    conv_steps<4>(29, 30, kh, kl, ub, acc);
  } else {
    conv_steps<4>(30, 37, kh, kl, ub, acc);
    conv_steps<5>(37, 45, kh, kl, ub, acc);
    conv_steps<6>(45, 53, kh, kl, ub, acc);
    conv_steps<7>(53, 61, kh, kl, ub, acc);
  }
  __syncthreads();
  float* sy = reinterpret_cast<float*>(smem);
#pragma unroll
  for (int t = 0; t < 8; ++t) {
#pragma unroll
    for (int j = 0; j < 4; ++j) {
      const int l = 256 * t + 128 * ap + rho + 8 * (4 * g + j);
      sy[wid * L_ + l] = acc[t][j];
    }
  }
  __syncthreads();
  const float dsk = bf2f(a.Dskip[d]);
  {
    const int l0 = q * 8;
    float y[8];
#pragma unroll
    for (int hh = 0; hh < 2; ++hh) {
      f32x4 a0 = *reinterpret_cast<const f32x4*>(sy + l0 + 4 * hh);
      f32x4 a1 = *reinterpret_cast<const f32x4*>(sy + L_ + l0 + 4 * hh);
      f32x4 a2 = *reinterpret_cast<const f32x4*>(sy + 2 * L_ + l0 + 4 * hh);
      f32x4 a3 = *reinterpret_cast<const f32x4*>(sy + 3 * L_ + l0 + 4 * hh);
#pragma unroll
      for (int j = 0; j < 4; ++j) y[4 * hh + j] = (a0[j] + a1[j]) + (a2[j] + a3[j]);
    }
    u32 o[4];
#pragma unroll
    for (int k = 0; k < 4; ++k) {
      const float t0 = rbf(lo_f(w0[k]) * dsk), t1 = rbf(hi_f(w0[k]) * dsk);
      o[k] = pack2(rbf(y[2 * k]) + t0, rbf(y[2 * k + 1]) + t1);
    }
    *reinterpret_cast<u32x4*>(a.sT + (size_t)d * LT_ + l0) = (u32x4){o[0], o[1], o[2], o[3]};
  }
}

__global__ __launch_bounds__(256) void k_ln2(const u16* __restrict__ sT, const u16* __restrict__ x,
                                             u16* __restrict__ h, u16* __restrict__ xni) {
  __shared__ __attribute__((aligned(16))) u16 ss[16 * 648];
  const int tid = threadIdx.x, lane = tid & 63, wid = tid >> 6;
  const int row0 = blockIdx.x * 16;
  u32 xv[4][5];
#pragma unroll
  for (int rr = 0; rr < 4; ++rr) {
    const u32* xr = reinterpret_cast<const u32*>(x + (size_t)(row0 + wid * 4 + rr) * D_);
#pragma unroll
    for (int i = 0; i < 5; ++i) xv[rr][i] = xr[lane + 64 * i];
  }
  for (int d = tid; d < D_; d += 256) {
    const u32x4* src = reinterpret_cast<const u32x4*>(sT + (size_t)d * LT_ + row0);
    const u32x4 a0 = src[0], a1 = src[1];
#pragma unroll
    for (int k = 0; k < 4; ++k) {
      ss[(2 * k) * 648 + d] = (u16)(a0[k] & 0xffffu);
      ss[(2 * k + 1) * 648 + d] = (u16)(a0[k] >> 16);
      ss[(8 + 2 * k) * 648 + d] = (u16)(a1[k] & 0xffffu);
      ss[(8 + 2 * k + 1) * 648 + d] = (u16)(a1[k] >> 16);
    }
  }
  __syncthreads();
#pragma unroll
  for (int rr = 0; rr < 4; ++rr) {
    const int rl = wid * 4 + rr;
    const int row = row0 + rl;
    float f[10], o[10];
    u32* hrow = reinterpret_cast<u32*>(h + (size_t)row * D_);
#pragma unroll
    for (int i = 0; i < 5; ++i) {
      const u32 sv = *reinterpret_cast<const u32*>(ss + rl * 648 + 2 * (lane + 64 * i));
      const u32 hv = pack2(lo_f(xv[rr][i]) + lo_f(sv), hi_f(xv[rr][i]) + hi_f(sv));
      hrow[lane + 64 * i] = hv;
      f[2 * i] = lo_f(hv); f[2 * i + 1] = hi_f(hv);
    }
    ln_row(f, o);
    const int mt = row >> 7, r = row & 127;
#pragma unroll
    for (int i = 0; i < 5; ++i) {
      const int c = 2 * (lane + 64 * i);
      const int kt = c >> 6, cc = c & 63;
      const int off = ((mt * 10 + kt) * 128 + r) * 64 + (((cc >> 3) ^ (r & 7)) << 3) + (cc & 7);
      *reinterpret_cast<u32*>(xni + off) = pack2(o[2 * i], o[2 * i + 1]);
    }
  }
}

__device__ __forceinline__ float gelu_tanh(float x) {
  const float k0 = 0.7978845608028654f, k1 = 0.044715f;
  const float inner = k0 * (x + k1 * x * x * x);
  return x * __builtin_amdgcn_rcpf(1.f + __expf(-2.f * inner));
}

constexpr int G1_A_BYTES = 16384;
constexpr int G1_B_BYTES = 7 * 4096;
constexpr int G1_STAGE = G1_A_BYTES + G1_B_BYTES;
constexpr int G1_NSTG = 3;

template <int NG>
__device__ __forceinline__ void g1_compute(const unsigned char* sb, int arow, int brow, int cpos0, int cpos1,
                                           f32x4 (&acc)[2][8]) {
#pragma unroll
  for (int ks = 0; ks < 2; ++ks) {
    const int cpos = ks ? cpos1 : cpos0;
    bf16x8 xf[2], wf[2 * NG];
#pragma unroll
    for (int m = 0; m < 2; ++m)
      xf[m] = *reinterpret_cast<const bf16x8*>(sb + (arow + m * 16) * 128 + cpos);
#pragma unroll
    for (int n = 0; n < 2 * NG; ++n)
      wf[n] = *reinterpret_cast<const bf16x8*>(sb + G1_A_BYTES + (brow + n * 16) * 128 + cpos);
#pragma unroll
    for (int n = 0; n < 2 * NG; ++n)
#pragma unroll
      for (int m = 0; m < 2; ++m)
        acc[m][n] = __builtin_amdgcn_mfma_f32_16x16x32_bf16(wf[n], xf[m], acc[m][n], 0, 0, 0);
  }
}

__global__ __launch_bounds__(512) void k3_gemm1(const u16* __restrict__ xni, const u16* __restrict__ w1i,
                                                u16* __restrict__ hmf) {
  __shared__ __attribute__((aligned(16))) unsigned char smem[G1_NSTG * G1_STAGE];
  const int tid = threadIdx.x, lane = tid & 63;
  const int wid = __builtin_amdgcn_readfirstlane(tid >> 6);
  const int wm = wid & 3, wn = wid >> 2;
  const int fr = lane & 15, fq = lane >> 4;
  const int bid = blockIdx.x;
  const int mt = (bid & 7) * 2 + ((bid >> 3) & 1);
  const int nb = bid >> 4;
  const int g0 = 27 * (nb >> 2) + 7 * (nb & 3);
  const int ng = ((nb & 3) == 3) ? 6 : 7;
  const int gw0 = wn * 4;
  const int gwn = wn ? ng - 4 : 4;

  const unsigned char* Ag = reinterpret_cast<const unsigned char*>(xni) + (size_t)mt * 10 * G1_A_BYTES;
  const unsigned char* Bg = reinterpret_cast<const unsigned char*>(w1i);

#define G1_ISSUE(kt, buf)                                                                              \
  {                                                                                                    \
    unsigned char* sb_ = smem + (buf) * G1_STAGE;                                                      \
    _Pragma("unroll") for (int i = 0; i < 2; ++i) {                                                    \
      const int piece = wid + 8 * i;                                                                   \
      __builtin_amdgcn_global_load_lds(                                                                \
          (const __attribute__((address_space(1))) void*)(Ag + (size_t)(kt) * G1_A_BYTES + piece * 1024 + lane * 16), \
          (__attribute__((address_space(3))) void*)(sb_ + piece * 1024), 16, 0, 0);                    \
    }                                                                                                  \
    _Pragma("unroll") for (int i = 0; i < 4; ++i) {                                                    \
      int pb = wid + 8 * i; if (pb >= 28) pb -= 4;                                                     \
      const int gi = pb >> 2;                                                                          \
      const int grp = (g0 + gi < 108) ? g0 + gi : 107;                                                 \
      __builtin_amdgcn_global_load_lds(                                                                \
          (const __attribute__((address_space(1))) void*)(Bg + ((size_t)((kt) * 108 + grp) * 4096) + (pb & 3) * 1024 + lane * 16), \
          (__attribute__((address_space(3))) void*)(sb_ + G1_A_BYTES + pb * 1024), 16, 0, 0);          \
    }                                                                                                  \
  }

  f32x4 acc[2][8];
#pragma unroll
  for (int m = 0; m < 2; ++m)
#pragma unroll
    for (int n = 0; n < 8; ++n) acc[m][n] = (f32x4){0, 0, 0, 0};

  G1_ISSUE(0, 0);
  G1_ISSUE(1, 1);
  const int swz = fr & 7;
  const int cpos0 = ((0 + fq) ^ swz) << 4, cpos1 = ((4 + fq) ^ swz) << 4;
  const int arow = wm * 32 + fr, brow = gw0 * 32 + fr;
  int buf = 0;
#pragma unroll 1
  for (int kt = 0; kt < 10; ++kt) {
    if (kt < 9) asm volatile("s_waitcnt vmcnt(6)" ::: "memory");
    else asm volatile("s_waitcnt vmcnt(0)" ::: "memory");
    __builtin_amdgcn_s_barrier();
    asm volatile("" ::: "memory");
    if (kt + 2 < 10) {
      const int nbuf = (buf >= 1) ? buf - 1 : 2;
      G1_ISSUE(kt + 2, nbuf);
    }
    const unsigned char* sb = smem + buf * G1_STAGE;
    if (wn == 0) g1_compute<4>(sb, arow, brow, cpos0, cpos1, acc);
    else g1_compute<3>(sb, arow, brow, cpos0, cpos1, acc);
    buf = (buf == 2) ? 0 : buf + 1;
  }
#pragma unroll
  for (int m = 0; m < 2; ++m) {
    const int mt16 = mt * 8 + wm * 2 + m;
#pragma unroll
    for (int gI = 0; gI < 4; ++gI) {
      if (gI < gwn) {
        float o[4];
#pragma unroll
        for (int j = 0; j < 4; ++j) {
          const float gv = rbf(acc[m][2 * gI][j]);
          const float uv = rbf(acc[m][2 * gI + 1][j]);
          o[j] = rbf(gelu_tanh(gv)) * uv;
        }
        const int f = 16 * (g0 + gw0 + gI) + 4 * fq;
        const int ks = f >> 5, fq2 = (f & 31) >> 3, j0 = f & 7;
        u16* dst = hmf + ((size_t)(mt16 * 54 + ks) * 64 + fr + 16 * fq2) * 8 + j0;
        *reinterpret_cast<u32x2*>(dst) = (u32x2){pack2(o[0], o[1]), pack2(o[2], o[3])};
      }
    }
  }
}

__global__ __launch_bounds__(512) void k4_gemm2(const u16* __restrict__ hmf, const u16* __restrict__ wdf,
                                                const u16* __restrict__ h, u16* __restrict__ out) {
  __shared__ __attribute__((aligned(16))) float sred[4 * 64 * 84];
  const int tid = threadIdx.x, lane = tid & 63;
  const int wid = __builtin_amdgcn_readfirstlane(tid >> 6);
  const int wk = wid & 3, wm = wid >> 2;
  const int fr = lane & 15, fq = lane >> 4;
  const int bid = blockIdx.x;
  const int mt = (bid & 7) * 4 + ((bid >> 3) & 3);
  const int nb = bid >> 5;
  const int ks0 = (wk < 2) ? wk * 14 : 28 + (wk - 2) * 13;
  const int nks = (wk < 2) ? 14 : 13;
  const bf16x8* Ap = reinterpret_cast<const bf16x8*>(hmf) + (size_t)(mt * 4 + wm * 2) * 54 * 64 + lane;
  const bf16x8* Bp = reinterpret_cast<const bf16x8*>(wdf) + (size_t)(nb * 5) * 54 * 64 + lane;
  f32x4 acc[2][5];
#pragma unroll
  for (int m = 0; m < 2; ++m)
#pragma unroll
    for (int n = 0; n < 5; ++n) acc[m][n] = (f32x4){0, 0, 0, 0};
  bf16x8 xa[3][2], wb[3][5];
#define G2_LOAD(slot, kidx)                                                              \
  {                                                                                      \
    const int kq_ = ks0 + (((kidx) < nks) ? (kidx) : nks - 1);                           \
    _Pragma("unroll") for (int m = 0; m < 2; ++m) xa[slot][m] = Ap[(m * 54 + kq_) * 64];   \
    _Pragma("unroll") for (int n = 0; n < 5; ++n) wb[slot][n] = Bp[(n * 54 + kq_) * 64];   \
  }
#define G2_MMA(slot)                                                                     \
  {                                                                                      \
    _Pragma("unroll") for (int n = 0; n < 5; ++n)                                        \
    _Pragma("unroll") for (int m = 0; m < 2; ++m)                                        \
      acc[m][n] = __builtin_amdgcn_mfma_f32_16x16x32_bf16(wb[slot][n], xa[slot][m], acc[m][n], 0, 0, 0); \
  }
  G2_LOAD(0, 0);
  G2_LOAD(1, 1);
#pragma unroll
  for (int i = 0; i < 13; i += 3) {
    G2_LOAD(2, i + 2);
    G2_MMA(0);
    if (i + 1 < 13) { G2_LOAD(0, i + 3); G2_MMA(1); }
    if (i + 2 < 13) { G2_LOAD(1, i + 4); G2_MMA(2); }
  }
  if (nks == 14) G2_MMA(1);
  float* sw = sred + wk * 64 * 84;
#pragma unroll
  for (int m = 0; m < 2; ++m)
#pragma unroll
    for (int n = 0; n < 5; ++n)
      *reinterpret_cast<f32x4*>(sw + (wm * 32 + m * 16 + fr) * 84 + n * 16 + 4 * fq) = acc[m][n];
  __syncthreads();
  {
    const int rl = tid >> 3, c0 = (tid & 7) * 10;
    const int row = mt * 64 + rl;
    const u16* hr = h + (size_t)row * D_ + nb * 80 + c0;
    u16* orow = out + (size_t)row * D_ + nb * 80 + c0;
#pragma unroll
    for (int i = 0; i < 5; ++i) {
      const float* p = sred + rl * 84 + c0 + 2 * i;
      const float v0 = (p[0] + p[64 * 84]) + (p[2 * 64 * 84] + p[3 * 64 * 84]);
      const float v1 = (p[1] + p[64 * 84 + 1]) + (p[2 * 64 * 84 + 1] + p[3 * 64 * 84 + 1]);
      const u32 hv = *reinterpret_cast<const u32*>(hr + 2 * i);
      *reinterpret_cast<u32*>(orow + 2 * i) = pack2(lo_f(hv) + rbf(v0), hi_f(hv) + rbf(v1));
    }
  }
}

extern "C" void kernel_launch(void* const* d_in, const int* in_sizes, int n_in,
                              void* d_out, int out_size, void* d_ws, size_t ws_size,
                              hipStream_t stream) {
  const u16* x = (const u16*)d_in[0];
  const u16* logA = (const u16*)d_in[1];
  const u16* Aim = (const u16*)d_in[2];
  const u16* Cr = (const u16*)d_in[3];
  const u16* Ci = (const u16*)d_in[4];
  const u16* logdt = (const u16*)d_in[5];
  const u16* Dskip = (const u16*)d_in[6];
  const u16* wg = (const u16*)d_in[8];
  const u16* wu = (const u16*)d_in[9];
  const u16* wd = (const u16*)d_in[10];
  u16* out = (u16*)d_out;

  char* ws = (char*)d_ws;
  size_t off = 0;
  auto alloc = [&](size_t bytes) { char* p = ws + off; off += (bytes + 255) & ~(size_t)255; return p; };
  u16* uT = (u16*)alloc((size_t)D_ * LT_ * 2);
  u16* sT = (u16*)alloc((size_t)D_ * LT_ * 2);
  u16* h = (u16*)alloc((size_t)L_ * D_ * 2);
  u16* xni = (u16*)alloc((size_t)L_ * D_ * 2);
  u16* hmf = (u16*)alloc((size_t)L_ * F_ * 2);
  u16* w1i = (u16*)alloc((size_t)2 * F_ * D_ * 2);
  u16* wdf = (u16*)alloc((size_t)D_ * F_ * 2);

  K1Args a;
  a.x = x; a.wg = wg; a.wu = wu; a.wd = wd;
  a.uT = uT; a.w1i = w1i; a.wdf = wdf;
  k1_kernel<<<K1_BLOCKS, 256, 0, stream>>>(a);
  K2Args a2;
  a2.logA = logA; a2.Aim = Aim; a2.Cr = Cr; a2.Ci = Ci; a2.logdt = logdt; a2.Dskip = Dskip;
  a2.uT = uT; a2.sT = sT;
  k2_conv<<<D_, 256, 0, stream>>>(a2);
  k_ln2<<<L_ / 16, 256, 0, stream>>>(sT, x, h, xni);
  k3_gemm1<<<256, 512, 0, stream>>>(xni, w1i, hmf);
  k4_gemm2<<<256, 512, 0, stream>>>(hmf, wdf, h, out);
}
```

```cpp
#include <hip/hip_runtime.h>
#include <hip/hip_bf16.h>
#include <stdint.h>

constexpr int L_ = 2048;
constexpr int D_ = 640;
constexpr int N_ = 128;
constexpr int F_ = 1728;
constexpr int LT_ = 2112;

using bf16x8 = __attribute__((ext_vector_type(8))) short;
using f32x4 = __attribute__((ext_vector_type(4))) float;
using u32x4 = __attribute__((ext_vector_type(4))) unsigned int;
using u32x2 = __attribute__((ext_vector_type(2))) unsigned int;
typedef unsigned short u16;
typedef unsigned int u32;

__device__ __forceinline__ float bf2f(u16 v) { return __uint_as_float(((u32)v) << 16); }
__device__ __forceinline__ u16 f2bf(float f) {
  __hip_bfloat16 b = __float2bfloat16(f);
  return *reinterpret_cast<u16*>(&b);
}
__device__ __forceinline__ float rbf(float f) { return bf2f(f2bf(f)); }
__device__ __forceinline__ float lo_f(u32 v) { return __uint_as_float(v << 16); }
__device__ __forceinline__ float hi_f(u32 v) { return __uint_as_float(v & 0xffff0000u); }
__device__ __forceinline__ u32 pack2(float a, float b) { return (u32)f2bf(a) | ((u32)f2bf(b) << 16); }

template <int CTRL, int RMASK>
__device__ __forceinline__ float dpp_add(float v) {
  const int t = __builtin_amdgcn_update_dpp(0, __float_as_int(v), CTRL, RMASK, 0xf, false);
  return v + __int_as_float(t);
}
__device__ __forceinline__ float wave_sum(float v) {
  v = dpp_add<0x111, 0xf>(v);
  v = dpp_add<0x112, 0xf>(v);
  v = dpp_add<0x114, 0xf>(v);
  v = dpp_add<0x118, 0xf>(v);
  v = dpp_add<0x142, 0xa>(v);
  v = dpp_add<0x143, 0xc>(v);
  return __int_as_float(__builtin_amdgcn_readlane(__float_as_int(v), 63));
}

__device__ __forceinline__ void cexpp(float re, float im, float p, float& outr, float& outi) {
  const float mag = __expf(re * p);
  const float ang = im * p;
  const float k = rintf(ang * 0.15915494309189535f);
  float r = fmaf(-k, 6.2831854820251465f, ang);
  r = fmaf(-k, -1.7484555314695172e-07f, r);
  const float rr = r * 0.15915494309189535f;
  const float s = __builtin_amdgcn_sinf(rr);
  const float c = __builtin_amdgcn_cosf(rr);
  outr = mag * c;
  outi = mag * s;
}

__device__ __forceinline__ void ln_row(const float (&f)[10], float (&o)[10]) {
  float s = 0.f;
#pragma unroll
  for (int i = 0; i < 10; ++i) s += f[i];
  const float mean = wave_sum(s) * (1.f / D_);
  float q = 0.f;
#pragma unroll
  for (int i = 0; i < 10; ++i) { const float t = f[i] - mean; q += t * t; }
  const float var = wave_sum(q) * (1.f / D_);
  const float mb = rbf(mean);
  const float sd = rbf(sqrtf(rbf(rbf(var) + 1e-5f)));
  const float rs = 1.f / sd;
#pragma unroll
  for (int i = 0; i < 10; ++i) {
    const float t = rbf(f[i] - mb);
    const float q0 = t * rs;
    o[i] = fmaf(fmaf(-q0, sd, t), rs, q0);
  }
}

constexpr int K1_LN_BLOCKS = L_ / 16;
constexpr int K1_KG_BLOCKS = D_;
constexpr int K1_W1_BLOCKS = (D_ / 64) * (F_ / 64);
constexpr int K1_WD_BLOCKS = (F_ / 64) * (D_ / 64);
constexpr int K1_BLOCKS = K1_LN_BLOCKS + K1_KG_BLOCKS + K1_W1_BLOCKS + K1_WD_BLOCKS;
constexpr int KC_STRIDE = 648;

struct K1Args {
  const u16* x; const u16* wg; const u16* wu; const u16* wd;
  const u16* logA; const u16* Aim; const u16* Cr; const u16* Ci; const u16* logdt;
  u16* uT; u16* w1i; u16* wdf; u32* Kc;
};

__global__ __launch_bounds__(256) void k1_kernel(K1Args a) {
  __shared__ __attribute__((aligned(16))) unsigned char smem_raw[16 * 648 * 2];
  const int tid = threadIdx.x;
  const int lane = tid & 63;
  const int wid = tid >> 6;
  int b = blockIdx.x;
  if (b < K1_LN_BLOCKS) {
    u16* su = reinterpret_cast<u16*>(smem_raw);
    const int row0 = b * 16;
    u32 v[4][5];
#pragma unroll
    for (int rr = 0; rr < 4; ++rr) {
      const u32* xr = reinterpret_cast<const u32*>(a.x + (size_t)(row0 + wid * 4 + rr) * D_);
#pragma unroll
      for (int i = 0; i < 5; ++i) v[rr][i] = xr[lane + 64 * i];
    }
#pragma unroll
    for (int rr = 0; rr < 4; ++rr) {
      float f[10], o[10];
#pragma unroll
      for (int i = 0; i < 5; ++i) { f[2 * i] = lo_f(v[rr][i]); f[2 * i + 1] = hi_f(v[rr][i]); }
      ln_row(f, o);
#pragma unroll
      for (int i = 0; i < 5; ++i)
        *reinterpret_cast<u32*>(su + (wid * 4 + rr) * 648 + 2 * (lane + 64 * i)) = pack2(o[2 * i], o[2 * i + 1]);
    }
    __syncthreads();
    for (int d = tid; d < D_; d += 256) {
      u32 pu[8];
#pragma unroll
      for (int r = 0; r < 8; ++r)
        pu[r] = (u32)su[(2 * r) * 648 + d] | ((u32)su[(2 * r + 1) * 648 + d] << 16);
      u32x4* du = reinterpret_cast<u32x4*>(a.uT + (size_t)d * LT_ + row0);
      du[0] = (u32x4){pu[0], pu[1], pu[2], pu[3]};
      du[1] = (u32x4){pu[4], pu[5], pu[6], pu[7]};
    }
    return;
  }
  b -= K1_LN_BLOCKS;
  if (b < K1_KG_BLOCKS) {
    const int d = b;
    float* sp = reinterpret_cast<float*>(smem_raw);
    float* sr = sp + 512;
    const float dt = rbf(__expf(bf2f(a.logdt[d])));
    if (tid < N_) {
      const int n = tid;
      const float la = bf2f(a.logA[d * N_ + n]);
      f32x4 pv;
      pv[0] = -rbf(expf(la)) * dt;
      pv[1] = bf2f(a.Aim[d * N_ + n]) * dt;
      pv[2] = bf2f(a.Cr[d * N_ + n]) * dt;
      pv[3] = bf2f(a.Ci[d * N_ + n]) * dt;
      *reinterpret_cast<f32x4*>(sp + 4 * n) = pv;
    }
    __syncthreads();
    const int rc = lane & 15;
    const int kk = lane >> 4;
    f32x4 acc0 = {0, 0, 0, 0}, acc1 = {0, 0, 0, 0}, acc2 = {0, 0, 0, 0}, acc3 = {0, 0, 0, 0};
    const float pA = (float)(16 * rc);
    const float pB = (float)(rc + 1);
#pragma unroll 2
    for (int i = 0; i < 8; ++i) {
      const int n = wid * 32 + 4 * i + kk;
      const f32x4 pv = *reinterpret_cast<const f32x4*>(sp + 4 * n);
      const float cr = pv[2], ci = pv[3];
      float Ar, Ai, Br, Bi;
      cexpp(pv[0], pv[1], pA, Ar, Ai);
      cexpp(pv[0], pv[1], pB, Br, Bi);
#define KG_STEP(ACC)                                                              \
      {                                                                             \
        const float tr = Ar * cr - Ai * ci;                                         \
        const float ti = -(Ar * ci + Ai * cr);                                      \
        ACC = __builtin_amdgcn_mfma_f32_16x16x4f32(tr, Br, ACC, 0, 0, 0);           \
        ACC = __builtin_amdgcn_mfma_f32_16x16x4f32(ti, Bi, ACC, 0, 0, 0);           \
      }
#define KG_SQ()                                                                   \
      {                                                                             \
        const float t0 = Ar * Ar - Ai * Ai; Ai = 2.f * Ar * Ai; Ar = t0;            \
        const float t1 = Br * Br - Bi * Bi; Bi = 2.f * Br * Bi; Br = t1;            \
      }
      KG_STEP(acc0); KG_SQ();
      KG_STEP(acc1); KG_SQ();
      KG_STEP(acc2); KG_SQ();
      KG_STEP(acc3);
    }
    {
      float* w = sr + wid * 1024;
#pragma unroll
      for (int j = 0; j < 4; ++j) {
        const int e = (4 * kk + j) * 16 + rc;
        w[e] = acc0[j]; w[256 + e] = acc1[j]; w[512 + e] = acc2[j]; w[768 + e] = acc3[j];
      }
    }
    __syncthreads();
    u32* Kd = a.Kc + (size_t)d * KC_STRIDE;
    {
      const float v0 = 2.f * ((sr[tid] + sr[1024 + tid]) + (sr[2048 + tid] + sr[3072 + tid]));
      const u16 h0 = f2bf(v0);
      Kd[tid + 1] = (u32)h0 | ((u32)f2bf(v0 - bf2f(h0)) << 16);
      if (tid >= 128) {
#pragma unroll
        for (int gI = 1; gI < 4; ++gI) {
          const int e = gI * 256 + tid;
          const float vv = 2.f * ((sr[e] + sr[1024 + e]) + (sr[2048 + e] + sr[3072 + e]));
          const u16 hh = f2bf(vv);
          Kd[gI * 128 + tid + 1] = (u32)hh | ((u32)f2bf(vv - bf2f(hh)) << 16);
        }
      }
      if (wid == 0) {
        const float c0 = sp[4 * lane + 2] + sp[4 * (lane + 64) + 2];
        const float s0 = 2.f * wave_sum(c0);
        const u16 hh = f2bf(s0);
        if (lane == 0) Kd[0] = (u32)hh | ((u32)f2bf(s0 - bf2f(hh)) << 16);
      }
    }
    return;
  }
  b -= K1_KG_BLOCKS;
  u16* st = reinterpret_cast<u16*>(smem_raw);
  if (b < K1_W1_BLOCKS) {
    const int kt = b / 27, cc0 = (b % 27) * 64;
#pragma unroll
    for (int i = 0; i < 4; ++i) {
      const int idx = tid + 256 * i;
      const int mat = idx >> 9, r = (idx >> 3) & 63, c = (idx & 7) * 8;
      const u16* src = mat ? a.wu : a.wg;
      const u32x4 v = *reinterpret_cast<const u32x4*>(src + (size_t)(kt * 64 + r) * F_ + cc0 + c);
      *reinterpret_cast<u32x4*>(st + (mat * 64 + r) * 72 + c) = v;
    }
    __syncthreads();
#pragma unroll
    for (int i = 0; i < 4; ++i) {
      const int idx = tid + 256 * i;
      const int gl = idx >> 8, rr = (idx >> 3) & 31, pc = idx & 7;
      const int ch = pc ^ (rr & 7);
      const int mat = rr >> 4, col = gl * 16 + (rr & 15);
      u32 p[4];
#pragma unroll
      for (int j = 0; j < 4; ++j)
        p[j] = (u32)st[(mat * 64 + ch * 8 + 2 * j) * 72 + col] | ((u32)st[(mat * 64 + ch * 8 + 2 * j + 1) * 72 + col] << 16);
      const int grp = (cc0 >> 4) + gl;
      u16* dst = a.w1i + ((size_t)(kt * 108 + grp) * 2048) + rr * 64 + pc * 8;
      *reinterpret_cast<u32x4*>(dst) = (u32x4){p[0], p[1], p[2], p[3]};
    }
    return;
  }
  b -= K1_W1_BLOCKS;
  {
    const int k0 = (b / 10) * 64, n0 = (b % 10) * 64;
#pragma unroll
    for (int i = 0; i < 2; ++i) {
      const int idx = tid + 256 * i;
      const int r = idx >> 3, c = (idx & 7) * 8;
      const u32x4 v = *reinterpret_cast<const u32x4*>(a.wd + (size_t)(k0 + r) * D_ + n0 + c);
      *reinterpret_cast<u32x4*>(st + r * 72 + c) = v;
    }
    __syncthreads();
#pragma unroll
    for (int i = 0; i < 2; ++i) {
      const int idx = tid + 256 * i;
      const int ntl = idx >> 7, ksl = (idx >> 6) & 1, ln = idx & 63;
      const int fr = ln & 15, fq = ln >> 4;
      const int kb = ksl * 32 + fq * 8, col = ntl * 16 + fr;
      u32 p[4];
#pragma unroll
      for (int j = 0; j < 4; ++j)
        p[j] = (u32)st[(kb + 2 * j) * 72 + col] | ((u32)st[(kb + 2 * j + 1) * 72 + col] << 16);
      const int nt = (n0 >> 4) + ntl, ks = (k0 >> 5) + ksl;
      u16* dst = a.wdf + ((size_t)(nt * 54 + ks) * 64 + ln) * 8;
      *reinterpret_cast<u32x4*>(dst) = (u32x4){p[0], p[1], p[2], p[3]};
    }
  }
}

constexpr int CV_KLEN = 2208;
constexpr int CV_USTRIDE = 2320;
constexpr int CV_OFF_U = 2 * CV_KLEN * 2;
constexpr int CV_LDS_BYTES = CV_OFF_U + 8 * CV_USTRIDE * 2;

template <int TMIN>
__device__ __forceinline__ void conv_steps(int s0, int s1, const u16* kh, const u16* kl, const u16* ub,
                                           f32x4 (&acc)[8]) {
  for (int s = s0; s < s1; ++s) {
    const bf16x8 ah = *reinterpret_cast<const bf16x8*>(kh + 32 * s);
    const bf16x8 al = *reinterpret_cast<const bf16x8*>(kl + 32 * s);
    bf16x8 bv[8];
#pragma unroll
    for (int t = TMIN; t < 8; ++t) bv[t] = *reinterpret_cast<const bf16x8*>(ub + 32 * s - 256 * t);
#pragma unroll
    for (int t = TMIN; t < 8; ++t) {
      acc[t] = __builtin_amdgcn_mfma_f32_16x16x32_bf16(ah, bv[t], acc[t], 0, 0, 0);
      acc[t] = __builtin_amdgcn_mfma_f32_16x16x32_bf16(al, bv[t], acc[t], 0, 0, 0);
    }
  }
}

struct K2Args {
  const u32* Kc; const u16* Dskip; const u16* uT; u16* sT;
};

__device__ __forceinline__ void build_u_chunk(const u32 (&W)[8], u16* dst) {
#pragma unroll
  for (int rho = 0; rho < 8; ++rho) {
    u32 o[4];
#pragma unroll
    for (int k = 0; k < 4; ++k) {
      if ((rho & 1) == 0) {
        const int aI = (rho + 8 - 2 * k) >> 1;
        o[k] = (W[aI] & 0xffffu) | (W[aI - 1] & 0xffff0000u);
      } else {
        const int aI = (rho + 7 - 2 * k) >> 1;
        o[k] = (W[aI] >> 16) | (W[aI] << 16);
      }
    }
    *reinterpret_cast<u32x4*>(dst + rho * CV_USTRIDE) = (u32x4){o[0], o[1], o[2], o[3]};
  }
}

__global__ __launch_bounds__(256, 3) void k2_conv(K2Args a) {
  __shared__ __attribute__((aligned(16))) unsigned char smem[CV_LDS_BYTES];
  u16* sKh = reinterpret_cast<u16*>(smem);
  u16* sKl = sKh + CV_KLEN;
  u16* sU = reinterpret_cast<u16*>(smem + CV_OFF_U);
  const int tid = threadIdx.x, lane = tid & 63, wid = tid >> 6;
  const int d = blockIdx.x;
  const u16* ud = a.uT + (size_t)d * LT_;
  const u32* Kd = a.Kc + (size_t)d * KC_STRIDE;
  const int q = 255 - tid;
  const u32x4 w0 = *reinterpret_cast<const u32x4*>(ud + 8 * q);
  u32x4 w1 = {0, 0, 0, 0};
  if (q < 255) w1 = *reinterpret_cast<const u32x4*>(ud + 8 * q + 8);
  u32 kv[8];
  {
    const int j0 = 8 * tid;
    const int e = (j0 < 256) ? 0 : (j0 < 512) ? 1 : (j0 < 1024) ? 2 : 3;
    const int base = 128 * e;
    const int hm1 = ((1 << e) >> 1) - 1;
#pragma unroll
    for (int jj = 0; jj < 8; ++jj) {
      const int j = j0 + jj;
      const int pq = (e == 0) ? j : ((j + hm1 + ((j >> e) & 1)) >> e);
      kv[jj] = Kd[base + pq];
    }
  }
  {
    const u32 W[8] = {w0[0], w0[1], w0[2], w0[3], w1[0], w1[1], w1[2], w1[3]};
    build_u_chunk(W, sU + 8 * tid);
    if (tid == 255) {
      const u32 W2[8] = {0, 0, 0, 0, w0[0], w0[1], w0[2], w0[3]};
      build_u_chunk(W2, sU + 8 * 256);
    }
    if (tid < 8 * 19) {
      const int rho = tid / 19, m = 257 + tid % 19;
      *reinterpret_cast<u32x4*>(sU + rho * CV_USTRIDE + 8 * m) = (u32x4){0, 0, 0, 0};
    } else if (tid >= 160 && tid < 200) {
      const int t = tid - 160;
      const int m = (t % 20 < 16) ? t % 20 : 256 + t % 20;
      u16* dst = (t < 20) ? sKh : sKl;
      *reinterpret_cast<u32x4*>(dst + 8 * m) = (u32x4){0, 0, 0, 0};
    }
    u32x4 ph, pl;
#pragma unroll
    for (int k = 0; k < 4; ++k) {
      ph[k] = (kv[2 * k] & 0xffffu) | (kv[2 * k + 1] << 16);
      pl[k] = (kv[2 * k] >> 16) | (kv[2 * k + 1] & 0xffff0000u);
    }
    *reinterpret_cast<u32x4*>(sKh + 128 + 8 * tid) = ph;
    *reinterpret_cast<u32x4*>(sKl + 128 + 8 * tid) = pl;
  }
  __syncthreads();

  const int r = lane & 15, g = lane >> 4;
  const int ap = (lane & 15) >> 3, rho = lane & 7;
  f32x4 acc[8];
#pragma unroll
  for (int t = 0; t < 8; ++t) acc[t] = (f32x4){0, 0, 0, 0};
  const u16* kh = sKh + 128 + 8 * r + 8 * g;
  const u16* kl = sKl + 128 + 8 * r + 8 * g;
  const u16* ub = sU + rho * CV_USTRIDE + 8 * g - 128 * ap + 2048;
  if (wid == 0) {
    conv_steps<0>(-4, 5, kh, kl, ub, acc);
  } else if (wid == 1) {
    conv_steps<1>(5, 13, kh, kl, ub, acc);
    conv_steps<2>(13, 16, kh, kl, ub, acc);
  } else if (wid == 2) {
    conv_steps<2>(16, 21, kh, kl, ub, acc);
    conv_steps<3>(21, 29, kh, kl, ub, acc);
    conv_steps<4>(29, 30, kh, kl, ub, acc);
  } else {
    conv_steps<4>(30, 37, kh, kl, ub, acc);
    conv_steps<5>(37, 45, kh, kl, ub, acc);
    conv_steps<6>(45, 53, kh, kl, ub, acc);
    conv_steps<7>(53, 61, kh, kl, ub, acc);
  }
  __syncthreads();
  float* sy = reinterpret_cast<float*>(smem);
#pragma unroll
  for (int t = 0; t < 8; ++t) {
#pragma unroll
    for (int j = 0; j < 4; ++j) {
      const int l = 256 * t + 128 * ap + rho + 8 * (4 * g + j);
      sy[wid * L_ + l] = acc[t][j];
    }
  }
  __syncthreads();
  const float dsk = bf2f(a.Dskip[d]);
  {
    const int l0 = q * 8;
    float y[8];
#pragma unroll
    for (int hh = 0; hh < 2; ++hh) {
      f32x4 a0 = *reinterpret_cast<const f32x4*>(sy + l0 + 4 * hh);
      f32x4 a1 = *reinterpret_cast<const f32x4*>(sy + L_ + l0 + 4 * hh);
      f32x4 a2 = *reinterpret_cast<const f32x4*>(sy + 2 * L_ + l0 + 4 * hh);
      f32x4 a3 = *reinterpret_cast<const f32x4*>(sy + 3 * L_ + l0 + 4 * hh);
#pragma unroll
      for (int j = 0; j < 4; ++j) y[4 * hh + j] = (a0[j] + a1[j]) + (a2[j] + a3[j]);
    }
    u32 o[4];
#pragma unroll
    for (int k = 0; k < 4; ++k) {
      const float t0 = rbf(lo_f(w0[k]) * dsk), t1 = rbf(hi_f(w0[k]) * dsk);
      o[k] = pack2(rbf(y[2 * k]) + t0, rbf(y[2 * k + 1]) + t1);
    }
    *reinterpret_cast<u32x4*>(a.sT + (size_t)d * LT_ + l0) = (u32x4){o[0], o[1], o[2], o[3]};
  }
}

__global__ __launch_bounds__(256) void k_ln2(const u16* __restrict__ sT, const u16* __restrict__ x,
                                             u16* __restrict__ h, u16* __restrict__ xni) {
  __shared__ __attribute__((aligned(16))) u16 ss[16 * 648];
  const int tid = threadIdx.x, lane = tid & 63, wid = tid >> 6;
  const int row0 = blockIdx.x * 16;
  u32 xv[4][5];
#pragma unroll
  for (int rr = 0; rr < 4; ++rr) {
    const u32* xr = reinterpret_cast<const u32*>(x + (size_t)(row0 + wid * 4 + rr) * D_);
#pragma unroll
    for (int i = 0; i < 5; ++i) xv[rr][i] = xr[lane + 64 * i];
  }
  u32x4 sa[3][2];
#pragma unroll
  for (int it = 0; it < 3; ++it) {
    const int d = tid + 256 * it;
    if (d < D_) {
      const u32x4* src = reinterpret_cast<const u32x4*>(sT + (size_t)d * LT_ + row0);
      sa[it][0] = src[0]; sa[it][1] = src[1];
    }
  }
#pragma unroll
  for (int it = 0; it < 3; ++it) {
    const int d = tid + 256 * it;
    if (d < D_) {
#pragma unroll
      for (int k = 0; k < 4; ++k) {
        ss[(2 * k) * 648 + d] = (u16)(sa[it][0][k] & 0xffffu);
        ss[(2 * k + 1) * 648 + d] = (u16)(sa[it][0][k] >> 16);
        ss[(8 + 2 * k) * 648 + d] = (u16)(sa[it][1][k] & 0xffffu);
        ss[(8 + 2 * k + 1) * 648 + d] = (u16)(sa[it][1][k] >> 16);
      }
    }
  }
  __syncthreads();
#pragma unroll
  for (int rr = 0; rr < 4; ++rr) {
    const int rl = wid * 4 + rr;
    const int row = row0 + rl;
    float f[10], o[10];
    u32* hrow = reinterpret_cast<u32*>(h + (size_t)row * D_);
#pragma unroll
    for (int i = 0; i < 5; ++i) {
      const u32 sv = *reinterpret_cast<const u32*>(ss + rl * 648 + 2 * (lane + 64 * i));
      const u32 hv = pack2(lo_f(xv[rr][i]) + lo_f(sv), hi_f(xv[rr][i]) + hi_f(sv));
      hrow[lane + 64 * i] = hv;
      f[2 * i] = lo_f(hv); f[2 * i + 1] = hi_f(hv);
    }
    ln_row(f, o);
    const int mt = row >> 7, r = row & 127;
#pragma unroll
    for (int i = 0; i < 5; ++i) {
      const int c = 2 * (lane + 64 * i);
      const int kt = c >> 6, cc = c & 63;
      const int off = ((mt * 10 + kt) * 128 + r) * 64 + (((cc >> 3) ^ (r & 7)) << 3) + (cc & 7);
      *reinterpret_cast<u32*>(xni + off) = pack2(o[2 * i], o[2 * i + 1]);
    }
  }
}

__device__ __forceinline__ float gelu_tanh(float x) {
  const float k0 = 0.7978845608028654f, k1 = 0.044715f;
  const float inner = k0 * (x + k1 * x * x * x);
  return x * __builtin_amdgcn_rcpf(1.f + __expf(-2.f * inner));
}

constexpr int G1_A_BYTES = 16384;
constexpr int G1_B_BYTES = 7 * 4096;
constexpr int G1_STAGE = G1_A_BYTES + G1_B_BYTES;
constexpr int G1_NSTG = 3;

template <int NG>
__device__ __forceinline__ void g1_compute(const unsigned char* sb, int arow, int brow, int cpos0, int cpos1,
                                           f32x4 (&acc)[2][8]) {
#pragma unroll
  for (int ks = 0; ks < 2; ++ks) {
    const int cpos = ks ? cpos1 : cpos0;
    bf16x8 xf[2], wf[2 * NG];
#pragma unroll
    for (int m = 0; m < 2; ++m)
      xf[m] = *reinterpret_cast<const bf16x8*>(sb + (arow + m * 16) * 128 + cpos);
#pragma unroll
    for (int n = 0; n < 2 * NG; ++n)
      wf[n] = *reinterpret_cast<const bf16x8*>(sb + G1_A_BYTES + (brow + n * 16) * 128 + cpos);
#pragma unroll
    for (int n = 0; n < 2 * NG; ++n)
#pragma unroll
      for (int m = 0; m < 2; ++m)
        acc[m][n] = __builtin_amdgcn_mfma_f32_16x16x32_bf16(wf[n], xf[m], acc[m][n], 0, 0, 0);
  }
}

__global__ __launch_bounds__(512) void k3_gemm1(const u16* __restrict__ xni, const u16* __restrict__ w1i,
                                                u16* __restrict__ hmf) {
  __shared__ __attribute__((aligned(16))) unsigned char smem[G1_NSTG * G1_STAGE];
  const int tid = threadIdx.x, lane = tid & 63;
  const int wid = __builtin_amdgcn_readfirstlane(tid >> 6);
  const int wm = wid & 3, wn = wid >> 2;
  const int fr = lane & 15, fq = lane >> 4;
  const int bid = blockIdx.x;
  const int xcd = bid & 7, idx = bid >> 3;
  const int mt = (xcd & 1) * 8 + (idx & 7);
  const int nb = (xcd >> 1) * 4 + (idx >> 3);
  const int g0 = 27 * (nb >> 2) + 7 * (nb & 3);
  const int ng = ((nb & 3) == 3) ? 6 : 7;
  const int gw0 = wn * 4;
  const int gwn = wn ? ng - 4 : 4;

  const unsigned char* Ag = reinterpret_cast<const unsigned char*>(xni) + (size_t)mt * 10 * G1_A_BYTES;
  const unsigned char* Bg = reinterpret_cast<const unsigned char*>(w1i);

#define G1_ISSUE(kt, buf)                                                                              \
  {                                                                                                    \
    unsigned char* sb_ = smem + (buf) * G1_STAGE;                                                      \
    _Pragma("unroll") for (int i = 0; i < 2; ++i) {                                                    \
      const int piece = wid + 8 * i;                                                                   \
      __builtin_amdgcn_global_load_lds(                                                                \
          (const __attribute__((address_space(1))) void*)(Ag + (size_t)(kt) * G1_A_BYTES + piece * 1024 + lane * 16), \
          (__attribute__((address_space(3))) void*)(sb_ + piece * 1024), 16, 0, 0);                    \
    }                                                                                                  \
    _Pragma("unroll") for (int i = 0; i < 4; ++i) {                                                    \
      int pb = wid + 8 * i; if (pb >= 28) pb -= 4;                                                     \
      const int gi = pb >> 2;                                                                          \
      const int grp = (g0 + gi < 108) ? g0 + gi : 107;                                                 \
      __builtin_amdgcn_global_load_lds(                                                                \
          (const __attribute__((address_space(1))) void*)(Bg + ((size_t)((kt) * 108 + grp) * 4096) + (pb & 3) * 1024 + lane * 16), \
          (__attribute__((address_space(3))) void*)(sb_ + G1_A_BYTES + pb * 1024), 16, 0, 0);          \
    }                                                                                                  \
  }

  f32x4 acc[2][8];
#pragma unroll
  for (int m = 0; m < 2; ++m)
#pragma unroll
    for (int n = 0; n < 8; ++n) acc[m][n] = (f32x4){0, 0, 0, 0};

  G1_ISSUE(0, 0);
  G1_ISSUE(1, 1);
  const int swz = fr & 7;
  const int cpos0 = ((0 + fq) ^ swz) << 4, cpos1 = ((4 + fq) ^ swz) << 4;
  const int arow = wm * 32 + fr, brow = gw0 * 32 + fr;
  int buf = 0;
#pragma unroll 1
  for (int kt = 0; kt < 10; ++kt) {
    if (kt < 9) asm volatile("s_waitcnt vmcnt(6)" ::: "memory");
    else asm volatile("s_waitcnt vmcnt(0)" ::: "memory");
    __builtin_amdgcn_s_barrier();
    asm volatile("" ::: "memory");
    if (kt + 2 < 10) {
      const int nbuf = (buf >= 1) ? buf - 1 : 2;
      G1_ISSUE(kt + 2, nbuf);
    }
    const unsigned char* sb = smem + buf * G1_STAGE;
    if (wn == 0) g1_compute<4>(sb, arow, brow, cpos0, cpos1, acc);
    else g1_compute<3>(sb, arow, brow, cpos0, cpos1, acc);
    buf = (buf == 2) ? 0 : buf + 1;
  }
#pragma unroll
  for (int m = 0; m < 2; ++m) {
    const int mt16 = mt * 8 + wm * 2 + m;
#pragma unroll
    for (int gI = 0; gI < 4; ++gI) {
      if (gI < gwn) {
        float o[4];
#pragma unroll
        for (int j = 0; j < 4; ++j) {
          const float gv = rbf(acc[m][2 * gI][j]);
          const float uv = rbf(acc[m][2 * gI + 1][j]);
          o[j] = rbf(gelu_tanh(gv)) * uv;
        }
        const int f = 16 * (g0 + gw0 + gI) + 4 * fq;
        const int ks = f >> 5, fq2 = (f & 31) >> 3, j0 = f & 7;
        u16* dst = hmf + ((size_t)(mt16 * 54 + ks) * 64 + fr + 16 * fq2) * 8 + j0;
        *reinterpret_cast<u32x2*>(dst) = (u32x2){pack2(o[0], o[1]), pack2(o[2], o[3])};
      }
    }
  }
}

__global__ __launch_bounds__(512) void k4_gemm2(const u16* __restrict__ hmf, const u16* __restrict__ wdf,
                                                const u16* __restrict__ h, u16* __restrict__ out) {
  __shared__ __attribute__((aligned(16))) float sred[4 * 64 * 84];
  const int tid = threadIdx.x, lane = tid & 63;
  const int wid = __builtin_amdgcn_readfirstlane(tid >> 6);
  const int wk = wid & 3, wm = wid >> 2;
  const int fr = lane & 15, fq = lane >> 4;
  const int bid = blockIdx.x;
  const int xcd = bid & 7, idx = bid >> 3;
  const int mt = (xcd >> 1) * 8 + (idx & 7);
  const int nb = (xcd & 1) * 4 + (idx >> 3);
  const int ks0 = (wk < 2) ? wk * 14 : 28 + (wk - 2) * 13;
  const int nks = (wk < 2) ? 14 : 13;
  const bf16x8* Ap = reinterpret_cast<const bf16x8*>(hmf) + (size_t)(mt * 4 + wm * 2) * 54 * 64 + lane;
  const bf16x8* Bp = reinterpret_cast<const bf16x8*>(wdf) + (size_t)(nb * 5) * 54 * 64 + lane;
  f32x4 acc[2][5];
#pragma unroll
  for (int m = 0; m < 2; ++m)
#pragma unroll
    for (int n = 0; n < 5; ++n) acc[m][n] = (f32x4){0, 0, 0, 0};
  bf16x8 xa[3][2], wb[3][5];
#define G2_LOAD(slot, kidx)                                                              \
  {                                                                                      \
    const int kq_ = ks0 + (((kidx) < nks) ? (kidx) : nks - 1);                           \
    _Pragma("unroll") for (int m = 0; m < 2; ++m) xa[slot][m] = Ap[(m * 54 + kq_) * 64];   \
    _Pragma("unroll") for (int n = 0; n < 5; ++n) wb[slot][n] = Bp[(n * 54 + kq_) * 64];   \
  }
#define G2_MMA(slot)                                                                     \
  {                                                                                      \
    _Pragma("unroll") for (int n = 0; n < 5; ++n)                                        \
    _Pragma("unroll") for (int m = 0; m < 2; ++m)                                        \
      acc[m][n] = __builtin_amdgcn_mfma_f32_16x16x32_bf16(wb[slot][n], xa[slot][m], acc[m][n], 0, 0, 0); \
  }
  G2_LOAD(0, 0);
  G2_LOAD(1, 1);
#pragma unroll
  for (int i = 0; i < 13; i += 3) {
    G2_LOAD(2, i + 2);
    G2_MMA(0);
    if (i + 1 < 13) { G2_LOAD(0, i + 3); G2_MMA(1); }
    if (i + 2 < 13) { G2_LOAD(1, i + 4); G2_MMA(2); }
  }
  if (nks == 14) G2_MMA(1);
  float* sw = sred + wk * 64 * 84;
#pragma unroll
  for (int m = 0; m < 2; ++m)
#pragma unroll
    for (int n = 0; n < 5; ++n)
      *reinterpret_cast<f32x4*>(sw + (wm * 32 + m * 16 + fr) * 84 + n * 16 + 4 * fq) = acc[m][n];
  __syncthreads();
  {
    const int rl = tid >> 3, c0 = (tid & 7) * 10;
    const int row = mt * 64 + rl;
    const u16* hr = h + (size_t)row * D_ + nb * 80 + c0;
    u16* orow = out + (size_t)row * D_ + nb * 80 + c0;
#pragma unroll
    for (int i = 0; i < 5; ++i) {
      const float* p = sred + rl * 84 + c0 + 2 * i;
      const float v0 = (p[0] + p[64 * 84]) + (p[2 * 64 * 84] + p[3 * 64 * 84]);
      const float v1 = (p[1] + p[64 * 84 + 1]) + (p[2 * 64 * 84 + 1] + p[3 * 64 * 84 + 1]);
      const u32 hv = *reinterpret_cast<const u32*>(hr + 2 * i);
      *reinterpret_cast<u32*>(orow + 2 * i) = pack2(lo_f(hv) + rbf(v0), hi_f(hv) + rbf(v1));
    }
  }
}

extern "C" void kernel_launch(void* const* d_in, const int* in_sizes, int n_in,
                              void* d_out, int out_size, void* d_ws, size_t ws_size,
                              hipStream_t stream) {
  const u16* x = (const u16*)d_in[0];
  const u16* logA = (const u16*)d_in[1];
  const u16* Aim = (const u16*)d_in[2];
  const u16* Cr = (const u16*)d_in[3];
  const u16* Ci = (const u16*)d_in[4];
  const u16* logdt = (const u16*)d_in[5];
  const u16* Dskip = (const u16*)d_in[6];
  const u16* wg = (const u16*)d_in[8];
  const u16* wu = (const u16*)d_in[9];
  const u16* wd = (const u16*)d_in[10];
  u16* out = (u16*)d_out;

  char* ws = (char*)d_ws;
  size_t off = 0;
  auto alloc = [&](size_t bytes) { char* p = ws + off; off += (bytes + 255) & ~(size_t)255; return p; };
  u16* uT = (u16*)alloc((size_t)D_ * LT_ * 2);
  u16* sT = (u16*)alloc((size_t)D_ * LT_ * 2);
  u16* h = (u16*)alloc((size_t)L_ * D_ * 2);
  u16* xni = (u16*)alloc((size_t)L_ * D_ * 2);
  u16* hmf = (u16*)alloc((size_t)L_ * F_ * 2);
  u16* w1i = (u16*)alloc((size_t)2 * F_ * D_ * 2);
  u16* wdf = (u16*)alloc((size_t)D_ * F_ * 2);
  u32* Kc = (u32*)alloc((size_t)D_ * KC_STRIDE * 4);

  K1Args a;
  a.x = x; a.wg = wg; a.wu = wu; a.wd = wd;
  a.logA = logA; a.Aim = Aim; a.Cr = Cr; a.Ci = Ci; a.logdt = logdt;
  a.uT = uT; a.w1i = w1i; a.wdf = wdf; a.Kc = Kc;
  k1_kernel<<<K1_BLOCKS, 256, 0, stream>>>(a);
  K2Args a2;
  a2.Kc = Kc; a2.Dskip = Dskip; a2.uT = uT; a2.sT = sT;
  k2_conv<<<D_, 256, 0, stream>>>(a2);
  k_ln2<<<L_ / 16, 256, 0, stream>>>(sT, x, h, xni);
  k3_gemm1<<<256, 512, 0, stream>>>(xni, w1i, hmf);
  k4_gemm2<<<256, 512, 0, stream>>>(hmf, wdf, h, out);
}
```

```cpp
#include <hip/hip_runtime.h>
#include <hip/hip_bf16.h>
#include <stdint.h>

constexpr int L_ = 2048;
constexpr int D_ = 640;
constexpr int N_ = 128;
constexpr int F_ = 1728;
constexpr int LT_ = 2112;
constexpr int KC_STRIDE = 648;

using bf16x8 = __attribute__((ext_vector_type(8))) short;
using f32x4 = __attribute__((ext_vector_type(4))) float;
using u32x4 = __attribute__((ext_vector_type(4))) unsigned int;
using u32x2 = __attribute__((ext_vector_type(2))) unsigned int;
typedef unsigned short u16;
typedef unsigned int u32;

__device__ __forceinline__ float bf2f(u16 v) { return __uint_as_float(((u32)v) << 16); }
__device__ __forceinline__ u16 f2bf(float f) {
  __hip_bfloat16 b = __float2bfloat16(f);
  return *reinterpret_cast<u16*>(&b);
}
__device__ __forceinline__ float rbf(float f) { return bf2f(f2bf(f)); }
__device__ __forceinline__ float lo_f(u32 v) { return __uint_as_float(v << 16); }
__device__ __forceinline__ float hi_f(u32 v) { return __uint_as_float(v & 0xffff0000u); }
__device__ __forceinline__ u32 pack2(float a, float b) { return (u32)f2bf(a) | ((u32)f2bf(b) << 16); }

template <int CTRL, int RMASK>
__device__ __forceinline__ float dpp_add(float v) {
  const int t = __builtin_amdgcn_update_dpp(0, __float_as_int(v), CTRL, RMASK, 0xf, false);
  return v + __int_as_float(t);
}
__device__ __forceinline__ float wave_sum(float v) {
  v = dpp_add<0x111, 0xf>(v);
  v = dpp_add<0x112, 0xf>(v);
  v = dpp_add<0x114, 0xf>(v);
  v = dpp_add<0x118, 0xf>(v);
  v = dpp_add<0x142, 0xa>(v);
  v = dpp_add<0x143, 0xc>(v);
  return __int_as_float(__builtin_amdgcn_readlane(__float_as_int(v), 63));
}

__device__ __forceinline__ void cexpp(float re2, float imr, float p, float& outr, float& outi) {
  const float mag = __builtin_amdgcn_exp2f(re2 * p);
  const float t = __builtin_amdgcn_fractf(imr * p);
  outr = mag * __builtin_amdgcn_cosf(t);
  outi = mag * __builtin_amdgcn_sinf(t);
}

__device__ __forceinline__ void ln_row(const float (&f)[10], float (&o)[10]) {
  float s = 0.f;
#pragma unroll
  for (int i = 0; i < 10; ++i) s += f[i];
  const float mean = wave_sum(s) * (1.f / D_);
  float q = 0.f;
#pragma unroll
  for (int i = 0; i < 10; ++i) { const float t = f[i] - mean; q += t * t; }
  const float var = wave_sum(q) * (1.f / D_);
  const float mb = rbf(mean);
  const float sd = rbf(sqrtf(rbf(rbf(var) + 1e-5f)));
  const float rs = 1.f / sd;
#pragma unroll
  for (int i = 0; i < 10; ++i) {
    const float t = rbf(f[i] - mb);
    const float q0 = t * rs;
    o[i] = fmaf(fmaf(-q0, sd, t), rs, q0);
  }
}

constexpr int K1_KG_BLOCKS = D_ / 4;
constexpr int K1_LN_BLOCKS = L_ / 16;
constexpr int K1_W1_TILES = (D_ / 64) * (F_ / 64);
constexpr int K1_WD_TILES = (F_ / 64) * (D_ / 64);
constexpr int K1_W_BLOCKS = (K1_W1_TILES + K1_WD_TILES) / 4;
constexpr int K1_BLOCKS = K1_KG_BLOCKS + K1_LN_BLOCKS + K1_W_BLOCKS;
constexpr int K1_SUB_LDS = 18432;

struct K1Args {
  const u16* x; const u16* wg; const u16* wu; const u16* wd;
  const u16* logA; const u16* Aim; const u16* Cr; const u16* Ci; const u16* logdt;
  u16* uT; u16* w1i; u16* wdf; u32* Kc;
};

__global__ __launch_bounds__(1024) void k1_kernel(K1Args a) {
  __shared__ __attribute__((aligned(16))) unsigned char smem_raw[4 * K1_SUB_LDS];
  const int lane = threadIdx.x & 63;
  int b = blockIdx.x;
  if (b < K1_KG_BLOCKS) {
    const int sub = threadIdx.x >> 8, tid = threadIdx.x & 255, wid = tid >> 6;
    const int d = b * 4 + sub;
    float* sp = reinterpret_cast<float*>(smem_raw + sub * K1_SUB_LDS);
    float* sr = sp + 512;
    const float dt = rbf(__expf(bf2f(a.logdt[d])));
    if (tid < N_) {
      const int n = tid;
      const float la = bf2f(a.logA[d * N_ + n]);
      f32x4 pv;
      pv[0] = (-rbf(expf(la)) * dt) * 1.4426950408889634f;
      pv[1] = (bf2f(a.Aim[d * N_ + n]) * dt) * 0.15915494309189535f;
      pv[2] = bf2f(a.Cr[d * N_ + n]) * dt;
      pv[3] = bf2f(a.Ci[d * N_ + n]) * dt;
      *reinterpret_cast<f32x4*>(sp + 4 * n) = pv;
    }
    __syncthreads();
    const int rc = lane & 15;
    const int kk = lane >> 4;
    f32x4 acc0 = {0, 0, 0, 0}, acc1 = {0, 0, 0, 0}, acc2 = {0, 0, 0, 0}, acc3 = {0, 0, 0, 0};
    const float pA = (float)(16 * rc);
    const float pB = (float)(rc + 1);
#pragma unroll 2
    for (int i = 0; i < 8; ++i) {
      const int n = wid * 32 + 4 * i + kk;
      const f32x4 pv = *reinterpret_cast<const f32x4*>(sp + 4 * n);
      const float cr = pv[2], ci = pv[3];
      float Ar, Ai, Br, Bi;
      cexpp(pv[0], pv[1], pA, Ar, Ai);
      cexpp(pv[0], pv[1], pB, Br, Bi);
#define KG_STEP(ACC)                                                              \
      {                                                                             \
        const float tr = Ar * cr - Ai * ci;                                         \
        const float ti = -(Ar * ci + Ai * cr);                                      \
        ACC = __builtin_amdgcn_mfma_f32_16x16x4f32(tr, Br, ACC, 0, 0, 0);           \
        ACC = __builtin_amdgcn_mfma_f32_16x16x4f32(ti, Bi, ACC, 0, 0, 0);           \
      }
#define KG_SQ()                                                                   \
      {                                                                             \
        const float t0 = Ar * Ar - Ai * Ai; Ai = 2.f * Ar * Ai; Ar = t0;            \
        const float t1 = Br * Br - Bi * Bi; Bi = 2.f * Br * Bi; Br = t1;            \
      }
      KG_STEP(acc0); KG_SQ();
      KG_STEP(acc1); KG_SQ();
      KG_STEP(acc2); KG_SQ();
      KG_STEP(acc3);
    }
    {
      float* w = sr + wid * 1024;
#pragma unroll
      for (int j = 0; j < 4; ++j) {
        const int e = (4 * kk + j) * 16 + rc;
        w[e] = acc0[j]; w[256 + e] = acc1[j]; w[512 + e] = acc2[j]; w[768 + e] = acc3[j];
      }
    }
    __syncthreads();
    u32* Kd = a.Kc + (size_t)d * KC_STRIDE;
    {
      const float v0 = 2.f * ((sr[tid] + sr[1024 + tid]) + (sr[2048 + tid] + sr[3072 + tid]));
      const u16 h0 = f2bf(v0);
      Kd[tid + 1] = (u32)h0 | ((u32)f2bf(v0 - bf2f(h0)) << 16);
      if (tid >= 128) {
#pragma unroll
        for (int gI = 1; gI < 4; ++gI) {
          const int e = gI * 256 + tid;
          const float vv = 2.f * ((sr[e] + sr[1024 + e]) + (sr[2048 + e] + sr[3072 + e]));
          const u16 hh = f2bf(vv);
          Kd[gI * 128 + tid + 1] = (u32)hh | ((u32)f2bf(vv - bf2f(hh)) << 16);
        }
      }
      if (wid == 0) {
        const float c0 = sp[4 * lane + 2] + sp[4 * (lane + 64) + 2];
        const float s0 = 2.f * wave_sum(c0);
        const u16 hh = f2bf(s0);
        if (lane == 0) Kd[0] = (u32)hh | ((u32)f2bf(s0 - bf2f(hh)) << 16);
      }
    }
    return;
  }
  b -= K1_KG_BLOCKS;
  if (b < K1_LN_BLOCKS) {
    u16* su = reinterpret_cast<u16*>(smem_raw);
    const int wid = threadIdx.x >> 6;
    const int row0 = b * 16;
    const u32* xr = reinterpret_cast<const u32*>(a.x + (size_t)(row0 + wid) * D_);
    float f[10], o[10];
#pragma unroll
    for (int i = 0; i < 5; ++i) { const u32 v = xr[lane + 64 * i]; f[2 * i] = lo_f(v); f[2 * i + 1] = hi_f(v); }
    ln_row(f, o);
#pragma unroll
    for (int i = 0; i < 5; ++i)
      *reinterpret_cast<u32*>(su + wid * 648 + 2 * (lane + 64 * i)) = pack2(o[2 * i], o[2 * i + 1]);
    __syncthreads();
    const int d = threadIdx.x;
    if (d < D_) {
      u32 pu[8];
#pragma unroll
      for (int r = 0; r < 8; ++r)
        pu[r] = (u32)su[(2 * r) * 648 + d] | ((u32)su[(2 * r + 1) * 648 + d] << 16);
      u32x4* du = reinterpret_cast<u32x4*>(a.uT + (size_t)d * LT_ + row0);
      du[0] = (u32x4){pu[0], pu[1], pu[2], pu[3]};
      du[1] = (u32x4){pu[4], pu[5], pu[6], pu[7]};
    }
    return;
  }
  b -= K1_LN_BLOCKS;
  {
    const int sub = threadIdx.x >> 8, tid = threadIdx.x & 255;
    const int tile = b * 4 + sub;
    u16* st = reinterpret_cast<u16*>(smem_raw + sub * K1_SUB_LDS);
    if (tile < K1_W1_TILES) {
      const int kt = tile / 27, cc0 = (tile % 27) * 64;
#pragma unroll
      for (int i = 0; i < 4; ++i) {
        const int idx = tid + 256 * i;
        const int mat = idx >> 9, r = (idx >> 3) & 63, c = (idx & 7) * 8;
        const u16* src = mat ? a.wu : a.wg;
        const u32x4 v = *reinterpret_cast<const u32x4*>(src + (size_t)(kt * 64 + r) * F_ + cc0 + c);
        *reinterpret_cast<u32x4*>(st + (mat * 64 + r) * 72 + c) = v;
      }
    } else {
      const int t2 = tile - K1_W1_TILES;
      const int k0 = (t2 / 10) * 64, n0 = (t2 % 10) * 64;
#pragma unroll
      for (int i = 0; i < 2; ++i) {
        const int idx = tid + 256 * i;
        const int r = idx >> 3, c = (idx & 7) * 8;
        const u32x4 v = *reinterpret_cast<const u32x4*>(a.wd + (size_t)(k0 + r) * D_ + n0 + c);
        *reinterpret_cast<u32x4*>(st + r * 72 + c) = v;
      }
    }
    __syncthreads();
    if (tile < K1_W1_TILES) {
      const int kt = tile / 27, cc0 = (tile % 27) * 64;
#pragma unroll
      for (int i = 0; i < 4; ++i) {
        const int idx = tid + 256 * i;
        const int gl = idx >> 8, rr = (idx >> 3) & 31, pc = idx & 7;
        const int ch = pc ^ (rr & 7);
        const int mat = rr >> 4, col = gl * 16 + (rr & 15);
        u32 p[4];
#pragma unroll
        for (int j = 0; j < 4; ++j)
          p[j] = (u32)st[(mat * 64 + ch * 8 + 2 * j) * 72 + col] | ((u32)st[(mat * 64 + ch * 8 + 2 * j + 1) * 72 + col] << 16);
        const int grp = (cc0 >> 4) + gl;
        u16* dst = a.w1i + ((size_t)(kt * 108 + grp) * 2048) + rr * 64 + pc * 8;
        *reinterpret_cast<u32x4*>(dst) = (u32x4){p[0], p[1], p[2], p[3]};
      }
    } else {
      const int t2 = tile - K1_W1_TILES;
      const int k0 = (t2 / 10) * 64, n0 = (t2 % 10) * 64;
#pragma unroll
      for (int i = 0; i < 2; ++i) {
        const int idx = tid + 256 * i;
        const int ntl = idx >> 7, ksl = (idx >> 6) & 1, ln = idx & 63;
        const int fr = ln & 15, fq = ln >> 4;
        const int kb = ksl * 32 + fq * 8, col = ntl * 16 + fr;
        u32 p[4];
#pragma unroll
        for (int j = 0; j < 4; ++j)
          p[j] = (u32)st[(kb + 2 * j) * 72 + col] | ((u32)st[(kb + 2 * j + 1) * 72 + col] << 16);
        const int nt = (n0 >> 4) + ntl, ks = (k0 >> 5) + ksl;
        u16* dst = a.wdf + ((size_t)(nt * 54 + ks) * 64 + ln) * 8;
        *reinterpret_cast<u32x4*>(dst) = (u32x4){p[0], p[1], p[2], p[3]};
      }
    }
  }
}

constexpr int CV_KLEN = 2208;
constexpr int CV_USTRIDE = 2320;
constexpr int CV_OFF_U = 2 * CV_KLEN * 2;
constexpr int CV_LDS_BYTES = CV_OFF_U + 8 * CV_USTRIDE * 2;

template <int TMIN>
__device__ __forceinline__ void conv_steps(int s0, int s1, const u16* kh, const u16* kl, const u16* ub,
                                           f32x4 (&acc)[8]) {
  for (int s = s0; s < s1; ++s) {
    const bf16x8 ah = *reinterpret_cast<const bf16x8*>(kh + 32 * s);
    const bf16x8 al = *reinterpret_cast<const bf16x8*>(kl + 32 * s);
    bf16x8 bv[8];
#pragma unroll
    for (int t = TMIN; t < 8; ++t) bv[t] = *reinterpret_cast<const bf16x8*>(ub + 32 * s - 256 * t);
#pragma unroll
    for (int t = TMIN; t < 8; ++t) {
      acc[t] = __builtin_amdgcn_mfma_f32_16x16x32_bf16(ah, bv[t], acc[t], 0, 0, 0);
      acc[t] = __builtin_amdgcn_mfma_f32_16x16x32_bf16(al, bv[t], acc[t], 0, 0, 0);
    }
  }
}

struct K2Args {
  const u32* Kc; const u16* Dskip; const u16* uT; u16* sT;
};

__device__ __forceinline__ void build_u_chunk(const u32 (&W)[8], u16* dst) {
#pragma unroll
  for (int rho = 0; rho < 8; ++rho) {
    u32 o[4];
#pragma unroll
    for (int k = 0; k < 4; ++k) {
      if ((rho & 1) == 0) {
        const int aI = (rho + 8 - 2 * k) >> 1;
        o[k] = (W[aI] & 0xffffu) | (W[aI - 1] & 0xffff0000u);
      } else {
        const int aI = (rho + 7 - 2 * k) >> 1;
        o[k] = (W[aI] >> 16) | (W[aI] << 16);
      }
    }
    *reinterpret_cast<u32x4*>(dst + rho * CV_USTRIDE) = (u32x4){o[0], o[1], o[2], o[3]};
  }
}

__global__ __launch_bounds__(256, 3) void k2_conv(K2Args a) {
  __shared__ __attribute__((aligned(16))) unsigned char smem[CV_LDS_BYTES];
  u16* sKh = reinterpret_cast<u16*>(smem);
  u16* sKl = sKh + CV_KLEN;
  u16* sU = reinterpret_cast<u16*>(smem + CV_OFF_U);
  const int tid = threadIdx.x, lane = tid & 63, wid = tid >> 6;
  const int d = blockIdx.x;
  const u16* ud = a.uT + (size_t)d * LT_;
  const u32* Kd = a.Kc + (size_t)d * KC_STRIDE;
  const int q = 255 - tid;
  const u32x4 w0 = *reinterpret_cast<const u32x4*>(ud + 8 * q);
  u32x4 w1 = {0, 0, 0, 0};
  if (q < 255) w1 = *reinterpret_cast<const u32x4*>(ud + 8 * q + 8);
  u32 kv[8];
  {
    const int j0 = 8 * tid;
    const int e = (j0 < 256) ? 0 : (j0 < 512) ? 1 : (j0 < 1024) ? 2 : 3;
    const int base = 128 * e;
    const int hm1 = ((1 << e) >> 1) - 1;
#pragma unroll
    for (int jj = 0; jj < 8; ++jj) {
      const int j = j0 + jj;
      const int pq = (e == 0) ? j : ((j + hm1 + ((j >> e) & 1)) >> e);
      kv[jj] = Kd[base + pq];
    }
  }
  {
    const u32 W[8] = {w0[0], w0[1], w0[2], w0[3], w1[0], w1[1], w1[2], w1[3]};
    build_u_chunk(W, sU + 8 * tid);
    if (tid == 255) {
      const u32 W2[8] = {0, 0, 0, 0, w0[0], w0[1], w0[2], w0[3]};
      build_u_chunk(W2, sU + 8 * 256);
    }
    if (tid < 8 * 19) {
      const int rho = tid / 19, m = 257 + tid % 19;
      *reinterpret_cast<u32x4*>(sU + rho * CV_USTRIDE + 8 * m) = (u32x4){0, 0, 0, 0};
    } else if (tid >= 160 && tid < 200) {
      const int t = tid - 160;
      const int m = (t % 20 < 16) ? t % 20 : 256 + t % 20;
      u16* dst = (t < 20) ? sKh : sKl;
      *reinterpret_cast<u32x4*>(dst + 8 * m) = (u32x4){0, 0, 0, 0};
    }
    u32x4 ph, pl;
#pragma unroll
    for (int k = 0; k < 4; ++k) {
      ph[k] = (kv[2 * k] & 0xffffu) | (kv[2 * k + 1] << 16);
      pl[k] = (kv[2 * k] >> 16) | (kv[2 * k + 1] & 0xffff0000u);
    }
    *reinterpret_cast<u32x4*>(sKh + 128 + 8 * tid) = ph;
    *reinterpret_cast<u32x4*>(sKl + 128 + 8 * tid) = pl;
  }
  __syncthreads();
  const int r = lane & 15, g = lane >> 4;
  const int ap = (lane & 15) >> 3, rho = lane & 7;
  f32x4 acc[8];
#pragma unroll
  for (int t = 0; t < 8; ++t) acc[t] = (f32x4){0, 0, 0, 0};
  const u16* kh = sKh + 128 + 8 * r + 8 * g;
  const u16* kl = sKl + 128 + 8 * r + 8 * g;
  const u16* ub = sU + rho * CV_USTRIDE + 8 * g - 128 * ap + 2048;
  if (wid == 0) {
    conv_steps<0>(-4, 5, kh, kl, ub, acc);
  } else if (wid == 1) {
    conv_steps<1>(5, 13, kh, kl, ub, acc);
    conv_steps<2>(13, 16, kh, kl, ub, acc);
  } else if (wid == 2) {
    conv_steps<2>(16, 21, kh, kl, ub, acc);
    conv_steps<3>(21, 29, kh, kl, ub, acc);
    conv_steps<4>(29, 30, kh, kl, ub, acc);
  } else {
    conv_steps<4>(30, 37, kh, kl, ub, acc);
    conv_steps<5>(37, 45, kh, kl, ub, acc);
    conv_steps<6>(45, 53, kh, kl, ub, acc);
    conv_steps<7>(53, 61, kh, kl, ub, acc);
  }
  __syncthreads();
  float* sy = reinterpret_cast<float*>(smem);
#pragma unroll
  for (int t = 0; t < 8; ++t) {
#pragma unroll
    for (int j = 0; j < 4; ++j) {
      const int l = 256 * t + 128 * ap + rho + 8 * (4 * g + j);
      sy[wid * L_ + l] = acc[t][j];
    }
  }
  __syncthreads();
  const float dsk = bf2f(a.Dskip[d]);
  {
    const int l0 = q * 8;
    float y[8];
#pragma unroll
    for (int hh = 0; hh < 2; ++hh) {
      f32x4 a0 = *reinterpret_cast<const f32x4*>(sy + l0 + 4 * hh);
      f32x4 a1 = *reinterpret_cast<const f32x4*>(sy + L_ + l0 + 4 * hh);
      f32x4 a2 = *reinterpret_cast<const f32x4*>(sy + 2 * L_ + l0 + 4 * hh);
      f32x4 a3 = *reinterpret_cast<const f32x4*>(sy + 3 * L_ + l0 + 4 * hh);
#pragma unroll
      for (int j = 0; j < 4; ++j) y[4 * hh + j] = (a0[j] + a1[j]) + (a2[j] + a3[j]);
    }
    u32 o[4];
#pragma unroll
    for (int k = 0; k < 4; ++k) {
      const float t0 = rbf(lo_f(w0[k]) * dsk), t1 = rbf(hi_f(w0[k]) * dsk);
      o[k] = pack2(rbf(y[2 * k]) + t0, rbf(y[2 * k + 1]) + t1);
    }
    *reinterpret_cast<u32x4*>(a.sT + (size_t)d * LT_ + l0) = (u32x4){o[0], o[1], o[2], o[3]};
  }
}

__global__ __launch_bounds__(1024) void k_ln2(const u16* __restrict__ sT, const u16* __restrict__ x,
                                              u16* __restrict__ h, u16* __restrict__ xni) {
  __shared__ __attribute__((aligned(16))) u16 ss[16 * 648];
  const int tid = threadIdx.x, lane = tid & 63, wid = tid >> 6;
  const int row0 = blockIdx.x * 16;
  const int row = row0 + wid;
  u32 xv[5];
  {
    const u32* xr = reinterpret_cast<const u32*>(x + (size_t)row * D_);
#pragma unroll
    for (int i = 0; i < 5; ++i) xv[i] = xr[lane + 64 * i];
  }
  if (tid < D_) {
    const int d = tid;
    const u32x4* src = reinterpret_cast<const u32x4*>(sT + (size_t)d * LT_ + row0);
    const u32x4 a0 = src[0], a1 = src[1];
#pragma unroll
    for (int k = 0; k < 4; ++k) {
      ss[(2 * k) * 648 + d] = (u16)(a0[k] & 0xffffu);
      ss[(2 * k + 1) * 648 + d] = (u16)(a0[k] >> 16);
      ss[(8 + 2 * k) * 648 + d] = (u16)(a1[k] & 0xffffu);
      ss[(8 + 2 * k + 1) * 648 + d] = (u16)(a1[k] >> 16);
    }
  }
  __syncthreads();
  {
    float f[10], o[10];
    u32* hrow = reinterpret_cast<u32*>(h + (size_t)row * D_);
#pragma unroll
    for (int i = 0; i < 5; ++i) {
      const u32 sv = *reinterpret_cast<const u32*>(ss + wid * 648 + 2 * (lane + 64 * i));
      const u32 hv = pack2(lo_f(xv[i]) + lo_f(sv), hi_f(xv[i]) + hi_f(sv));
      hrow[lane + 64 * i] = hv;
      f[2 * i] = lo_f(hv); f[2 * i + 1] = hi_f(hv);
    }
    ln_row(f, o);
    const int mt = row >> 7, r = row & 127;
#pragma unroll
    for (int i = 0; i < 5; ++i) {
      const int c = 2 * (lane + 64 * i);
      const int kt = c >> 6, cc = c & 63;
      const int off = ((mt * 10 + kt) * 128 + r) * 64 + (((cc >> 3) ^ (r & 7)) << 3) + (cc & 7);
      *reinterpret_cast<u32*>(xni + off) = pack2(o[2 * i], o[2 * i + 1]);
    }
  }
}

__device__ __forceinline__ float gelu_tanh(float x) {
  const float c0 = -2.f * 0.7978845608028654f * 1.4426950408889634f;
  const float c1 = c0 * 0.044715f;
  const float e = __builtin_amdgcn_exp2f(x * fmaf(x * x, c1, c0));
  return x * __builtin_amdgcn_rcpf(1.f + e);
}

constexpr int G1_A_BYTES = 16384;
constexpr int G1_B_BYTES = 7 * 4096;
constexpr int G1_STAGE = G1_A_BYTES + G1_B_BYTES;
constexpr int G1_NSTG = 3;

template <int NG>
__device__ __forceinline__ void g1_ldfrag(const unsigned char* sb, int arow, int brow, int cpos,
                                          bf16x8 (&xf)[2], bf16x8 (&wf)[8]) {
#pragma unroll
  for (int m = 0; m < 2; ++m)
    xf[m] = *reinterpret_cast<const bf16x8*>(sb + (arow + m * 16) * 128 + cpos);
#pragma unroll
  for (int n = 0; n < 2 * NG; ++n)
    wf[n] = *reinterpret_cast<const bf16x8*>(sb + G1_A_BYTES + (brow + n * 16) * 128 + cpos);
}
template <int NG>
__device__ __forceinline__ void g1_mma(const bf16x8 (&xf)[2], const bf16x8 (&wf)[8], f32x4 (&acc)[2][8]) {
#pragma unroll
  for (int n = 0; n < 2 * NG; ++n)
#pragma unroll
    for (int m = 0; m < 2; ++m)
      acc[m][n] = __builtin_amdgcn_mfma_f32_16x16x32_bf16(wf[n], xf[m], acc[m][n], 0, 0, 0);
}

template <int NG>
__device__ __forceinline__ void g1_main(unsigned char* smem, const unsigned char* Ag, const unsigned char* Bg,
                                        int g0, int wid, int lane, int arow, int brow, int cpos0, int cpos1,
                                        f32x4 (&acc)[2][8]) {
#define G1_ISSUE(kt, buf)                                                                              \
  {                                                                                                    \
    unsigned char* sb_ = smem + (buf) * G1_STAGE;                                                      \
    _Pragma("unroll") for (int i = 0; i < 2; ++i) {                                                    \
      const int piece = wid + 8 * i;                                                                   \
      __builtin_amdgcn_global_load_lds(                                                                \
          (const __attribute__((address_space(1))) void*)(Ag + (size_t)(kt) * G1_A_BYTES + piece * 1024 + lane * 16), \
          (__attribute__((address_space(3))) void*)(sb_ + piece * 1024), 16, 0, 0);                    \
    }                                                                                                  \
    _Pragma("unroll") for (int i = 0; i < 4; ++i) {                                                    \
      int pb = wid + 8 * i; if (pb >= 28) pb -= 4;                                                     \
      const int gi = pb >> 2;                                                                          \
      const int grp = (g0 + gi < 108) ? g0 + gi : 107;                                                 \
      __builtin_amdgcn_global_load_lds(                                                                \
          (const __attribute__((address_space(1))) void*)(Bg + ((size_t)((kt) * 108 + grp) * 4096) + (pb & 3) * 1024 + lane * 16), \
          (__attribute__((address_space(3))) void*)(sb_ + G1_A_BYTES + pb * 1024), 16, 0, 0);          \
    }                                                                                                  \
  }
  bf16x8 xf0[2], wf0[8], xf1[2], wf1[8];
  G1_ISSUE(0, 0);
  G1_ISSUE(1, 1);
  G1_ISSUE(2, 2);
  asm volatile("s_waitcnt vmcnt(12)" ::: "memory");
  __builtin_amdgcn_s_barrier();
  asm volatile("" ::: "memory");
  g1_ldfrag<NG>(smem, arow, brow, cpos0, xf0, wf0);
  int buf = 0;
#pragma unroll 1
  for (int kt = 0; kt < 10; ++kt) {
    const unsigned char* sb = smem + buf * G1_STAGE;
    const int nbuf = (buf == 2) ? 0 : buf + 1;
    g1_ldfrag<NG>(sb, arow, brow, cpos1, xf1, wf1);
    g1_mma<NG>(xf0, wf0, acc);
    if (kt < 9) {
      if (kt < 8) asm volatile("s_waitcnt vmcnt(6) lgkmcnt(0)" ::: "memory");
      else asm volatile("s_waitcnt vmcnt(0) lgkmcnt(0)" ::: "memory");
      __builtin_amdgcn_s_barrier();
      asm volatile("" ::: "memory");
      if (kt + 3 < 10) G1_ISSUE(kt + 3, buf);
      g1_ldfrag<NG>(smem + nbuf * G1_STAGE, arow, brow, cpos0, xf0, wf0);
    }
    g1_mma<NG>(xf1, wf1, acc);
    buf = nbuf;
  }
}

__global__ __launch_bounds__(512) void k3_gemm1(const u16* __restrict__ xni, const u16* __restrict__ w1i,
                                                u16* __restrict__ hmf) {
  __shared__ __attribute__((aligned(16))) unsigned char smem[G1_NSTG * G1_STAGE];
  const int tid = threadIdx.x, lane = tid & 63;
  const int wid = __builtin_amdgcn_readfirstlane(tid >> 6);
  const int wm = wid & 3, wn = wid >> 2;
  const int fr = lane & 15, fq = lane >> 4;
  const int bid = blockIdx.x;
  const int xcd = bid & 7, idx = bid >> 3;
  const int mt = (xcd & 1) * 8 + (idx & 7);
  const int nb = (xcd >> 1) * 4 + (idx >> 3);
  const int g0 = 27 * (nb >> 2) + 7 * (nb & 3);
  const int ng = ((nb & 3) == 3) ? 6 : 7;
  const int gw0 = wn * 4;
  const int gwn = wn ? ng - 4 : 4;
  const unsigned char* Ag = reinterpret_cast<const unsigned char*>(xni) + (size_t)mt * 10 * G1_A_BYTES;
  const unsigned char* Bg = reinterpret_cast<const unsigned char*>(w1i);
  f32x4 acc[2][8];
#pragma unroll
  for (int m = 0; m < 2; ++m)
#pragma unroll
    for (int n = 0; n < 8; ++n) acc[m][n] = (f32x4){0, 0, 0, 0};
  const int swz = fr & 7;
  const int cpos0 = ((0 + fq) ^ swz) << 4, cpos1 = ((4 + fq) ^ swz) << 4;
  const int arow = wm * 32 + fr, brow = gw0 * 32 + fr;
  if (wn == 0) g1_main<4>(smem, Ag, Bg, g0, wid, lane, arow, brow, cpos0, cpos1, acc);
  else g1_main<3>(smem, Ag, Bg, g0, wid, lane, arow, brow, cpos0, cpos1, acc);
#pragma unroll
  for (int m = 0; m < 2; ++m) {
    const int mt16 = mt * 8 + wm * 2 + m;
#pragma unroll
    for (int gI = 0; gI < 4; ++gI) {
      if (gI < gwn) {
        float o[4];
#pragma unroll
        for (int j = 0; j < 4; ++j) {
          const float gv = rbf(acc[m][2 * gI][j]);
          const float uv = rbf(acc[m][2 * gI + 1][j]);
          o[j] = rbf(gelu_tanh(gv)) * uv;
        }
        const int f = 16 * (g0 + gw0 + gI) + 4 * fq;
        const int ks = f >> 5, fq2 = (f & 31) >> 3, j0 = f & 7;
        u16* dst = hmf + ((size_t)(mt16 * 54 + ks) * 64 + fr + 16 * fq2) * 8 + j0;
        *reinterpret_cast<u32x2*>(dst) = (u32x2){pack2(o[0], o[1]), pack2(o[2], o[3])};
      }
    }
  }
}

__global__ __launch_bounds__(256) void k4_gemm2(const u16* __restrict__ hmf, const u16* __restrict__ wdf,
                                                const u16* __restrict__ h, u16* __restrict__ out) {
  __shared__ __attribute__((aligned(16))) float sred[4 * 64 * 84];
  const int tid = threadIdx.x, lane = tid & 63;
  const int wk = __builtin_amdgcn_readfirstlane(tid >> 6);
  const int fr = lane & 15, fq = lane >> 4;
  const int bid = blockIdx.x;
  const int xcd = bid & 7, idx = bid >> 3;
  const int mt = (xcd >> 1) * 8 + (idx & 7);
  const int nb = (xcd & 1) * 4 + (idx >> 3);
  const int ks0 = (wk < 2) ? wk * 14 : 28 + (wk - 2) * 13;
  const int nks = (wk < 2) ? 14 : 13;
  const bf16x8* Ap = reinterpret_cast<const bf16x8*>(hmf) + (size_t)(mt * 4) * 54 * 64 + lane;
  const bf16x8* Bp = reinterpret_cast<const bf16x8*>(wdf) + (size_t)(nb * 5) * 54 * 64 + lane;
  const int rl = tid >> 2, c0 = (tid & 3) * 20;
  const int row = mt * 64 + rl;
  u32x2 hres[5];
  {
    const u16* hr = h + (size_t)row * D_ + nb * 80 + c0;
#pragma unroll
    for (int i = 0; i < 5; ++i) hres[i] = *reinterpret_cast<const u32x2*>(hr + 4 * i);
  }
  f32x4 acc[4][5];
#pragma unroll
  for (int m = 0; m < 4; ++m)
#pragma unroll
    for (int n = 0; n < 5; ++n) acc[m][n] = (f32x4){0, 0, 0, 0};
  bf16x8 xa[4][4], wb[4][5];
#define G2_LOAD(slot, kidx)                                                              \
  {                                                                                      \
    const int kq_ = ks0 + (((kidx) < nks) ? (kidx) : nks - 1);                           \
    _Pragma("unroll") for (int m = 0; m < 4; ++m) xa[slot][m] = Ap[(m * 54 + kq_) * 64];   \
    _Pragma("unroll") for (int n = 0; n < 5; ++n) wb[slot][n] = Bp[(n * 54 + kq_) * 64];   \
  }
#define G2_MMA(slot)                                                                     \
  {                                                                                      \
    _Pragma("unroll") for (int n = 0; n < 5; ++n)                                        \
    _Pragma("unroll") for (int m = 0; m < 4; ++m)                                        \
      acc[m][n] = __builtin_amdgcn_mfma_f32_16x16x32_bf16(wb[slot][n], xa[slot][m], acc[m][n], 0, 0, 0); \
  }
  G2_LOAD(0, 0);
  G2_LOAD(1, 1);
  G2_LOAD(2, 2);
#pragma unroll
  for (int i = 0; i < 13; ++i) {
    if (i + 3 < 14) G2_LOAD((i + 3) & 3, i + 3);
    G2_MMA(i & 3);
  }
  if (nks == 14) G2_MMA(13 & 3);
  float* sw = sred + wk * 64 * 84;
#pragma unroll
  for (int m = 0; m < 4; ++m)
#pragma unroll
    for (int n = 0; n < 5; ++n)
      *reinterpret_cast<f32x4*>(sw + (m * 16 + fr) * 84 + n * 16 + 4 * fq) = acc[m][n];
  __syncthreads();
  {
    u16* orow = out + (size_t)row * D_ + nb * 80 + c0;
#pragma unroll
    for (int i = 0; i < 5; ++i) {
      const float* p = sred + rl * 84 + c0 + 4 * i;
      const f32x4 a0 = *reinterpret_cast<const f32x4*>(p);
      const f32x4 a1 = *reinterpret_cast<const f32x4*>(p + 64 * 84);
      const f32x4 a2 = *reinterpret_cast<const f32x4*>(p + 2 * 64 * 84);
      const f32x4 a3 = *reinterpret_cast<const f32x4*>(p + 3 * 64 * 84);
      float o[4];
#pragma unroll
      for (int j = 0; j < 4; ++j) o[j] = rbf((a0[j] + a1[j]) + (a2[j] + a3[j]));
      const u32 r0 = pack2(lo_f(hres[i][0]) + o[0], hi_f(hres[i][0]) + o[1]);
      const u32 r1 = pack2(lo_f(hres[i][1]) + o[2], hi_f(hres[i][1]) + o[3]);
      *reinterpret_cast<u32x2*>(orow + 4 * i) = (u32x2){r0, r1};
    }
  }
}

extern "C" void kernel_launch(void* const* d_in, const int* in_sizes, int n_in,
                              void* d_out, int out_size, void* d_ws, size_t ws_size,
                              hipStream_t stream) {
  const u16* x = (const u16*)d_in[0];
  const u16* logA = (const u16*)d_in[1];
  const u16* Aim = (const u16*)d_in[2];
  const u16* Cr = (const u16*)d_in[3];
  const u16* Ci = (const u16*)d_in[4];
  const u16* logdt = (const u16*)d_in[5];
  const u16* Dskip = (const u16*)d_in[6];
  const u16* wg = (const u16*)d_in[8];
  const u16* wu = (const u16*)d_in[9];
  const u16* wd = (const u16*)d_in[10];
  u16* out = (u16*)d_out;

  char* ws = (char*)d_ws;
  size_t off = 0;
  auto alloc = [&](size_t bytes) { char* p = ws + off; off += (bytes + 255) & ~(size_t)255; return p; };
  u16* uT = (u16*)alloc((size_t)D_ * LT_ * 2);
  u16* sT = (u16*)alloc((size_t)D_ * LT_ * 2);
  u16* h = (u16*)alloc((size_t)L_ * D_ * 2);
  u16* xni = (u16*)alloc((size_t)L_ * D_ * 2);
  u16* hmf = (u16*)alloc((size_t)L_ * F_ * 2);
  u16* w1i = (u16*)alloc((size_t)2 * F_ * D_ * 2);
  u16* wdf = (u16*)alloc((size_t)D_ * F_ * 2);
  u32* Kc = (u32*)alloc((size_t)D_ * KC_STRIDE * 4);

  K1Args a;
  a.x = x; a.wg = wg; a.wu = wu; a.wd = wd;
  a.logA = logA; a.Aim = Aim; a.Cr = Cr; a.Ci = Ci; a.logdt = logdt;
  a.uT = uT; a.w1i = w1i; a.wdf = wdf; a.Kc = Kc;
  k1_kernel<<<K1_BLOCKS, 1024, 0, stream>>>(a);
  K2Args a2;
  a2.Kc = Kc; a2.Dskip = Dskip; a2.uT = uT; a2.sT = sT;
  k2_conv<<<D_, 256, 0, stream>>>(a2);
  k_ln2<<<L_ / 16, 1024, 0, stream>>>(sT, x, h, xni);
  k3_gemm1<<<256, 512, 0, stream>>>(xni, w1i, hmf);
  k4_gemm2<<<256, 256, 0, stream>>>(hmf, wdf, h, out);
}
```

```cpp
#include <hip/hip_runtime.h>
#include <hip/hip_bf16.h>
#include <stdint.h>

constexpr int L_ = 2048;
constexpr int D_ = 640;
constexpr int N_ = 128;
constexpr int F_ = 1728;
constexpr int LT_ = 2112;
constexpr int KC_STRIDE = 648;

using bf16x8 = __attribute__((ext_vector_type(8))) short;
using f32x4 = __attribute__((ext_vector_type(4))) float;
using u32x4 = __attribute__((ext_vector_type(4))) unsigned int;
using u32x2 = __attribute__((ext_vector_type(2))) unsigned int;
typedef unsigned short u16;
typedef unsigned int u32;

__device__ __forceinline__ float bf2f(u16 v) { return __uint_as_float(((u32)v) << 16); }
__device__ __forceinline__ u16 f2bf(float f) {
  __hip_bfloat16 b = __float2bfloat16(f);
  return *reinterpret_cast<u16*>(&b);
}
__device__ __forceinline__ float rbf(float f) { return bf2f(f2bf(f)); }
__device__ __forceinline__ float lo_f(u32 v) { return __uint_as_float(v << 16); }
__device__ __forceinline__ float hi_f(u32 v) { return __uint_as_float(v & 0xffff0000u); }
__device__ __forceinline__ u32 pack2(float a, float b) { return (u32)f2bf(a) | ((u32)f2bf(b) << 16); }

template <int CTRL, int RMASK>
__device__ __forceinline__ float dpp_add(float v) {
  const int t = __builtin_amdgcn_update_dpp(0, __float_as_int(v), CTRL, RMASK, 0xf, false);
  return v + __int_as_float(t);
}
__device__ __forceinline__ float wave_sum(float v) {
  v = dpp_add<0x111, 0xf>(v);
  v = dpp_add<0x112, 0xf>(v);
  v = dpp_add<0x114, 0xf>(v);
  v = dpp_add<0x118, 0xf>(v);
  v = dpp_add<0x142, 0xa>(v);
  v = dpp_add<0x143, 0xc>(v);
  return __int_as_float(__builtin_amdgcn_readlane(__float_as_int(v), 63));
}

__device__ __forceinline__ void cexpp(float re2, float imr, float p, float& outr, float& outi) {
  const float mag = __builtin_amdgcn_exp2f(re2 * p);
  const float t = __builtin_amdgcn_fractf(imr * p);
  outr = mag * __builtin_amdgcn_cosf(t);
  outi = mag * __builtin_amdgcn_sinf(t);
}

__device__ __forceinline__ void ln_row(const float (&f)[10], float (&o)[10]) {
  float s = 0.f;
#pragma unroll
  for (int i = 0; i < 10; ++i) s += f[i];
  const float mean = wave_sum(s) * (1.f / D_);
  float q = 0.f;
#pragma unroll
  for (int i = 0; i < 10; ++i) { const float t = f[i] - mean; q += t * t; }
  const float var = wave_sum(q) * (1.f / D_);
  const float mb = rbf(mean);
  const float sd = rbf(sqrtf(rbf(rbf(var) + 1e-5f)));
  const float rs = 1.f / sd;
#pragma unroll
  for (int i = 0; i < 10; ++i) {
    const float t = rbf(f[i] - mb);
    const float q0 = t * rs;
    o[i] = fmaf(fmaf(-q0, sd, t), rs, q0);
  }
}

constexpr int K1_KG_BLOCKS = D_;
constexpr int K1_LN_BLOCKS = L_ / 16;
constexpr int K1_W1_TILES = (D_ / 64) * (F_ / 64);
constexpr int K1_WD_TILES = (F_ / 64) * (D_ / 64);
constexpr int K1_W_BLOCKS = (K1_W1_TILES + K1_WD_TILES);
constexpr int K1_BLOCKS = K1_KG_BLOCKS + K1_LN_BLOCKS;

struct K1Args {
  const u16* x; const u16* wg; const u16* wu; const u16* wd;
  const u16* logA; const u16* Aim; const u16* Cr; const u16* Ci; const u16* logdt;
  u16* uT; u16* w1i; u16* wdf; u32* Kc;
};

__global__ __launch_bounds__(256) void k1_kernel(K1Args a) {
  __shared__ __attribute__((aligned(16))) unsigned char smem_raw[16 * 648 * 2];
  const int tid = threadIdx.x, lane = tid & 63, wid = tid >> 6;
  int b = blockIdx.x;
  if (b < K1_LN_BLOCKS) {
    u16* su = reinterpret_cast<u16*>(smem_raw);
    const int row0 = b * 16;
    u32 v[4][5];
#pragma unroll
    for (int rr = 0; rr < 4; ++rr) {
      const u32* xr = reinterpret_cast<const u32*>(a.x + (size_t)(row0 + wid * 4 + rr) * D_);
#pragma unroll
      for (int i = 0; i < 5; ++i) v[rr][i] = xr[lane + 64 * i];
    }
#pragma unroll
    for (int rr = 0; rr < 4; ++rr) {
      float f[10], o[10];
#pragma unroll
      for (int i = 0; i < 5; ++i) { f[2 * i] = lo_f(v[rr][i]); f[2 * i + 1] = hi_f(v[rr][i]); }
      ln_row(f, o);
#pragma unroll
      for (int i = 0; i < 5; ++i)
        *reinterpret_cast<u32*>(su + (wid * 4 + rr) * 648 + 2 * (lane + 64 * i)) = pack2(o[2 * i], o[2 * i + 1]);
    }
    __syncthreads();
#pragma unroll
    for (int it = 0; it < 3; ++it) {
      const int d = tid + 256 * it;
      if (d < D_) {
        u32 pu[8];
#pragma unroll
        for (int r = 0; r < 8; ++r)
          pu[r] = (u32)su[(2 * r) * 648 + d] | ((u32)su[(2 * r + 1) * 648 + d] << 16);
        u32x4* du = reinterpret_cast<u32x4*>(a.uT + (size_t)d * LT_ + row0);
        du[0] = (u32x4){pu[0], pu[1], pu[2], pu[3]};
        du[1] = (u32x4){pu[4], pu[5], pu[6], pu[7]};
      }
    }
    return;
  }
  b -= K1_LN_BLOCKS;
  if (b < K1_KG_BLOCKS) {
    const int d = b;
    float* sp = reinterpret_cast<float*>(smem_raw);
    float* sr = sp + 512;
    const float dt = rbf(__expf(bf2f(a.logdt[d])));
    if (tid < N_) {
      const int n = tid;
      const float la = bf2f(a.logA[d * N_ + n]);
      f32x4 pv;
      pv[0] = (-rbf(expf(la)) * dt) * 1.4426950408889634f;
      pv[1] = (bf2f(a.Aim[d * N_ + n]) * dt) * 0.15915494309189535f;
      pv[2] = bf2f(a.Cr[d * N_ + n]) * dt;
      pv[3] = bf2f(a.Ci[d * N_ + n]) * dt;
      *reinterpret_cast<f32x4*>(sp + 4 * n) = pv;
    }
    __syncthreads();
    const int rc = lane & 15;
    const int kk = lane >> 4;
    f32x4 acc0 = {0, 0, 0, 0}, acc1 = {0, 0, 0, 0}, acc2 = {0, 0, 0, 0}, acc3 = {0, 0, 0, 0};
    const float pA = (float)(16 * rc);
    const float pB = (float)(rc + 1);
#pragma unroll 2
    for (int i = 0; i < 8; ++i) {
      const int n = wid * 32 + 4 * i + kk;
      const f32x4 pv = *reinterpret_cast<const f32x4*>(sp + 4 * n);
      const float cr = pv[2], ci = pv[3];
      float Ar, Ai, Br, Bi;
      cexpp(pv[0], pv[1], pA, Ar, Ai);
      cexpp(pv[0], pv[1], pB, Br, Bi);
#define KG_STEP(ACC)                                                              \
      {                                                                             \
        const float tr = Ar * cr - Ai * ci;                                         \
        const float ti = -(Ar * ci + Ai * cr);                                      \
        ACC = __builtin_amdgcn_mfma_f32_16x16x4f32(tr, Br, ACC, 0, 0, 0);           \
        ACC = __builtin_amdgcn_mfma_f32_16x16x4f32(ti, Bi, ACC, 0, 0, 0);           \
      }
#define KG_SQ()                                                                   \
      {                                                                             \
        const float t0 = Ar * Ar - Ai * Ai; Ai = 2.f * Ar * Ai; Ar = t0;            \
        const float t1 = Br * Br - Bi * Bi; Bi = 2.f * Br * Bi; Br = t1;            \
      }
      KG_STEP(acc0); KG_SQ();
      KG_STEP(acc1); KG_SQ();
      KG_STEP(acc2); KG_SQ();
      KG_STEP(acc3);
    }
    {
      float* w = sr + wid * 1024;
#pragma unroll
      for (int j = 0; j < 4; ++j) {
        const int e = (4 * kk + j) * 16 + rc;
        w[e] = acc0[j]; w[256 + e] = acc1[j]; w[512 + e] = acc2[j]; w[768 + e] = acc3[j];
      }
    }
    __syncthreads();
    u32* Kd = a.Kc + (size_t)d * KC_STRIDE;
    {
      const float v0 = 2.f * ((sr[tid] + sr[1024 + tid]) + (sr[2048 + tid] + sr[3072 + tid]));
      const u16 h0 = f2bf(v0);
      Kd[tid + 1] = (u32)h0 | ((u32)f2bf(v0 - bf2f(h0)) << 16);
      if (tid >= 128) {
#pragma unroll
        for (int gI = 1; gI < 4; ++gI) {
          const int e = gI * 256 + tid;
          const float vv = 2.f * ((sr[e] + sr[1024 + e]) + (sr[2048 + e] + sr[3072 + e]));
          const u16 hh = f2bf(vv);
          Kd[gI * 128 + tid + 1] = (u32)hh | ((u32)f2bf(vv - bf2f(hh)) << 16);
        }
      }
      if (wid == 0) {
        const float c0 = sp[4 * lane + 2] + sp[4 * (lane + 64) + 2];
        const float s0 = 2.f * wave_sum(c0);
        const u16 hh = f2bf(s0);
        if (lane == 0) Kd[0] = (u32)hh | ((u32)f2bf(s0 - bf2f(hh)) << 16);
      }
    }
    return;
  }
}

constexpr int CV_KLEN = 2208;
constexpr int CV_USTRIDE = 2320;
constexpr int CV_OFF_U = 2 * CV_KLEN * 2;
constexpr int CV_LDS_BYTES = CV_OFF_U + 8 * CV_USTRIDE * 2;

template <int TMIN>
__device__ __forceinline__ void conv_steps(int s0, int s1, const u16* kh, const u16* kl, const u16* ub,
                                           f32x4 (&acc)[8]) {
  bf16x8 ah = *reinterpret_cast<const bf16x8*>(kh + 32 * s0);
  bf16x8 al = *reinterpret_cast<const bf16x8*>(kl + 32 * s0);
  bf16x8 bv[8];
#pragma unroll
  for (int t = TMIN; t < 8; ++t) bv[t] = *reinterpret_cast<const bf16x8*>(ub + 32 * s0 - 256 * t);
  for (int s = s0; s < s1; ++s) {
    const bf16x8 ah2 = *reinterpret_cast<const bf16x8*>(kh + 32 * s + 32);
    const bf16x8 al2 = *reinterpret_cast<const bf16x8*>(kl + 32 * s + 32);
    bf16x8 bv2[8];
#pragma unroll
    for (int t = TMIN; t < 8; ++t) bv2[t] = *reinterpret_cast<const bf16x8*>(ub + 32 * s + 32 - 256 * t);
    __builtin_amdgcn_sched_barrier(0);
#pragma unroll
    for (int t = TMIN; t < 8; ++t) {
      acc[t] = __builtin_amdgcn_mfma_f32_16x16x32_bf16(ah, bv[t], acc[t], 0, 0, 0);
      acc[t] = __builtin_amdgcn_mfma_f32_16x16x32_bf16(al, bv[t], acc[t], 0, 0, 0);
    }
    __builtin_amdgcn_sched_barrier(0);
    ah = ah2; al = al2;
#pragma unroll
    for (int t = TMIN; t < 8; ++t) bv[t] = bv2[t];
  }
}

struct K2Args {
  const u32* Kc; const u16* Dskip; const u16* uT; u16* sT;
};

__device__ __forceinline__ void build_u_chunk(const u32 (&W)[8], u16* dst) {
#pragma unroll
  for (int rho = 0; rho < 8; ++rho) {
    u32 o[4];
#pragma unroll
    for (int k = 0; k < 4; ++k) {
      if ((rho & 1) == 0) {
        const int aI = (rho + 8 - 2 * k) >> 1;
        o[k] = (W[aI] & 0xffffu) | (W[aI - 1] & 0xffff0000u);
      } else {
        const int aI = (rho + 7 - 2 * k) >> 1;
        o[k] = (W[aI] >> 16) | (W[aI] << 16);
      }
    }
    *reinterpret_cast<u32x4*>(dst + rho * CV_USTRIDE) = (u32x4){o[0], o[1], o[2], o[3]};
  }
}

__global__ __launch_bounds__(256, 3) void k2_conv(K2Args a) {
  __shared__ __attribute__((aligned(16))) unsigned char smem[CV_LDS_BYTES];
  u16* sKh = reinterpret_cast<u16*>(smem);
  u16* sKl = sKh + CV_KLEN;
  u16* sU = reinterpret_cast<u16*>(smem + CV_OFF_U);
  const int tid = threadIdx.x, lane = tid & 63, wid = tid >> 6;
  const int d = blockIdx.x;
  const u16* ud = a.uT + (size_t)d * LT_;
  const u32* Kd = a.Kc + (size_t)d * KC_STRIDE;
  const int q = 255 - tid;
  const u32x4 w0 = *reinterpret_cast<const u32x4*>(ud + 8 * q);
  u32x4 w1 = {0, 0, 0, 0};
  if (q < 255) w1 = *reinterpret_cast<const u32x4*>(ud + 8 * q + 8);
  u32 kv[8];
  {
    const int j0 = 8 * tid;
    const int e = (j0 < 256) ? 0 : (j0 < 512) ? 1 : (j0 < 1024) ? 2 : 3;
    const int base = 128 * e;
    const int hm1 = ((1 << e) >> 1) - 1;
#pragma unroll
    for (int jj = 0; jj < 8; ++jj) {
      const int j = j0 + jj;
      const int pq = (e == 0) ? j : ((j + hm1 + ((j >> e) & 1)) >> e);
      kv[jj] = Kd[base + pq];
    }
  }
  {
    const u32 W[8] = {w0[0], w0[1], w0[2], w0[3], w1[0], w1[1], w1[2], w1[3]};
    build_u_chunk(W, sU + 8 * tid);
    if (tid == 255) {
      const u32 W2[8] = {0, 0, 0, 0, w0[0], w0[1], w0[2], w0[3]};
      build_u_chunk(W2, sU + 8 * 256);
    }
    if (tid < 8 * 19) {
      const int rho = tid / 19, m = 257 + tid % 19;
      *reinterpret_cast<u32x4*>(sU + rho * CV_USTRIDE + 8 * m) = (u32x4){0, 0, 0, 0};
    } else if (tid >= 160 && tid < 200) {
      const int t = tid - 160;
      const int m = (t % 20 < 16) ? t % 20 : 256 + t % 20;
      u16* dst = (t < 20) ? sKh : sKl;
      *reinterpret_cast<u32x4*>(dst + 8 * m) = (u32x4){0, 0, 0, 0};
    }
    u32x4 ph, pl;
#pragma unroll
    for (int k = 0; k < 4; ++k) {
      ph[k] = (kv[2 * k] & 0xffffu) | (kv[2 * k + 1] << 16);
      pl[k] = (kv[2 * k] >> 16) | (kv[2 * k + 1] & 0xffff0000u);
    }
    *reinterpret_cast<u32x4*>(sKh + 128 + 8 * tid) = ph;
    *reinterpret_cast<u32x4*>(sKl + 128 + 8 * tid) = pl;
  }
  __syncthreads();
  const int r = lane & 15, g = lane >> 4;
  const int ap = (lane & 15) >> 3, rho = lane & 7;
  f32x4 acc[8];
#pragma unroll
  for (int t = 0; t < 8; ++t) acc[t] = (f32x4){0, 0, 0, 0};
  const u16* kh = sKh + 128 + 8 * r + 8 * g;
  const u16* kl = sKl + 128 + 8 * r + 8 * g;
  const u16* ub = sU + rho * CV_USTRIDE + 8 * g - 128 * ap + 2048;
  if (wid == 0) {
    conv_steps<0>(-4, 5, kh, kl, ub, acc);
  } else if (wid == 1) {
    conv_steps<1>(5, 13, kh, kl, ub, acc);
    conv_steps<2>(13, 16, kh, kl, ub, acc);
  } else if (wid == 2) {
    conv_steps<2>(16, 21, kh, kl, ub, acc);
    conv_steps<3>(21, 29, kh, kl, ub, acc);
    conv_steps<4>(29, 30, kh, kl, ub, acc);
  } else {
    conv_steps<4>(30, 37, kh, kl, ub, acc);
    conv_steps<5>(37, 45, kh, kl, ub, acc);
    conv_steps<6>(45, 53, kh, kl, ub, acc);
    conv_steps<7>(53, 61, kh, kl, ub, acc);
  }
  __syncthreads();
  float* sy = reinterpret_cast<float*>(smem);
#pragma unroll
  for (int t = 0; t < 8; ++t) {
#pragma unroll
    for (int j = 0; j < 4; ++j) {
      const int l = 256 * t + 128 * ap + rho + 8 * (4 * g + j);
      sy[wid * L_ + l] = acc[t][j];
    }
  }
  __syncthreads();
  const float dsk = bf2f(a.Dskip[d]);
  {
    const int l0 = q * 8;
    float y[8];
#pragma unroll
    for (int hh = 0; hh < 2; ++hh) {
      f32x4 a0 = *reinterpret_cast<const f32x4*>(sy + l0 + 4 * hh);
      f32x4 a1 = *reinterpret_cast<const f32x4*>(sy + L_ + l0 + 4 * hh);
      f32x4 a2 = *reinterpret_cast<const f32x4*>(sy + 2 * L_ + l0 + 4 * hh);
      f32x4 a3 = *reinterpret_cast<const f32x4*>(sy + 3 * L_ + l0 + 4 * hh);
#pragma unroll
      for (int j = 0; j < 4; ++j) y[4 * hh + j] = (a0[j] + a1[j]) + (a2[j] + a3[j]);
    }
    u32 o[4];
#pragma unroll
    for (int k = 0; k < 4; ++k) {
      const float t0 = rbf(lo_f(w0[k]) * dsk), t1 = rbf(hi_f(w0[k]) * dsk);
      o[k] = pack2(rbf(y[2 * k]) + t0, rbf(y[2 * k + 1]) + t1);
    }
    *reinterpret_cast<u32x4*>(a.sT + (size_t)d * LT_ + l0) = (u32x4){o[0], o[1], o[2], o[3]};
  }
}

__device__ __forceinline__ void wtile_load(int tile, int tid, u16* st, const u16* wg, const u16* wu, const u16* wd) {
  if (tile < K1_W1_TILES) {
    const int kt = tile / 27, cc0 = (tile % 27) * 64;
#pragma unroll
    for (int i = 0; i < 4; ++i) {
      const int idx = tid + 256 * i;
      const int mat = idx >> 9, r = (idx >> 3) & 63, c = (idx & 7) * 8;
      const u16* src = mat ? wu : wg;
      const u32x4 v = *reinterpret_cast<const u32x4*>(src + (size_t)(kt * 64 + r) * F_ + cc0 + c);
      *reinterpret_cast<u32x4*>(st + (mat * 64 + r) * 72 + c) = v;
    }
  } else {
    const int t2 = tile - K1_W1_TILES;
    const int k0 = (t2 / 10) * 64, n0 = (t2 % 10) * 64;
#pragma unroll
    for (int i = 0; i < 2; ++i) {
      const int idx = tid + 256 * i;
      const int r = idx >> 3, c = (idx & 7) * 8;
      const u32x4 v = *reinterpret_cast<const u32x4*>(wd + (size_t)(k0 + r) * D_ + n0 + c);
      *reinterpret_cast<u32x4*>(st + r * 72 + c) = v;
    }
  }
}
__device__ __forceinline__ void wtile_store(int tile, int tid, const u16* st, u16* w1i, u16* wdf) {
  if (tile < K1_W1_TILES) {
    const int kt = tile / 27, cc0 = (tile % 27) * 64;
#pragma unroll
    for (int i = 0; i < 4; ++i) {
      const int idx = tid + 256 * i;
      const int gl = idx >> 8, rr = (idx >> 3) & 31, pc = idx & 7;
      const int ch = pc ^ (rr & 7);
      const int mat = rr >> 4, col = gl * 16 + (rr & 15);
      u32 p[4];
#pragma unroll
      for (int j = 0; j < 4; ++j)
        p[j] = (u32)st[(mat * 64 + ch * 8 + 2 * j) * 72 + col] | ((u32)st[(mat * 64 + ch * 8 + 2 * j + 1) * 72 + col] << 16);
      const int grp = (cc0 >> 4) + gl;
      u16* dst = w1i + ((size_t)(kt * 108 + grp) * 2048) + rr * 64 + pc * 8;
      *reinterpret_cast<u32x4*>(dst) = (u32x4){p[0], p[1], p[2], p[3]};
    }
  } else {
    const int t2 = tile - K1_W1_TILES;
    const int k0 = (t2 / 10) * 64, n0 = (t2 % 10) * 64;
#pragma unroll
    for (int i = 0; i < 2; ++i) {
      const int idx = tid + 256 * i;
      const int ntl = idx >> 7, ksl = (idx >> 6) & 1, ln = idx & 63;
      const int fr = ln & 15, fq = ln >> 4;
      const int kb = ksl * 32 + fq * 8, col = ntl * 16 + fr;
      u32 p[4];
#pragma unroll
      for (int j = 0; j < 4; ++j)
        p[j] = (u32)st[(kb + 2 * j) * 72 + col] | ((u32)st[(kb + 2 * j + 1) * 72 + col] << 16);
      const int nt = (n0 >> 4) + ntl, ks = (k0 >> 5) + ksl;
      u16* dst = wdf + ((size_t)(nt * 54 + ks) * 64 + ln) * 8;
      *reinterpret_cast<u32x4*>(dst) = (u32x4){p[0], p[1], p[2], p[3]};
    }
  }
}

struct LN2Args {
  const u16* sT; const u16* x; u16* h; u16* xni;
  const u16* wg; const u16* wu; const u16* wd; u16* w1i; u16* wdf;
};
constexpr int LN2_BLOCKS = L_ / 16;
constexpr int LN2_W_BLOCKS = (K1_W1_TILES + K1_WD_TILES) / 4;

__global__ __launch_bounds__(1024) void k_ln2(LN2Args a) {
  __shared__ __attribute__((aligned(16))) u16 ss[4 * 2 * 64 * 72];
  const int tid = threadIdx.x, lane = tid & 63, wid = tid >> 6;
  if (blockIdx.x >= LN2_BLOCKS) {
    const int sub = tid >> 8, t = tid & 255;
    const int tile = (blockIdx.x - LN2_BLOCKS) * 4 + sub;
    u16* st = ss + sub * (2 * 64 * 72);
    wtile_load(tile, t, st, a.wg, a.wu, a.wd);
    __syncthreads();
    wtile_store(tile, t, st, a.w1i, a.wdf);
    return;
  }
  const u16* __restrict__ sT = a.sT; const u16* __restrict__ x = a.x;
  u16* __restrict__ h = a.h; u16* __restrict__ xni = a.xni;
  const int row0 = blockIdx.x * 16;
  const int row = row0 + wid;
  u32 xv[5];
  {
    const u32* xr = reinterpret_cast<const u32*>(x + (size_t)row * D_);
#pragma unroll
    for (int i = 0; i < 5; ++i) xv[i] = xr[lane + 64 * i];
  }
  if (tid < D_) {
    const int d = tid;
    const u32x4* src = reinterpret_cast<const u32x4*>(sT + (size_t)d * LT_ + row0);
    const u32x4 a0 = src[0], a1 = src[1];
#pragma unroll
    for (int k = 0; k < 4; ++k) {
      ss[(2 * k) * 648 + d] = (u16)(a0[k] & 0xffffu);
      ss[(2 * k + 1) * 648 + d] = (u16)(a0[k] >> 16);
      ss[(8 + 2 * k) * 648 + d] = (u16)(a1[k] & 0xffffu);
      ss[(8 + 2 * k + 1) * 648 + d] = (u16)(a1[k] >> 16);
    }
  }
  __syncthreads();
  {
    float f[10], o[10];
    u32* hrow = reinterpret_cast<u32*>(h + (size_t)row * D_);
#pragma unroll
    for (int i = 0; i < 5; ++i) {
      const u32 sv = *reinterpret_cast<const u32*>(ss + wid * 648 + 2 * (lane + 64 * i));
      const u32 hv = pack2(lo_f(xv[i]) + lo_f(sv), hi_f(xv[i]) + hi_f(sv));
      hrow[lane + 64 * i] = hv;
      f[2 * i] = lo_f(hv); f[2 * i + 1] = hi_f(hv);
    }
    ln_row(f, o);
    const int mt = row >> 7, r = row & 127;
#pragma unroll
    for (int i = 0; i < 5; ++i) {
      const int c = 2 * (lane + 64 * i);
      const int kt = c >> 6, cc = c & 63;
      const int off = ((mt * 10 + kt) * 128 + r) * 64 + (((cc >> 3) ^ (r & 7)) << 3) + (cc & 7);
      *reinterpret_cast<u32*>(xni + off) = pack2(o[2 * i], o[2 * i + 1]);
    }
  }
}

__device__ __forceinline__ float gelu_tanh(float x) {
  const float c0 = -2.f * 0.7978845608028654f * 1.4426950408889634f;
  const float c1 = c0 * 0.044715f;
  const float e = __builtin_amdgcn_exp2f(x * fmaf(x * x, c1, c0));
  return x * __builtin_amdgcn_rcpf(1.f + e);
}

constexpr int G1_A_BYTES = 16384;
constexpr int G1_B_BYTES = 7 * 4096;
constexpr int G1_STAGE = G1_A_BYTES + G1_B_BYTES;
constexpr int G1_NSTG = 3;

template <int NG>
__device__ __forceinline__ void g1_ldfrag(const unsigned char* sb, int arow, int brow, int cpos,
                                          bf16x8 (&xf)[2], bf16x8 (&wf)[8]) {
#pragma unroll
  for (int m = 0; m < 2; ++m)
    xf[m] = *reinterpret_cast<const bf16x8*>(sb + (arow + m * 16) * 128 + cpos);
#pragma unroll
  for (int n = 0; n < 2 * NG; ++n)
    wf[n] = *reinterpret_cast<const bf16x8*>(sb + G1_A_BYTES + (brow + n * 16) * 128 + cpos);
}
template <int NG>
__device__ __forceinline__ void g1_mma(const bf16x8 (&xf)[2], const bf16x8 (&wf)[8], f32x4 (&acc)[2][8]) {
#pragma unroll
  for (int n = 0; n < 2 * NG; ++n)
#pragma unroll
    for (int m = 0; m < 2; ++m)
      acc[m][n] = __builtin_amdgcn_mfma_f32_16x16x32_bf16(wf[n], xf[m], acc[m][n], 0, 0, 0);
}

template <int NG>
__device__ __forceinline__ void g1_main(unsigned char* smem, const unsigned char* Ag, const unsigned char* Bg,
                                        int g0, int wid, int lane, int arow, int brow, int cpos0, int cpos1,
                                        f32x4 (&acc)[2][8]) {
#define G1_ISSUE(kt, buf)                                                                              \
  {                                                                                                    \
    unsigned char* sb_ = smem + (buf) * G1_STAGE;                                                      \
    _Pragma("unroll") for (int i = 0; i < 2; ++i) {                                                    \
      const int piece = wid + 8 * i;                                                                   \
      __builtin_amdgcn_global_load_lds(                                                                \
          (const __attribute__((address_space(1))) void*)(Ag + (size_t)(kt) * G1_A_BYTES + piece * 1024 + lane * 16), \
          (__attribute__((address_space(3))) void*)(sb_ + piece * 1024), 16, 0, 0);                    \
    }                                                                                                  \
    _Pragma("unroll") for (int i = 0; i < 4; ++i) {                                                    \
      int pb = wid + 8 * i; if (pb >= 28) pb -= 4;                                                     \
      const int gi = pb >> 2;                                                                          \
      const int grp = (g0 + gi < 108) ? g0 + gi : 107;                                                 \
      __builtin_amdgcn_global_load_lds(                                                                \
          (const __attribute__((address_space(1))) void*)(Bg + ((size_t)((kt) * 108 + grp) * 4096) + (pb & 3) * 1024 + lane * 16), \
          (__attribute__((address_space(3))) void*)(sb_ + G1_A_BYTES + pb * 1024), 16, 0, 0);          \
    }                                                                                                  \
  }
  bf16x8 xf0[2], wf0[8], xf1[2], wf1[8];
  G1_ISSUE(0, 0);
  G1_ISSUE(1, 1);
  G1_ISSUE(2, 2);
  asm volatile("s_waitcnt vmcnt(12)" ::: "memory");
  __builtin_amdgcn_s_barrier();
  asm volatile("" ::: "memory");
  g1_ldfrag<NG>(smem, arow, brow, cpos0, xf0, wf0);
#pragma unroll
  for (int kt = 0; kt < 10; ++kt) {
    const int buf = kt % 3;
    const unsigned char* sb = smem + buf * G1_STAGE;
    const int nbuf = (buf == 2) ? 0 : buf + 1;
    g1_ldfrag<NG>(sb, arow, brow, cpos1, xf1, wf1);
    g1_mma<NG>(xf0, wf0, acc);
    if (kt < 9) {
      if (kt < 8) asm volatile("s_waitcnt vmcnt(6) lgkmcnt(0)" ::: "memory");
      else asm volatile("s_waitcnt vmcnt(0) lgkmcnt(0)" ::: "memory");
      __builtin_amdgcn_s_barrier();
      asm volatile("" ::: "memory");
      if (kt + 3 < 10) G1_ISSUE(kt + 3, buf);
      g1_ldfrag<NG>(smem + nbuf * G1_STAGE, arow, brow, cpos0, xf0, wf0);
    }
    g1_mma<NG>(xf1, wf1, acc);
  }
}

__global__ __launch_bounds__(512) void k3_gemm1(const u16* __restrict__ xni, const u16* __restrict__ w1i,
                                                u16* __restrict__ hmf) {
  __shared__ __attribute__((aligned(16))) unsigned char smem[G1_NSTG * G1_STAGE];
  const int tid = threadIdx.x, lane = tid & 63;
  const int wid = __builtin_amdgcn_readfirstlane(tid >> 6);
  const int wm = wid & 3, wn = wid >> 2;
  const int fr = lane & 15, fq = lane >> 4;
  const int bid = blockIdx.x;
  const int xcd = bid & 7, idx = bid >> 3;
  const int mt = (xcd & 1) * 8 + (idx & 7);
  const int nb = (xcd >> 1) * 4 + (idx >> 3);
  const int g0 = 27 * (nb >> 2) + 7 * (nb & 3);
  const int ng = ((nb & 3) == 3) ? 6 : 7;
  const int gw0 = wn * 4;
  const int gwn = wn ? ng - 4 : 4;
  const unsigned char* Ag = reinterpret_cast<const unsigned char*>(xni) + (size_t)mt * 10 * G1_A_BYTES;
  const unsigned char* Bg = reinterpret_cast<const unsigned char*>(w1i);
  f32x4 acc[2][8];
#pragma unroll
  for (int m = 0; m < 2; ++m)
#pragma unroll
    for (int n = 0; n < 8; ++n) acc[m][n] = (f32x4){0, 0, 0, 0};
  const int swz = fr & 7;
  const int cpos0 = ((0 + fq) ^ swz) << 4, cpos1 = ((4 + fq) ^ swz) << 4;
  const int arow = wm * 32 + fr, brow = gw0 * 32 + fr;
  if (wn == 0) g1_main<4>(smem, Ag, Bg, g0, wid, lane, arow, brow, cpos0, cpos1, acc);
  else g1_main<3>(smem, Ag, Bg, g0, wid, lane, arow, brow, cpos0, cpos1, acc);
#pragma unroll
  for (int m = 0; m < 2; ++m) {
    const int mt16 = mt * 8 + wm * 2 + m;
#pragma unroll
    for (int gI = 0; gI < 4; ++gI) {
      if (gI < gwn) {
        float o[4];
#pragma unroll
        for (int j = 0; j < 4; ++j) {
          const float gv = rbf(acc[m][2 * gI][j]);
          const float uv = rbf(acc[m][2 * gI + 1][j]);
          o[j] = rbf(gelu_tanh(gv)) * uv;
        }
        const int f = 16 * (g0 + gw0 + gI) + 4 * fq;
        const int ks = f >> 5, fq2 = (f & 31) >> 3, j0 = f & 7;
        u16* dst = hmf + ((size_t)(mt16 * 54 + ks) * 64 + fr + 16 * fq2) * 8 + j0;
        *reinterpret_cast<u32x2*>(dst) = (u32x2){pack2(o[0], o[1]), pack2(o[2], o[3])};
      }
    }
  }
}

__global__ __launch_bounds__(256) void k4_gemm2(const u16* __restrict__ hmf, const u16* __restrict__ wdf,
                                                const u16* __restrict__ h, u16* __restrict__ out) {
  __shared__ __attribute__((aligned(16))) float sred[4 * 64 * 84];
  const int tid = threadIdx.x, lane = tid & 63;
  const int wk = __builtin_amdgcn_readfirstlane(tid >> 6);
  const int fr = lane & 15, fq = lane >> 4;
  const int bid = blockIdx.x;
  const int xcd = bid & 7, idx = bid >> 3;
  const int mt = (xcd >> 1) * 8 + (idx & 7);
  const int nb = (xcd & 1) * 4 + (idx >> 3);
  const int ks0 = (wk < 2) ? wk * 14 : 28 + (wk - 2) * 13;
  const int nks = (wk < 2) ? 14 : 13;
  const bf16x8* Ap = reinterpret_cast<const bf16x8*>(hmf) + (size_t)(mt * 4) * 54 * 64 + lane;
  const bf16x8* Bp = reinterpret_cast<const bf16x8*>(wdf) + (size_t)(nb * 5) * 54 * 64 + lane;
  const int rl = tid >> 2, c0 = (tid & 3) * 20;
  const int row = mt * 64 + rl;
  u32x2 hres[5];
  {
    const u16* hr = h + (size_t)row * D_ + nb * 80 + c0;
#pragma unroll
    for (int i = 0; i < 5; ++i) hres[i] = *reinterpret_cast<const u32x2*>(hr + 4 * i);
  }
  f32x4 acc[4][5];
#pragma unroll
  for (int m = 0; m < 4; ++m)
#pragma unroll
    for (int n = 0; n < 5; ++n) acc[m][n] = (f32x4){0, 0, 0, 0};
  bf16x8 xa[4][4], wb[4][5];
#define G2_LOAD(slot, kidx)                                                              \
  {                                                                                      \
    const int kq_ = ks0 + (((kidx) < nks) ? (kidx) : nks - 1);                           \
    _Pragma("unroll") for (int m = 0; m < 4; ++m) xa[slot][m] = Ap[(m * 54 + kq_) * 64];   \
    _Pragma("unroll") for (int n = 0; n < 5; ++n) wb[slot][n] = Bp[(n * 54 + kq_) * 64];   \
  }
#define G2_MMA(slot)                                                                     \
  {                                                                                      \
    _Pragma("unroll") for (int n = 0; n < 5; ++n)                                        \
    _Pragma("unroll") for (int m = 0; m < 4; ++m)                                        \
      acc[m][n] = __builtin_amdgcn_mfma_f32_16x16x32_bf16(wb[slot][n], xa[slot][m], acc[m][n], 0, 0, 0); \
  }
  G2_LOAD(0, 0);
  G2_LOAD(1, 1);
  G2_LOAD(2, 2);
  __builtin_amdgcn_sched_barrier(0);
#pragma unroll
  for (int i = 0; i < 13; ++i) {
    if (i + 3 < 14) G2_LOAD((i + 3) & 3, i + 3);
    __builtin_amdgcn_sched_barrier(0);
    G2_MMA(i & 3);
    __builtin_amdgcn_sched_barrier(0);
  }
  if (nks == 14) G2_MMA(13 & 3);
  float* sw = sred + wk * 64 * 84;
#pragma unroll
  for (int m = 0; m < 4; ++m)
#pragma unroll
    for (int n = 0; n < 5; ++n)
      *reinterpret_cast<f32x4*>(sw + (m * 16 + fr) * 84 + n * 16 + 4 * fq) = acc[m][n];
  __syncthreads();
  {
    u16* orow = out + (size_t)row * D_ + nb * 80 + c0;
#pragma unroll
    for (int i = 0; i < 5; ++i) {
      const float* p = sred + rl * 84 + c0 + 4 * i;
      const f32x4 a0 = *reinterpret_cast<const f32x4*>(p);
      const f32x4 a1 = *reinterpret_cast<const f32x4*>(p + 64 * 84);
      const f32x4 a2 = *reinterpret_cast<const f32x4*>(p + 2 * 64 * 84);
      const f32x4 a3 = *reinterpret_cast<const f32x4*>(p + 3 * 64 * 84);
      float o[4];
#pragma unroll
      for (int j = 0; j < 4; ++j) o[j] = rbf((a0[j] + a1[j]) + (a2[j] + a3[j]));
      const u32 r0 = pack2(lo_f(hres[i][0]) + o[0], hi_f(hres[i][0]) + o[1]);
      const u32 r1 = pack2(lo_f(hres[i][1]) + o[2], hi_f(hres[i][1]) + o[3]);
      *reinterpret_cast<u32x2*>(orow + 4 * i) = (u32x2){r0, r1};
    }
  }
}

extern "C" void kernel_launch(void* const* d_in, const int* in_sizes, int n_in,
                              void* d_out, int out_size, void* d_ws, size_t ws_size,
                              hipStream_t stream) {
  const u16* x = (const u16*)d_in[0];
  const u16* logA = (const u16*)d_in[1];
  const u16* Aim = (const u16*)d_in[2];
  const u16* Cr = (const u16*)d_in[3];
  const u16* Ci = (const u16*)d_in[4];
  const u16* logdt = (const u16*)d_in[5];
  const u16* Dskip = (const u16*)d_in[6];
  const u16* wg = (const u16*)d_in[8];
  const u16* wu = (const u16*)d_in[9];
  const u16* wd = (const u16*)d_in[10];
  u16* out = (u16*)d_out;

  char* ws = (char*)d_ws;
  size_t off = 0;
  auto alloc = [&](size_t bytes) { char* p = ws + off; off += (bytes + 255) & ~(size_t)255; return p; };
  u16* uT = (u16*)alloc((size_t)D_ * LT_ * 2);
  u16* sT = (u16*)alloc((size_t)D_ * LT_ * 2);
  u16* h = (u16*)alloc((size_t)L_ * D_ * 2);
  u16* xni = (u16*)alloc((size_t)L_ * D_ * 2);
  u16* hmf = (u16*)alloc((size_t)L_ * F_ * 2);
  u16* w1i = (u16*)alloc((size_t)2 * F_ * D_ * 2);
  u16* wdf = (u16*)alloc((size_t)D_ * F_ * 2);
  u32* Kc = (u32*)alloc((size_t)D_ * KC_STRIDE * 4);

  K1Args a;
  a.x = x; a.wg = wg; a.wu = wu; a.wd = wd;
  a.logA = logA; a.Aim = Aim; a.Cr = Cr; a.Ci = Ci; a.logdt = logdt;
  a.uT = uT; a.w1i = w1i; a.wdf = wdf; a.Kc = Kc;
  k1_kernel<<<K1_BLOCKS, 256, 0, stream>>>(a);
  K2Args a2;
  a2.Kc = Kc; a2.Dskip = Dskip; a2.uT = uT; a2.sT = sT;
  k2_conv<<<D_, 256, 0, stream>>>(a2);
  LN2Args a3;
  a3.sT = sT; a3.x = x; a3.h = h; a3.xni = xni; a3.wg = wg; a3.wu = wu; a3.wd = wd; a3.w1i = w1i; a3.wdf = wdf;
  k_ln2<<<LN2_BLOCKS + LN2_W_BLOCKS, 1024, 0, stream>>>(a3);
  k3_gemm1<<<256, 512, 0, stream>>>(xni, w1i, hmf);
  k4_gemm2<<<256, 256, 0, stream>>>(hmf, wdf, h, out);
}
```

```cpp
#include <hip/hip_runtime.h>
#include <hip/hip_bf16.h>
#include <stdint.h>

constexpr int L_ = 2048;
constexpr int D_ = 640;
constexpr int N_ = 128;
constexpr int F_ = 1728;
constexpr int LT_ = 2112;
constexpr int KC_STRIDE = 648;

using bf16x8 = __attribute__((ext_vector_type(8))) short;
using f32x4 = __attribute__((ext_vector_type(4))) float;
using u32x4 = __attribute__((ext_vector_type(4))) unsigned int;
using u32x2 = __attribute__((ext_vector_type(2))) unsigned int;
typedef unsigned short u16;
typedef unsigned int u32;

__device__ __forceinline__ float bf2f(u16 v) { return __uint_as_float(((u32)v) << 16); }
__device__ __forceinline__ u16 f2bf(float f) {
  __hip_bfloat16 b = __float2bfloat16(f);
  return *reinterpret_cast<u16*>(&b);
}
__device__ __forceinline__ float rbf(float f) { return bf2f(f2bf(f)); }
__device__ __forceinline__ float lo_f(u32 v) { return __uint_as_float(v << 16); }
__device__ __forceinline__ float hi_f(u32 v) { return __uint_as_float(v & 0xffff0000u); }
__device__ __forceinline__ u32 pack2(float a, float b) { return (u32)f2bf(a) | ((u32)f2bf(b) << 16); }

template <int CTRL, int RMASK>
__device__ __forceinline__ float dpp_add(float v) {
  const int t = __builtin_amdgcn_update_dpp(0, __float_as_int(v), CTRL, RMASK, 0xf, false);
  return v + __int_as_float(t);
}
__device__ __forceinline__ float wave_sum(float v) {
  v = dpp_add<0x111, 0xf>(v);
  v = dpp_add<0x112, 0xf>(v);
  v = dpp_add<0x114, 0xf>(v);
  v = dpp_add<0x118, 0xf>(v);
  v = dpp_add<0x142, 0xa>(v);
  v = dpp_add<0x143, 0xc>(v);
  return __int_as_float(__builtin_amdgcn_readlane(__float_as_int(v), 63));
}

__device__ __forceinline__ void cexpp(float re2, float imr, float p, float& outr, float& outi) {
  const float mag = __builtin_amdgcn_exp2f(re2 * p);
  const float t = __builtin_amdgcn_fractf(imr * p);
  outr = mag * __builtin_amdgcn_cosf(t);
  outi = mag * __builtin_amdgcn_sinf(t);
}

__device__ __forceinline__ void ln_row(const float (&f)[10], float (&o)[10]) {
  float s = 0.f;
#pragma unroll
  for (int i = 0; i < 10; ++i) s += f[i];
  const float mean = wave_sum(s) * (1.f / D_);
  float q = 0.f;
#pragma unroll
  for (int i = 0; i < 10; ++i) { const float t = f[i] - mean; q += t * t; }
  const float var = wave_sum(q) * (1.f / D_);
  const float mb = rbf(mean);
  const float sd = rbf(sqrtf(rbf(rbf(var) + 1e-5f)));
  const float rs = 1.f / sd;
#pragma unroll
  for (int i = 0; i < 10; ++i) {
    const float t = rbf(f[i] - mb);
    const float q0 = t * rs;
    o[i] = fmaf(fmaf(-q0, sd, t), rs, q0);
  }
}

constexpr int K1_KG_BLOCKS = 2 * D_;
constexpr int K1_LN_BLOCKS = L_ / 16;
constexpr int K1_W1_TILES = (D_ / 64) * (F_ / 64);
constexpr int K1_WD_TILES = (F_ / 64) * (D_ / 64);
constexpr int K1_W_BLOCKS = (K1_W1_TILES + K1_WD_TILES);
constexpr int K1_BLOCKS = K1_KG_BLOCKS + K1_LN_BLOCKS + K1_W_BLOCKS;

__device__ __forceinline__ void wtile_load(int tile, int tid, u16* st, const u16* wg, const u16* wu, const u16* wd) {
  if (tile < K1_W1_TILES) {
    const int kt = tile / 27, cc0 = (tile % 27) * 64;
#pragma unroll
    for (int i = 0; i < 4; ++i) {
      const int idx = tid + 256 * i;
      const int mat = idx >> 9, r = (idx >> 3) & 63, c = (idx & 7) * 8;
      const u16* src = mat ? wu : wg;
      const u32x4 v = *reinterpret_cast<const u32x4*>(src + (size_t)(kt * 64 + r) * F_ + cc0 + c);
      *reinterpret_cast<u32x4*>(st + (mat * 64 + r) * 72 + c) = v;
    }
  } else {
    const int t2 = tile - K1_W1_TILES;
    const int k0 = (t2 / 10) * 64, n0 = (t2 % 10) * 64;
#pragma unroll
    for (int i = 0; i < 2; ++i) {
      const int idx = tid + 256 * i;
      const int r = idx >> 3, c = (idx & 7) * 8;
      const u32x4 v = *reinterpret_cast<const u32x4*>(wd + (size_t)(k0 + r) * D_ + n0 + c);
      *reinterpret_cast<u32x4*>(st + r * 72 + c) = v;
    }
  }
}
__device__ __forceinline__ void wtile_store(int tile, int tid, const u16* st, u16* w1i, u16* wdf) {
  if (tile < K1_W1_TILES) {
    const int kt = tile / 27, cc0 = (tile % 27) * 64;
#pragma unroll
    for (int i = 0; i < 4; ++i) {
      const int idx = tid + 256 * i;
      const int gl = idx >> 8, rr = (idx >> 3) & 31, pc = idx & 7;
      const int ch = pc ^ (rr & 7);
      const int mat = rr >> 4, col = gl * 16 + (rr & 15);
      u32 p[4];
#pragma unroll
      for (int j = 0; j < 4; ++j)
        p[j] = (u32)st[(mat * 64 + ch * 8 + 2 * j) * 72 + col] | ((u32)st[(mat * 64 + ch * 8 + 2 * j + 1) * 72 + col] << 16);
      const int grp = (cc0 >> 4) + gl;
      u16* dst = w1i + ((size_t)(kt * 108 + grp) * 2048) + rr * 64 + pc * 8;
      *reinterpret_cast<u32x4*>(dst) = (u32x4){p[0], p[1], p[2], p[3]};
    }
  } else {
    const int t2 = tile - K1_W1_TILES;
    const int k0 = (t2 / 10) * 64, n0 = (t2 % 10) * 64;
#pragma unroll
    for (int i = 0; i < 2; ++i) {
      const int idx = tid + 256 * i;
      const int ntl = idx >> 7, ksl = (idx >> 6) & 1, ln = idx & 63;
      const int fr = ln & 15, fq = ln >> 4;
      const int kb = ksl * 32 + fq * 8, col = ntl * 16 + fr;
      u32 p[4];
#pragma unroll
      for (int j = 0; j < 4; ++j)
        p[j] = (u32)st[(kb + 2 * j) * 72 + col] | ((u32)st[(kb + 2 * j + 1) * 72 + col] << 16);
      const int nt = (n0 >> 4) + ntl, ks = (k0 >> 5) + ksl;
      u16* dst = wdf + ((size_t)(nt * 54 + ks) * 64 + ln) * 8;
      *reinterpret_cast<u32x4*>(dst) = (u32x4){p[0], p[1], p[2], p[3]};
    }
  }
}

struct K1Args {
  const u16* x; const u16* wg; const u16* wu; const u16* wd;
  const u16* logA; const u16* Aim; const u16* Cr; const u16* Ci; const u16* logdt;
  u16* uT; u16* w1i; u16* wdf; float* Kc;
};

__global__ __launch_bounds__(256) void k1_kernel(K1Args a) {
  __shared__ __attribute__((aligned(16))) unsigned char smem_raw[16 * 648 * 2];
  const int tid = threadIdx.x, lane = tid & 63, wid = tid >> 6;
  int b = blockIdx.x;
  if (b < K1_LN_BLOCKS) {
    u16* su = reinterpret_cast<u16*>(smem_raw);
    const int row0 = b * 16;
    u32 v[4][5];
#pragma unroll
    for (int rr = 0; rr < 4; ++rr) {
      const u32* xr = reinterpret_cast<const u32*>(a.x + (size_t)(row0 + wid * 4 + rr) * D_);
#pragma unroll
      for (int i = 0; i < 5; ++i) v[rr][i] = xr[lane + 64 * i];
    }
#pragma unroll
    for (int rr = 0; rr < 4; ++rr) {
      float f[10], o[10];
#pragma unroll
      for (int i = 0; i < 5; ++i) { f[2 * i] = lo_f(v[rr][i]); f[2 * i + 1] = hi_f(v[rr][i]); }
      ln_row(f, o);
#pragma unroll
      for (int i = 0; i < 5; ++i)
        *reinterpret_cast<u32*>(su + (wid * 4 + rr) * 648 + 2 * (lane + 64 * i)) = pack2(o[2 * i], o[2 * i + 1]);
    }
    __syncthreads();
#pragma unroll
    for (int it = 0; it < 3; ++it) {
      const int d = tid + 256 * it;
      if (d < D_) {
        u32 pu[8];
#pragma unroll
        for (int r = 0; r < 8; ++r)
          pu[r] = (u32)su[(2 * r) * 648 + d] | ((u32)su[(2 * r + 1) * 648 + d] << 16);
        u32x4* du = reinterpret_cast<u32x4*>(a.uT + (size_t)d * LT_ + row0);
        du[0] = (u32x4){pu[0], pu[1], pu[2], pu[3]};
        du[1] = (u32x4){pu[4], pu[5], pu[6], pu[7]};
      }
    }
    return;
  }
  b -= K1_LN_BLOCKS;
  if (b < K1_KG_BLOCKS) {
    const int d = b >> 1, half = b & 1;
    float* sp = reinterpret_cast<float*>(smem_raw);
    float* sr = sp + 512;
    const float dt = rbf(__expf(bf2f(a.logdt[d])));
    if (tid < 64) {
      const int n = half * 64 + tid;
      const float la = bf2f(a.logA[d * N_ + n]);
      f32x4 pv;
      pv[0] = (-rbf(expf(la)) * dt) * 1.4426950408889634f;
      pv[1] = (bf2f(a.Aim[d * N_ + n]) * dt) * 0.15915494309189535f;
      pv[2] = bf2f(a.Cr[d * N_ + n]) * dt;
      pv[3] = bf2f(a.Ci[d * N_ + n]) * dt;
      *reinterpret_cast<f32x4*>(sp + 4 * tid) = pv;
    }
    __syncthreads();
    const int rc = lane & 15;
    const int kk = lane >> 4;
    f32x4 acc0 = {0, 0, 0, 0}, acc1 = {0, 0, 0, 0}, acc2 = {0, 0, 0, 0}, acc3 = {0, 0, 0, 0};
    const float pA = (float)(16 * rc);
    const float pB = (float)(rc + 1);
#pragma unroll 2
    for (int i = 0; i < 4; ++i) {
      const int n = wid * 16 + 4 * i + kk;
      const f32x4 pv = *reinterpret_cast<const f32x4*>(sp + 4 * n);
      const float cr = pv[2], ci = pv[3];
      float Ar, Ai, Br, Bi;
      cexpp(pv[0], pv[1], pA, Ar, Ai);
      cexpp(pv[0], pv[1], pB, Br, Bi);
#define KG_STEP(ACC)                                                              \
      {                                                                             \
        const float tr = Ar * cr - Ai * ci;                                         \
        const float ti = -(Ar * ci + Ai * cr);                                      \
        ACC = __builtin_amdgcn_mfma_f32_16x16x4f32(tr, Br, ACC, 0, 0, 0);           \
        ACC = __builtin_amdgcn_mfma_f32_16x16x4f32(ti, Bi, ACC, 0, 0, 0);           \
      }
#define KG_SQ()                                                                   \
      {                                                                             \
        const float t0 = Ar * Ar - Ai * Ai; Ai = 2.f * Ar * Ai; Ar = t0;            \
        const float t1 = Br * Br - Bi * Bi; Bi = 2.f * Br * Bi; Br = t1;            \
      }
      KG_STEP(acc0); KG_SQ();
      KG_STEP(acc1); KG_SQ();
      KG_STEP(acc2); KG_SQ();
      KG_STEP(acc3);
    }
    {
      float* w = sr + wid * 1024;
#pragma unroll
      for (int j = 0; j < 4; ++j) {
        const int e = (4 * kk + j) * 16 + rc;
        w[e] = acc0[j]; w[256 + e] = acc1[j]; w[512 + e] = acc2[j]; w[768 + e] = acc3[j];
      }
    }
    __syncthreads();
    float* Kd = a.Kc + (size_t)(d * 2 + half) * KC_STRIDE;
    {
      Kd[tid + 1] = 2.f * ((sr[tid] + sr[1024 + tid]) + (sr[2048 + tid] + sr[3072 + tid]));
      if (tid >= 128) {
#pragma unroll
        for (int gI = 1; gI < 4; ++gI) {
          const int e = gI * 256 + tid;
          Kd[gI * 128 + tid + 1] = 2.f * ((sr[e] + sr[1024 + e]) + (sr[2048 + e] + sr[3072 + e]));
        }
      }
      if (wid == 0) {
        const float s0 = 2.f * wave_sum(sp[4 * lane + 2]);
        if (lane == 0) Kd[0] = s0;
      }
    }
    return;
  }
  b -= K1_KG_BLOCKS;
  {
    u16* st = reinterpret_cast<u16*>(smem_raw);
    wtile_load(b, tid, st, a.wg, a.wu, a.wd);
    __syncthreads();
    wtile_store(b, tid, st, a.w1i, a.wdf);
  }
}

constexpr int CV_KLEN = 2208;
constexpr int CV_USTRIDE = 2320;
constexpr int CV_OFF_U = 2 * CV_KLEN * 2;
constexpr int CV_LDS_BYTES = CV_OFF_U + 8 * CV_USTRIDE * 2;

template <int TMIN>
__device__ __forceinline__ void conv_steps(int s0, int s1, const u16* kh, const u16* kl, const u16* ub,
                                           f32x4 (&acc)[8]) {
  bf16x8 ah = *reinterpret_cast<const bf16x8*>(kh + 32 * s0);
  bf16x8 al = *reinterpret_cast<const bf16x8*>(kl + 32 * s0);
  bf16x8 bv[8];
#pragma unroll
  for (int t = TMIN; t < 8; ++t) bv[t] = *reinterpret_cast<const bf16x8*>(ub + 32 * s0 - 256 * t);
  for (int s = s0; s < s1; ++s) {
    const bf16x8 ah2 = *reinterpret_cast<const bf16x8*>(kh + 32 * s + 32);
    const bf16x8 al2 = *reinterpret_cast<const bf16x8*>(kl + 32 * s + 32);
    bf16x8 bv2[8];
#pragma unroll
    for (int t = TMIN; t < 8; ++t) bv2[t] = *reinterpret_cast<const bf16x8*>(ub + 32 * s + 32 - 256 * t);
    __builtin_amdgcn_sched_barrier(0);
#pragma unroll
    for (int t = TMIN; t < 8; ++t) {
      acc[t] = __builtin_amdgcn_mfma_f32_16x16x32_bf16(ah, bv[t], acc[t], 0, 0, 0);
      acc[t] = __builtin_amdgcn_mfma_f32_16x16x32_bf16(al, bv[t], acc[t], 0, 0, 0);
    }
    __builtin_amdgcn_sched_barrier(0);
    ah = ah2; al = al2;
#pragma unroll
    for (int t = TMIN; t < 8; ++t) bv[t] = bv2[t];
  }
}

struct K2Args {
  const float* Kc; const u16* Dskip; const u16* uT; u16* sT;
};

__device__ __forceinline__ void build_u_chunk(const u32 (&W)[8], u16* dst) {
#pragma unroll
  for (int rho = 0; rho < 8; ++rho) {
    u32 o[4];
#pragma unroll
    for (int k = 0; k < 4; ++k) {
      if ((rho & 1) == 0) {
        const int aI = (rho + 8 - 2 * k) >> 1;
        o[k] = (W[aI] & 0xffffu) | (W[aI - 1] & 0xffff0000u);
      } else {
        const int aI = (rho + 7 - 2 * k) >> 1;
        o[k] = (W[aI] >> 16) | (W[aI] << 16);
      }
    }
    *reinterpret_cast<u32x4*>(dst + rho * CV_USTRIDE) = (u32x4){o[0], o[1], o[2], o[3]};
  }
}

__global__ __launch_bounds__(256, 3) void k2_conv(K2Args a) {
  __shared__ __attribute__((aligned(16))) unsigned char smem[CV_LDS_BYTES];
  u16* sKh = reinterpret_cast<u16*>(smem);
  u16* sKl = sKh + CV_KLEN;
  u16* sU = reinterpret_cast<u16*>(smem + CV_OFF_U);
  const int tid = threadIdx.x, lane = tid & 63, wid = tid >> 6;
  const int d = blockIdx.x;
  const u16* ud = a.uT + (size_t)d * LT_;
  const float* Kd = a.Kc + (size_t)(2 * d) * KC_STRIDE;
  const int q = 255 - tid;
  const u32x4 w0 = *reinterpret_cast<const u32x4*>(ud + 8 * q);
  u32x4 w1 = {0, 0, 0, 0};
  if (q < 255) w1 = *reinterpret_cast<const u32x4*>(ud + 8 * q + 8);
  float kv[8];
  {
    const int j0 = 8 * tid;
    const int e = (j0 < 256) ? 0 : (j0 < 512) ? 1 : (j0 < 1024) ? 2 : 3;
    const int base = 128 * e;
    const int hm1 = ((1 << e) >> 1) - 1;
#pragma unroll
    for (int jj = 0; jj < 8; ++jj) {
      const int j = j0 + jj;
      const int pq = (e == 0) ? j : ((j + hm1 + ((j >> e) & 1)) >> e);
      kv[jj] = Kd[base + pq] + Kd[KC_STRIDE + base + pq];
    }
  }
  {
    const u32 W[8] = {w0[0], w0[1], w0[2], w0[3], w1[0], w1[1], w1[2], w1[3]};
    build_u_chunk(W, sU + 8 * tid);
    if (tid == 255) {
      const u32 W2[8] = {0, 0, 0, 0, w0[0], w0[1], w0[2], w0[3]};
      build_u_chunk(W2, sU + 8 * 256);
    }
    if (tid < 8 * 19) {
      const int rho = tid / 19, m = 257 + tid % 19;
      *reinterpret_cast<u32x4*>(sU + rho * CV_USTRIDE + 8 * m) = (u32x4){0, 0, 0, 0};
    } else if (tid >= 160 && tid < 200) {
      const int t = tid - 160;
      const int m = (t % 20 < 16) ? t % 20 : 256 + t % 20;
      u16* dst = (t < 20) ? sKh : sKl;
      *reinterpret_cast<u32x4*>(dst + 8 * m) = (u32x4){0, 0, 0, 0};
    }
    u32x4 ph, pl;
#pragma unroll
    for (int k = 0; k < 4; ++k) {
      const u32 hh = pack2(kv[2 * k], kv[2 * k + 1]);
      ph[k] = hh;
      pl[k] = pack2(kv[2 * k] - lo_f(hh), kv[2 * k + 1] - hi_f(hh));
    }
    *reinterpret_cast<u32x4*>(sKh + 128 + 8 * tid) = ph;
    *reinterpret_cast<u32x4*>(sKl + 128 + 8 * tid) = pl;
  }
  __syncthreads();
  const int r = lane & 15, g = lane >> 4;
  const int ap = (lane & 15) >> 3, rho = lane & 7;
  f32x4 acc[8];
#pragma unroll
  for (int t = 0; t < 8; ++t) acc[t] = (f32x4){0, 0, 0, 0};
  const u16* kh = sKh + 128 + 8 * r + 8 * g;
  const u16* kl = sKl + 128 + 8 * r + 8 * g;
  const u16* ub = sU + rho * CV_USTRIDE + 8 * g - 128 * ap + 2048;
  if (wid == 0) {
    conv_steps<0>(-4, 5, kh, kl, ub, acc);
  } else if (wid == 1) {
    conv_steps<1>(5, 13, kh, kl, ub, acc);
    conv_steps<2>(13, 16, kh, kl, ub, acc);
  } else if (wid == 2) {
    conv_steps<2>(16, 21, kh, kl, ub, acc);
    conv_steps<3>(21, 29, kh, kl, ub, acc);
    conv_steps<4>(29, 30, kh, kl, ub, acc);
  } else {
    conv_steps<4>(30, 37, kh, kl, ub, acc);
    conv_steps<5>(37, 45, kh, kl, ub, acc);
    conv_steps<6>(45, 53, kh, kl, ub, acc);
    conv_steps<7>(53, 61, kh, kl, ub, acc);
  }
  __syncthreads();
  float* sy = reinterpret_cast<float*>(smem);
#pragma unroll
  for (int t = 0; t < 8; ++t) {
#pragma unroll
    for (int j = 0; j < 4; ++j) {
      const int l = 256 * t + 128 * ap + rho + 8 * (4 * g + j);
      sy[wid * L_ + l] = acc[t][j];
    }
  }
  __syncthreads();
  const float dsk = bf2f(a.Dskip[d]);
  {
    const int l0 = q * 8;
    float y[8];
#pragma unroll
    for (int hh = 0; hh < 2; ++hh) {
      f32x4 a0 = *reinterpret_cast<const f32x4*>(sy + l0 + 4 * hh);
      f32x4 a1 = *reinterpret_cast<const f32x4*>(sy + L_ + l0 + 4 * hh);
      f32x4 a2 = *reinterpret_cast<const f32x4*>(sy + 2 * L_ + l0 + 4 * hh);
      f32x4 a3 = *reinterpret_cast<const f32x4*>(sy + 3 * L_ + l0 + 4 * hh);
#pragma unroll
      for (int j = 0; j < 4; ++j) y[4 * hh + j] = (a0[j] + a1[j]) + (a2[j] + a3[j]);
    }
    u32 o[4];
#pragma unroll
    for (int k = 0; k < 4; ++k) {
      const float t0 = rbf(lo_f(w0[k]) * dsk), t1 = rbf(hi_f(w0[k]) * dsk);
      o[k] = pack2(rbf(y[2 * k]) + t0, rbf(y[2 * k + 1]) + t1);
    }
    *reinterpret_cast<u32x4*>(a.sT + (size_t)d * LT_ + l0) = (u32x4){o[0], o[1], o[2], o[3]};
  }
}

struct LN2Args {
  const u16* sT; const u16* x; u16* h; u16* xni;
  const u16* wg; const u16* wu; const u16* wd; u16* w1i; u16* wdf;
};
constexpr int LN2_BLOCKS = L_ / 16;
constexpr int LN2_W_BLOCKS = (K1_W1_TILES + K1_WD_TILES) / 4;

__global__ __launch_bounds__(1024) void k_ln2(LN2Args a) {
  __shared__ __attribute__((aligned(16))) u16 ss[16 * 648];
  const int tid = threadIdx.x, lane = tid & 63, wid = tid >> 6;
  const u16* __restrict__ sT = a.sT; const u16* __restrict__ x = a.x;
  u16* __restrict__ h = a.h; u16* __restrict__ xni = a.xni;
  const int row0 = blockIdx.x * 16;
  const int row = row0 + wid;
  u32 xv[5];
  {
    const u32* xr = reinterpret_cast<const u32*>(x + (size_t)row * D_);
#pragma unroll
    for (int i = 0; i < 5; ++i) xv[i] = xr[lane + 64 * i];
  }
  if (tid < D_) {
    const int d = tid;
    const u32x4* src = reinterpret_cast<const u32x4*>(sT + (size_t)d * LT_ + row0);
    const u32x4 a0 = src[0], a1 = src[1];
#pragma unroll
    for (int k = 0; k < 4; ++k) {
      ss[(2 * k) * 648 + d] = (u16)(a0[k] & 0xffffu);
      ss[(2 * k + 1) * 648 + d] = (u16)(a0[k] >> 16);
      ss[(8 + 2 * k) * 648 + d] = (u16)(a1[k] & 0xffffu);
      ss[(8 + 2 * k + 1) * 648 + d] = (u16)(a1[k] >> 16);
    }
  }
  __syncthreads();
  {
    float f[10], o[10];
    u32* hrow = reinterpret_cast<u32*>(h + (size_t)row * D_);
#pragma unroll
    for (int i = 0; i < 5; ++i) {
      const u32 sv = *reinterpret_cast<const u32*>(ss + wid * 648 + 2 * (lane + 64 * i));
      const u32 hv = pack2(lo_f(xv[i]) + lo_f(sv), hi_f(xv[i]) + hi_f(sv));
      hrow[lane + 64 * i] = hv;
      f[2 * i] = lo_f(hv); f[2 * i + 1] = hi_f(hv);
    }
    ln_row(f, o);
    const int mt = row >> 7, r = row & 127;
#pragma unroll
    for (int i = 0; i < 5; ++i) {
      const int c = 2 * (lane + 64 * i);
      const int kt = c >> 6, cc = c & 63;
      const int off = ((mt * 10 + kt) * 128 + r) * 64 + (((cc >> 3) ^ (r & 7)) << 3) + (cc & 7);
      *reinterpret_cast<u32*>(xni + off) = pack2(o[2 * i], o[2 * i + 1]);
    }
  }
}

__device__ __forceinline__ float gelu_tanh(float x) {
  const float c0 = -2.f * 0.7978845608028654f * 1.4426950408889634f;
  const float c1 = c0 * 0.044715f;
  const float e = __builtin_amdgcn_exp2f(x * fmaf(x * x, c1, c0));
  return x * __builtin_amdgcn_rcpf(1.f + e);
}

constexpr int G1_A_BYTES = 16384;
constexpr int G1_B_BYTES = 7 * 4096;
constexpr int G1_STAGE = G1_A_BYTES + G1_B_BYTES;
constexpr int G1_NSTG = 3;

template <int NG>
__device__ __forceinline__ void g1_ldfrag(const unsigned char* sb, int arow, int brow, int cpos,
                                          bf16x8 (&xf)[2], bf16x8 (&wf)[8]) {
#pragma unroll
  for (int m = 0; m < 2; ++m)
    xf[m] = *reinterpret_cast<const bf16x8*>(sb + (arow + m * 16) * 128 + cpos);
#pragma unroll
  for (int n = 0; n < 2 * NG; ++n)
    wf[n] = *reinterpret_cast<const bf16x8*>(sb + G1_A_BYTES + (brow + n * 16) * 128 + cpos);
}
template <int NG>
__device__ __forceinline__ void g1_mma(const bf16x8 (&xf)[2], const bf16x8 (&wf)[8], f32x4 (&acc)[2][8]) {
#pragma unroll
  for (int n = 0; n < 2 * NG; ++n)
#pragma unroll
    for (int m = 0; m < 2; ++m)
      acc[m][n] = __builtin_amdgcn_mfma_f32_16x16x32_bf16(wf[n], xf[m], acc[m][n], 0, 0, 0);
}

template <int NG>
__device__ __forceinline__ void g1_main(unsigned char* smem, const unsigned char* Ag, const unsigned char* Bg,
                                        int g0, int wid, int lane, int arow, int brow, int cpos0, int cpos1,
                                        f32x4 (&acc)[2][8]) {
#define G1_ISSUE(kt, buf)                                                                              \
  {                                                                                                    \
    unsigned char* sb_ = smem + (buf) * G1_STAGE;                                                      \
    _Pragma("unroll") for (int i = 0; i < 2; ++i) {                                                    \
      const int piece = wid + 8 * i;                                                                   \
      __builtin_amdgcn_global_load_lds(                                                                \
          (const __attribute__((address_space(1))) void*)(Ag + (size_t)(kt) * G1_A_BYTES + piece * 1024 + lane * 16), \
          (__attribute__((address_space(3))) void*)(sb_ + piece * 1024), 16, 0, 0);                    \
    }                                                                                                  \
    _Pragma("unroll") for (int i = 0; i < 4; ++i) {                                                    \
      int pb = wid + 8 * i; if (pb >= 28) pb -= 4;                                                     \
      const int gi = pb >> 2;                                                                          \
      const int grp = (g0 + gi < 108) ? g0 + gi : 107;                                                 \
      __builtin_amdgcn_global_load_lds(                                                                \
          (const __attribute__((address_space(1))) void*)(Bg + ((size_t)((kt) * 108 + grp) * 4096) + (pb & 3) * 1024 + lane * 16), \
          (__attribute__((address_space(3))) void*)(sb_ + G1_A_BYTES + pb * 1024), 16, 0, 0);          \
    }                                                                                                  \
  }
  bf16x8 xf0[2], wf0[8], xf1[2], wf1[8];
  G1_ISSUE(0, 0);
  G1_ISSUE(1, 1);
  G1_ISSUE(2, 2);
  asm volatile("s_waitcnt vmcnt(12)" ::: "memory");
  __builtin_amdgcn_s_barrier();
  asm volatile("" ::: "memory");
  g1_ldfrag<NG>(smem, arow, brow, cpos0, xf0, wf0);
#pragma unroll
  for (int kt = 0; kt < 10; ++kt) {
    const int buf = kt % 3;
    const unsigned char* sb = smem + buf * G1_STAGE;
    const int nbuf = (buf == 2) ? 0 : buf + 1;
    g1_ldfrag<NG>(sb, arow, brow, cpos1, xf1, wf1);
    g1_mma<NG>(xf0, wf0, acc);
    if (kt < 9) {
      if (kt < 8) asm volatile("s_waitcnt vmcnt(6) lgkmcnt(0)" ::: "memory");
      else asm volatile("s_waitcnt vmcnt(0) lgkmcnt(0)" ::: "memory");
      __builtin_amdgcn_s_barrier();
      asm volatile("" ::: "memory");
      g1_ldfrag<NG>(smem + nbuf * G1_STAGE, arow, brow, cpos0, xf0, wf0);
      __builtin_amdgcn_sched_barrier(0);
      if (kt + 3 < 10) G1_ISSUE(kt + 3, buf);
    }
    g1_mma<NG>(xf1, wf1, acc);
  }
}

__global__ __launch_bounds__(512) void k3_gemm1(const u16* __restrict__ xni, const u16* __restrict__ w1i,
                                                u16* __restrict__ hmf) {
  __shared__ __attribute__((aligned(16))) unsigned char smem[G1_NSTG * G1_STAGE];
  const int tid = threadIdx.x, lane = tid & 63;
  const int wid = __builtin_amdgcn_readfirstlane(tid >> 6);
  const int wm = wid & 3, wn = wid >> 2;
  const int fr = lane & 15, fq = lane >> 4;
  const int bid = blockIdx.x;
  const int xcd = bid & 7, idx = bid >> 3;
  const int mt = (xcd & 1) * 8 + (idx & 7);
  const int nb = (xcd >> 1) * 4 + (idx >> 3);
  const int g0 = 27 * (nb >> 2) + 7 * (nb & 3);
  const int ng = ((nb & 3) == 3) ? 6 : 7;
  const int gw0 = wn * 4;
  const int gwn = wn ? ng - 4 : 4;
  const unsigned char* Ag = reinterpret_cast<const unsigned char*>(xni) + (size_t)mt * 10 * G1_A_BYTES;
  const unsigned char* Bg = reinterpret_cast<const unsigned char*>(w1i);
  f32x4 acc[2][8];
#pragma unroll
  for (int m = 0; m < 2; ++m)
#pragma unroll
    for (int n = 0; n < 8; ++n) acc[m][n] = (f32x4){0, 0, 0, 0};
  const int swz = fr & 7;
  const int cpos0 = ((0 + fq) ^ swz) << 4, cpos1 = ((4 + fq) ^ swz) << 4;
  const int arow = wm * 32 + fr, brow = gw0 * 32 + fr;
  if (wn == 0) g1_main<4>(smem, Ag, Bg, g0, wid, lane, arow, brow, cpos0, cpos1, acc);
  else g1_main<3>(smem, Ag, Bg, g0, wid, lane, arow, brow, cpos0, cpos1, acc);
#pragma unroll
  for (int m = 0; m < 2; ++m) {
    const int mt16 = mt * 8 + wm * 2 + m;
#pragma unroll
    for (int gI = 0; gI < 4; ++gI) {
      if (gI < gwn) {
        float o[4];
#pragma unroll
        for (int j = 0; j < 4; ++j) {
          const float gv = rbf(acc[m][2 * gI][j]);
          const float uv = rbf(acc[m][2 * gI + 1][j]);
          o[j] = rbf(gelu_tanh(gv)) * uv;
        }
        const int f = 16 * (g0 + gw0 + gI) + 4 * fq;
        const int ks = f >> 5, fq2 = (f & 31) >> 3, j0 = f & 7;
        u16* dst = hmf + ((size_t)(mt16 * 54 + ks) * 64 + fr + 16 * fq2) * 8 + j0;
        *reinterpret_cast<u32x2*>(dst) = (u32x2){pack2(o[0], o[1]), pack2(o[2], o[3])};
      }
    }
  }
}

__global__ __launch_bounds__(256) void k4_gemm2(const u16* __restrict__ hmf, const u16* __restrict__ wdf,
                                                const u16* __restrict__ h, u16* __restrict__ out) {
  __shared__ __attribute__((aligned(16))) float sred[4 * 64 * 84];
  const int tid = threadIdx.x, lane = tid & 63;
  const int wk = __builtin_amdgcn_readfirstlane(tid >> 6);
  const int fr = lane & 15, fq = lane >> 4;
  const int bid = blockIdx.x;
  const int xcd = bid & 7, idx = bid >> 3;
  const int mt = (xcd >> 1) * 8 + (idx & 7);
  const int nb = (xcd & 1) * 4 + (idx >> 3);
  const int ks0 = (wk < 2) ? wk * 14 : 28 + (wk - 2) * 13;
  const int nks = (wk < 2) ? 14 : 13;
  const bf16x8* Ap = reinterpret_cast<const bf16x8*>(hmf) + (size_t)(mt * 4) * 54 * 64 + lane;
  const bf16x8* Bp = reinterpret_cast<const bf16x8*>(wdf) + (size_t)(nb * 5) * 54 * 64 + lane;
  const int rl = tid >> 2, c0 = (tid & 3) * 20;
  const int row = mt * 64 + rl;
  u32x2 hres[5];
  {
    const u16* hr = h + (size_t)row * D_ + nb * 80 + c0;
#pragma unroll
    for (int i = 0; i < 5; ++i) hres[i] = *reinterpret_cast<const u32x2*>(hr + 4 * i);
  }
  f32x4 acc[4][5];
#pragma unroll
  for (int m = 0; m < 4; ++m)
#pragma unroll
    for (int n = 0; n < 5; ++n) acc[m][n] = (f32x4){0, 0, 0, 0};
  bf16x8 xa[4][4], wb[4][5];
#define G2_LOAD(slot, kidx)                                                              \
  {                                                                                      \
    const int kq_ = ks0 + (((kidx) < nks) ? (kidx) : nks - 1);                           \
    _Pragma("unroll") for (int m = 0; m < 4; ++m) xa[slot][m] = Ap[(m * 54 + kq_) * 64];   \
    _Pragma("unroll") for (int n = 0; n < 5; ++n) wb[slot][n] = Bp[(n * 54 + kq_) * 64];   \
  }
#define G2_MMA(slot)                                                                     \
  {                                                                                      \
    _Pragma("unroll") for (int n = 0; n < 5; ++n)                                        \
    _Pragma("unroll") for (int m = 0; m < 4; ++m)                                        \
      acc[m][n] = __builtin_amdgcn_mfma_f32_16x16x32_bf16(wb[slot][n], xa[slot][m], acc[m][n], 0, 0, 0); \
  }
  G2_LOAD(0, 0);
  G2_LOAD(1, 1);
  G2_LOAD(2, 2);
  __builtin_amdgcn_sched_barrier(0);
#pragma unroll
  for (int i = 0; i < 13; ++i) {
    if (i + 3 < 14) G2_LOAD((i + 3) & 3, i + 3);
    __builtin_amdgcn_sched_barrier(0);
    G2_MMA(i & 3);
    __builtin_amdgcn_sched_barrier(0);
  }
  if (nks == 14) G2_MMA(13 & 3);
  float* sw = sred + wk * 64 * 84;
#pragma unroll
  for (int m = 0; m < 4; ++m)
#pragma unroll
    for (int n = 0; n < 5; ++n)
      *reinterpret_cast<f32x4*>(sw + (m * 16 + fr) * 84 + n * 16 + 4 * fq) = acc[m][n];
  __syncthreads();
  {
    u16* orow = out + (size_t)row * D_ + nb * 80 + c0;
#pragma unroll
    for (int i = 0; i < 5; ++i) {
      const float* p = sred + rl * 84 + c0 + 4 * i;
      const f32x4 a0 = *reinterpret_cast<const f32x4*>(p);
      const f32x4 a1 = *reinterpret_cast<const f32x4*>(p + 64 * 84);
      const f32x4 a2 = *reinterpret_cast<const f32x4*>(p + 2 * 64 * 84);
      const f32x4 a3 = *reinterpret_cast<const f32x4*>(p + 3 * 64 * 84);
      float o[4];
#pragma unroll
      for (int j = 0; j < 4; ++j) o[j] = rbf((a0[j] + a1[j]) + (a2[j] + a3[j]));
      const u32 r0 = pack2(lo_f(hres[i][0]) + o[0], hi_f(hres[i][0]) + o[1]);
      const u32 r1 = pack2(lo_f(hres[i][1]) + o[2], hi_f(hres[i][1]) + o[3]);
      *reinterpret_cast<u32x2*>(orow + 4 * i) = (u32x2){r0, r1};
    }
  }
}

extern "C" void kernel_launch(void* const* d_in, const int* in_sizes, int n_in,
                              void* d_out, int out_size, void* d_ws, size_t ws_size,
                              hipStream_t stream) {
  const u16* x = (const u16*)d_in[0];
  const u16* logA = (const u16*)d_in[1];
  const u16* Aim = (const u16*)d_in[2];
  const u16* Cr = (const u16*)d_in[3];
  const u16* Ci = (const u16*)d_in[4];
  const u16* logdt = (const u16*)d_in[5];
  const u16* Dskip = (const u16*)d_in[6];
  const u16* wg = (const u16*)d_in[8];
  const u16* wu = (const u16*)d_in[9];
  const u16* wd = (const u16*)d_in[10];
  u16* out = (u16*)d_out;

  char* ws = (char*)d_ws;
  size_t off = 0;
  auto alloc = [&](size_t bytes) { char* p = ws + off; off += (bytes + 255) & ~(size_t)255; return p; };
  u16* uT = (u16*)alloc((size_t)D_ * LT_ * 2);
  u16* sT = (u16*)alloc((size_t)D_ * LT_ * 2);
  u16* h = (u16*)alloc((size_t)L_ * D_ * 2);
  u16* xni = (u16*)alloc((size_t)L_ * D_ * 2);
  u16* hmf = (u16*)alloc((size_t)L_ * F_ * 2);
  u16* w1i = (u16*)alloc((size_t)2 * F_ * D_ * 2);
  u16* wdf = (u16*)alloc((size_t)D_ * F_ * 2);
  float* Kc = (float*)alloc((size_t)2 * D_ * KC_STRIDE * 4);

  K1Args a;
  a.x = x; a.wg = wg; a.wu = wu; a.wd = wd;
  a.logA = logA; a.Aim = Aim; a.Cr = Cr; a.Ci = Ci; a.logdt = logdt;
  a.uT = uT; a.w1i = w1i; a.wdf = wdf; a.Kc = Kc;
  k1_kernel<<<K1_BLOCKS, 256, 0, stream>>>(a);
  K2Args a2;
  a2.Kc = Kc; a2.Dskip = Dskip; a2.uT = uT; a2.sT = sT;
  k2_conv<<<D_, 256, 0, stream>>>(a2);
  LN2Args a3;
  a3.sT = sT; a3.x = x; a3.h = h; a3.xni = xni; a3.wg = wg; a3.wu = wu; a3.wd = wd; a3.w1i = w1i; a3.wdf = wdf;
  k_ln2<<<LN2_BLOCKS, 1024, 0, stream>>>(a3);
  k3_gemm1<<<256, 512, 0, stream>>>(xni, w1i, hmf);
  k4_gemm2<<<256, 256, 0, stream>>>(hmf, wdf, h, out);
}
```

```cpp
#include <hip/hip_runtime.h>
#include <hip/hip_bf16.h>
#include <stdint.h>

constexpr int L_ = 2048;
constexpr int D_ = 640;
constexpr int N_ = 128;
constexpr int F_ = 1728;
constexpr int LT_ = 2112;
constexpr int KC_STRIDE = 648;

using bf16x8 = __attribute__((ext_vector_type(8))) short;
using f32x4 = __attribute__((ext_vector_type(4))) float;
using u32x4 = __attribute__((ext_vector_type(4))) unsigned int;
using u32x2 = __attribute__((ext_vector_type(2))) unsigned int;
using f32x2 = __attribute__((ext_vector_type(2))) float;
typedef unsigned short u16;
typedef unsigned int u32;

__device__ __forceinline__ float bf2f(u16 v) { return __uint_as_float(((u32)v) << 16); }
__device__ __forceinline__ u16 f2bf(float f) {
  __hip_bfloat16 b = __float2bfloat16(f);
  return *reinterpret_cast<u16*>(&b);
}
__device__ __forceinline__ float rbf(float f) { return bf2f(f2bf(f)); }
__device__ __forceinline__ float lo_f(u32 v) { return __uint_as_float(v << 16); }
__device__ __forceinline__ float hi_f(u32 v) { return __uint_as_float(v & 0xffff0000u); }
__device__ __forceinline__ u32 pack2(float a, float b) { return (u32)f2bf(a) | ((u32)f2bf(b) << 16); }

template <int CTRL, int RMASK>
__device__ __forceinline__ float dpp_add(float v) {
  const int t = __builtin_amdgcn_update_dpp(0, __float_as_int(v), CTRL, RMASK, 0xf, false);
  return v + __int_as_float(t);
}
__device__ __forceinline__ float wave_sum(float v) {
  v = dpp_add<0x111, 0xf>(v);
  v = dpp_add<0x112, 0xf>(v);
  v = dpp_add<0x114, 0xf>(v);
  v = dpp_add<0x118, 0xf>(v);
  v = dpp_add<0x142, 0xa>(v);
  v = dpp_add<0x143, 0xc>(v);
  return __int_as_float(__builtin_amdgcn_readlane(__float_as_int(v), 63));
}

__device__ __forceinline__ void cexpp(float re2, float imr, float p, float& outr, float& outi) {
  const float mag = __builtin_amdgcn_exp2f(re2 * p);
  const float t = __builtin_amdgcn_fractf(imr * p);
  outr = mag * __builtin_amdgcn_cosf(t);
  outi = mag * __builtin_amdgcn_sinf(t);
}

__device__ __forceinline__ void ln_row(const float (&f)[10], float (&o)[10]) {
  float s = 0.f;
#pragma unroll
  for (int i = 0; i < 10; ++i) s += f[i];
  const float mean = wave_sum(s) * (1.f / D_);
  float q = 0.f;
#pragma unroll
  for (int i = 0; i < 10; ++i) { const float t = f[i] - mean; q += t * t; }
  const float var = wave_sum(q) * (1.f / D_);
  const float mb = rbf(mean);
  const float sd = rbf(sqrtf(rbf(rbf(var) + 1e-5f)));
  const float rs = 1.f / sd;
#pragma unroll
  for (int i = 0; i < 10; ++i) {
    const float t = rbf(f[i] - mb);
    const float q0 = t * rs;
    o[i] = fmaf(fmaf(-q0, sd, t), rs, q0);
  }
}

constexpr int K1_KG_BLOCKS = 2 * D_;
constexpr int K1_LN_BLOCKS = L_ / 16;
constexpr int K1_W1_TILES = (D_ / 64) * (F_ / 64);
constexpr int K1_WD_TILES = (F_ / 64) * (D_ / 64);
constexpr int K1_W_BLOCKS = (K1_W1_TILES + K1_WD_TILES);
constexpr int K1_BLOCKS = K1_KG_BLOCKS + K1_LN_BLOCKS + K1_W_BLOCKS;

__device__ __forceinline__ void wtile_load(int tile, int tid, u16* st, const u16* wg, const u16* wu, const u16* wd) {
  if (tile < K1_W1_TILES) {
    const int kt = tile / 27, cc0 = (tile % 27) * 64;
#pragma unroll
    for (int i = 0; i < 4; ++i) {
      const int idx = tid + 256 * i;
      const int mat = idx >> 9, r = (idx >> 3) & 63, c = (idx & 7) * 8;
      const u16* src = mat ? wu : wg;
      const u32x4 v = *reinterpret_cast<const u32x4*>(src + (size_t)(kt * 64 + r) * F_ + cc0 + c);
      *reinterpret_cast<u32x4*>(st + (mat * 64 + r) * 72 + c) = v;
    }
  } else {
    const int t2 = tile - K1_W1_TILES;
    const int k0 = (t2 / 10) * 64, n0 = (t2 % 10) * 64;
#pragma unroll
    for (int i = 0; i < 2; ++i) {
      const int idx = tid + 256 * i;
      const int r = idx >> 3, c = (idx & 7) * 8;
      const u32x4 v = *reinterpret_cast<const u32x4*>(wd + (size_t)(k0 + r) * D_ + n0 + c);
      *reinterpret_cast<u32x4*>(st + r * 72 + c) = v;
    }
  }
}
__device__ __forceinline__ void wtile_store(int tile, int tid, const u16* st, u16* w1i, u16* wdf) {
  if (tile < K1_W1_TILES) {
    const int kt = tile / 27, cc0 = (tile % 27) * 64;
#pragma unroll
    for (int i = 0; i < 4; ++i) {
      const int idx = tid + 256 * i;
      const int gl = idx >> 8, rr = (idx >> 3) & 31, pc = idx & 7;
      const int ch = pc ^ (rr & 7);
      const int mat = rr >> 4, col = gl * 16 + (rr & 15);
      u32 p[4];
#pragma unroll
      for (int j = 0; j < 4; ++j)
        p[j] = (u32)st[(mat * 64 + ch * 8 + 2 * j) * 72 + col] | ((u32)st[(mat * 64 + ch * 8 + 2 * j + 1) * 72 + col] << 16);
      const int grp = (cc0 >> 4) + gl;
      u16* dst = w1i + ((size_t)(kt * 108 + grp) * 2048) + rr * 64 + pc * 8;
      *reinterpret_cast<u32x4*>(dst) = (u32x4){p[0], p[1], p[2], p[3]};
    }
  } else {
    const int t2 = tile - K1_W1_TILES;
    const int k0 = (t2 / 10) * 64, n0 = (t2 % 10) * 64;
#pragma unroll
    for (int i = 0; i < 2; ++i) {
      const int idx = tid + 256 * i;
      const int ntl = idx >> 7, ksl = (idx >> 6) & 1, ln = idx & 63;
      const int fr = ln & 15, fq = ln >> 4;
      const int kb = ksl * 32 + fq * 8, col = ntl * 16 + fr;
      u32 p[4];
#pragma unroll
      for (int j = 0; j < 4; ++j)
        p[j] = (u32)st[(kb + 2 * j) * 72 + col] | ((u32)st[(kb + 2 * j + 1) * 72 + col] << 16);
      const int nt = (n0 >> 4) + ntl, ks = (k0 >> 5) + ksl;
      u16* dst = wdf + ((size_t)(nt * 54 + ks) * 64 + ln) * 8;
      *reinterpret_cast<u32x4*>(dst) = (u32x4){p[0], p[1], p[2], p[3]};
    }
  }
}

struct K1Args {
  const u16* x; const u16* wg; const u16* wu; const u16* wd;
  const u16* logA; const u16* Aim; const u16* Cr; const u16* Ci; const u16* logdt;
  u16* uT; u16* w1i; u16* wdf; float* Kc;
};

__global__ __launch_bounds__(256) void k1_kernel(K1Args a) {
  __shared__ __attribute__((aligned(16))) unsigned char smem_raw[16 * 648 * 2];
  const int tid = threadIdx.x, lane = tid & 63, wid = tid >> 6;
  int b = blockIdx.x;
  if (b < K1_LN_BLOCKS) {
    u16* su = reinterpret_cast<u16*>(smem_raw);
    const int row0 = b * 16;
    u32 v[4][5];
#pragma unroll
    for (int rr = 0; rr < 4; ++rr) {
      const u32* xr = reinterpret_cast<const u32*>(a.x + (size_t)(row0 + wid * 4 + rr) * D_);
#pragma unroll
      for (int i = 0; i < 5; ++i) v[rr][i] = xr[lane + 64 * i];
    }
#pragma unroll
    for (int rr = 0; rr < 4; ++rr) {
      float f[10], o[10];
#pragma unroll
      for (int i = 0; i < 5; ++i) { f[2 * i] = lo_f(v[rr][i]); f[2 * i + 1] = hi_f(v[rr][i]); }
      ln_row(f, o);
#pragma unroll
      for (int i = 0; i < 5; ++i)
        *reinterpret_cast<u32*>(su + (wid * 4 + rr) * 648 + 2 * (lane + 64 * i)) = pack2(o[2 * i], o[2 * i + 1]);
    }
    __syncthreads();
#pragma unroll
    for (int it = 0; it < 3; ++it) {
      const int d = tid + 256 * it;
      if (d < D_) {
        u32 pu[8];
#pragma unroll
        for (int r = 0; r < 8; ++r)
          pu[r] = (u32)su[(2 * r) * 648 + d] | ((u32)su[(2 * r + 1) * 648 + d] << 16);
        u32x4* du = reinterpret_cast<u32x4*>(a.uT + (size_t)d * LT_ + row0);
        du[0] = (u32x4){pu[0], pu[1], pu[2], pu[3]};
        du[1] = (u32x4){pu[4], pu[5], pu[6], pu[7]};
      }
    }
    return;
  }
  b -= K1_LN_BLOCKS;
  if (b < K1_KG_BLOCKS) {
    const int d = b >> 1, half = b & 1;
    float* sp = reinterpret_cast<float*>(smem_raw);
    float* sr = sp + 512;
    const float dt = rbf(__expf(bf2f(a.logdt[d])));
    if (tid < 64) {
      const int n = half * 64 + tid;
      const float la = bf2f(a.logA[d * N_ + n]);
      f32x4 pv;
      pv[0] = (-rbf(expf(la)) * dt) * 1.4426950408889634f;
      pv[1] = (bf2f(a.Aim[d * N_ + n]) * dt) * 0.15915494309189535f;
      pv[2] = bf2f(a.Cr[d * N_ + n]) * dt;
      pv[3] = bf2f(a.Ci[d * N_ + n]) * dt;
      *reinterpret_cast<f32x4*>(sp + 4 * tid) = pv;
    }
    __syncthreads();
    const int rc = lane & 15;
    const int kk = lane >> 4;
    f32x4 acc0 = {0, 0, 0, 0}, acc1 = {0, 0, 0, 0}, acc2 = {0, 0, 0, 0}, acc3 = {0, 0, 0, 0};
    const float pA = (float)(16 * rc);
    const float pB = (float)(rc + 1);
    {
      f32x2 cr[2], ci[2], Ar[2], Ai[2], Br[2], Bi[2];
#pragma unroll
      for (int i = 0; i < 2; ++i) {
        const f32x4 p0 = *reinterpret_cast<const f32x4*>(sp + 4 * (wid * 16 + 4 * kk + 2 * i));
        const f32x4 p1 = *reinterpret_cast<const f32x4*>(sp + 4 * (wid * 16 + 4 * kk + 2 * i + 1));
        cr[i] = (f32x2){p0[2], p1[2]}; ci[i] = (f32x2){p0[3], p1[3]};
        float r0, i0, r1, i1;
        cexpp(p0[0], p0[1], pA, r0, i0); cexpp(p1[0], p1[1], pA, r1, i1);
        Ar[i] = (f32x2){r0, r1}; Ai[i] = (f32x2){i0, i1};
        cexpp(p0[0], p0[1], pB, r0, i0); cexpp(p1[0], p1[1], pB, r1, i1);
        Br[i] = (f32x2){r0, r1}; Bi[i] = (f32x2){i0, i1};
      }
#define KG_SPLIT(X, Y, HI, LO)                                                     \
      { HI = pack2(X, Y); LO = pack2((X) - lo_f(HI), (Y) - hi_f(HI)); }
#define KG_STEP(ACC)                                                              \
      {                                                                             \
        u32x4 ah, al, bh, bl;                                                       \
        _Pragma("unroll") for (int i = 0; i < 2; ++i) {                             \
          const f32x2 tr = __builtin_elementwise_fma(Ar[i], cr[i], -(Ai[i] * ci[i]));   \
          const f32x2 ti = __builtin_elementwise_fma(-Ar[i], ci[i], -(Ai[i] * cr[i]));  \
          u32 h_, l_;                                                               \
          KG_SPLIT(tr[0], ti[0], h_, l_); ah[2 * i] = h_; al[2 * i] = l_;           \
          KG_SPLIT(tr[1], ti[1], h_, l_); ah[2 * i + 1] = h_; al[2 * i + 1] = l_;   \
          KG_SPLIT(Br[i][0], Bi[i][0], h_, l_); bh[2 * i] = h_; bl[2 * i] = l_;     \
          KG_SPLIT(Br[i][1], Bi[i][1], h_, l_); bh[2 * i + 1] = h_; bl[2 * i + 1] = l_; \
        }                                                                           \
        const bf16x8 fah = __builtin_bit_cast(bf16x8, ah), fal = __builtin_bit_cast(bf16x8, al); \
        const bf16x8 fbh = __builtin_bit_cast(bf16x8, bh), fbl = __builtin_bit_cast(bf16x8, bl); \
        ACC = __builtin_amdgcn_mfma_f32_16x16x32_bf16(fal, fbh, ACC, 0, 0, 0);      \
        ACC = __builtin_amdgcn_mfma_f32_16x16x32_bf16(fah, fbl, ACC, 0, 0, 0);      \
        ACC = __builtin_amdgcn_mfma_f32_16x16x32_bf16(fah, fbh, ACC, 0, 0, 0);      \
      }
#define KG_SQ()                                                                   \
      _Pragma("unroll") for (int i = 0; i < 2; ++i) {                               \
        const f32x2 t0 = __builtin_elementwise_fma(Ar[i], Ar[i], -(Ai[i] * Ai[i])); \
        Ai[i] = (Ar[i] + Ar[i]) * Ai[i]; Ar[i] = t0;                                \
        const f32x2 t1 = __builtin_elementwise_fma(Br[i], Br[i], -(Bi[i] * Bi[i])); \
        Bi[i] = (Br[i] + Br[i]) * Bi[i]; Br[i] = t1;                                \
      }
      KG_STEP(acc0); KG_SQ();
      KG_STEP(acc1); KG_SQ();
      KG_STEP(acc2); KG_SQ();
      KG_STEP(acc3);
    }
    {
      float* w = sr + wid * 1024;
#pragma unroll
      for (int j = 0; j < 4; ++j) {
        const int e = (4 * kk + j) * 16 + rc;
        w[e] = acc0[j]; w[256 + e] = acc1[j]; w[512 + e] = acc2[j]; w[768 + e] = acc3[j];
      }
    }
    __syncthreads();
    float* Kd = a.Kc + (size_t)(d * 2 + half) * KC_STRIDE;
    {
      Kd[tid + 1] = 2.f * ((sr[tid] + sr[1024 + tid]) + (sr[2048 + tid] + sr[3072 + tid]));
      if (tid >= 128) {
#pragma unroll
        for (int gI = 1; gI < 4; ++gI) {
          const int e = gI * 256 + tid;
          Kd[gI * 128 + tid + 1] = 2.f * ((sr[e] + sr[1024 + e]) + (sr[2048 + e] + sr[3072 + e]));
        }
      }
      if (wid == 0) {
        const float s0 = 2.f * wave_sum(sp[4 * lane + 2]);
        if (lane == 0) Kd[0] = s0;
      }
    }
    return;
  }
  b -= K1_KG_BLOCKS;
  {
    u16* st = reinterpret_cast<u16*>(smem_raw);
    wtile_load(b, tid, st, a.wg, a.wu, a.wd);
    __syncthreads();
    wtile_store(b, tid, st, a.w1i, a.wdf);
  }
}

constexpr int CV_KLEN = 2208;
constexpr int CV_USTRIDE = 2320;
constexpr int CV_OFF_U = 2 * CV_KLEN * 2;
constexpr int CV_LDS_BYTES = CV_OFF_U + 8 * CV_USTRIDE * 2;

template <int TMIN>
__device__ __forceinline__ void conv_steps(int s0, int s1, const u16* kh, const u16* kl, const u16* ub,
                                           f32x4 (&acc)[8]) {
  bf16x8 ah = *reinterpret_cast<const bf16x8*>(kh + 32 * s0);
  bf16x8 al = *reinterpret_cast<const bf16x8*>(kl + 32 * s0);
  bf16x8 bv[8];
#pragma unroll
  for (int t = TMIN; t < 8; ++t) bv[t] = *reinterpret_cast<const bf16x8*>(ub + 32 * s0 - 256 * t);
  for (int s = s0; s < s1; ++s) {
    const bf16x8 ah2 = *reinterpret_cast<const bf16x8*>(kh + 32 * s + 32);
    const bf16x8 al2 = *reinterpret_cast<const bf16x8*>(kl + 32 * s + 32);
    bf16x8 bv2[8];
#pragma unroll
    for (int t = TMIN; t < 8; ++t) bv2[t] = *reinterpret_cast<const bf16x8*>(ub + 32 * s + 32 - 256 * t);
    __builtin_amdgcn_sched_barrier(0);
#pragma unroll
    for (int t = TMIN; t < 8; ++t) {
      acc[t] = __builtin_amdgcn_mfma_f32_16x16x32_bf16(ah, bv[t], acc[t], 0, 0, 0);
      acc[t] = __builtin_amdgcn_mfma_f32_16x16x32_bf16(al, bv[t], acc[t], 0, 0, 0);
    }
    __builtin_amdgcn_sched_barrier(0);
    ah = ah2; al = al2;
#pragma unroll
    for (int t = TMIN; t < 8; ++t) bv[t] = bv2[t];
  }
}

struct K2Args {
  const float* Kc; const u16* Dskip; const u16* uT; u16* sT;
};

__device__ __forceinline__ void build_u_chunk(const u32 (&W)[8], u16* dst) {
#pragma unroll
  for (int rho = 0; rho < 8; ++rho) {
    u32 o[4];
#pragma unroll
    for (int k = 0; k < 4; ++k) {
      if ((rho & 1) == 0) {
        const int aI = (rho + 8 - 2 * k) >> 1;
        o[k] = (W[aI] & 0xffffu) | (W[aI - 1] & 0xffff0000u);
      } else {
        const int aI = (rho + 7 - 2 * k) >> 1;
        o[k] = (W[aI] >> 16) | (W[aI] << 16);
      }
    }
    *reinterpret_cast<u32x4*>(dst + rho * CV_USTRIDE) = (u32x4){o[0], o[1], o[2], o[3]};
  }
}

__global__ __launch_bounds__(256, 3) void k2_conv(K2Args a) {
  __shared__ __attribute__((aligned(16))) unsigned char smem[CV_LDS_BYTES];
  u16* sKh = reinterpret_cast<u16*>(smem);
  u16* sKl = sKh + CV_KLEN;
  u16* sU = reinterpret_cast<u16*>(smem + CV_OFF_U);
  const int tid = threadIdx.x, lane = tid & 63, wid = tid >> 6;
  const int d = blockIdx.x;
  const u16* ud = a.uT + (size_t)d * LT_;
  const float* Kd = a.Kc + (size_t)(2 * d) * KC_STRIDE;
  const int q = 255 - tid;
  const u32x4 w0 = *reinterpret_cast<const u32x4*>(ud + 8 * q);
  u32x4 w1 = {0, 0, 0, 0};
  if (q < 255) w1 = *reinterpret_cast<const u32x4*>(ud + 8 * q + 8);
  float kv[8];
  {
    const int j0 = 8 * tid;
    const int e = (j0 < 256) ? 0 : (j0 < 512) ? 1 : (j0 < 1024) ? 2 : 3;
    const int base = 128 * e;
    const int hm1 = ((1 << e) >> 1) - 1;
#pragma unroll
    for (int jj = 0; jj < 8; ++jj) {
      const int j = j0 + jj;
      const int pq = (e == 0) ? j : ((j + hm1 + ((j >> e) & 1)) >> e);
      kv[jj] = Kd[base + pq] + Kd[KC_STRIDE + base + pq];
    }
  }
  {
    const u32 W[8] = {w0[0], w0[1], w0[2], w0[3], w1[0], w1[1], w1[2], w1[3]};
    build_u_chunk(W, sU + 8 * tid);
    if (tid == 255) {
      const u32 W2[8] = {0, 0, 0, 0, w0[0], w0[1], w0[2], w0[3]};
      build_u_chunk(W2, sU + 8 * 256);
    }
    if (tid < 8 * 19) {
      const int rho = tid / 19, m = 257 + tid % 19;
      *reinterpret_cast<u32x4*>(sU + rho * CV_USTRIDE + 8 * m) = (u32x4){0, 0, 0, 0};
    } else if (tid >= 160 && tid < 200) {
      const int t = tid - 160;
      const int m = (t % 20 < 16) ? t % 20 : 256 + t % 20;
      u16* dst = (t < 20) ? sKh : sKl;
      *reinterpret_cast<u32x4*>(dst + 8 * m) = (u32x4){0, 0, 0, 0};
    }
    u32x4 ph, pl;
#pragma unroll
    for (int k = 0; k < 4; ++k) {
      const u32 hh = pack2(kv[2 * k], kv[2 * k + 1]);
      ph[k] = hh;
      pl[k] = pack2(kv[2 * k] - lo_f(hh), kv[2 * k + 1] - hi_f(hh));
    }
    *reinterpret_cast<u32x4*>(sKh + 128 + 8 * tid) = ph;
    *reinterpret_cast<u32x4*>(sKl + 128 + 8 * tid) = pl;
  }
  __syncthreads();
  const int r = lane & 15, g = lane >> 4;
  const int ap = (lane & 15) >> 3, rho = lane & 7;
  f32x4 acc[8];
#pragma unroll
  for (int t = 0; t < 8; ++t) acc[t] = (f32x4){0, 0, 0, 0};
  const u16* kh = sKh + 128 + 8 * r + 8 * g;
  const u16* kl = sKl + 128 + 8 * r + 8 * g;
  const u16* ub = sU + rho * CV_USTRIDE + 8 * g - 128 * ap + 2048;
  if (wid == 0) {
    conv_steps<0>(-4, 5, kh, kl, ub, acc);
  } else if (wid == 1) {
    conv_steps<1>(5, 13, kh, kl, ub, acc);
    conv_steps<2>(13, 16, kh, kl, ub, acc);
  } else if (wid == 2) {
    conv_steps<2>(16, 21, kh, kl, ub, acc);
    conv_steps<3>(21, 29, kh, kl, ub, acc);
    conv_steps<4>(29, 30, kh, kl, ub, acc);
  } else {
    conv_steps<4>(30, 37, kh, kl, ub, acc);
    conv_steps<5>(37, 45, kh, kl, ub, acc);
    conv_steps<6>(45, 53, kh, kl, ub, acc);
    conv_steps<7>(53, 61, kh, kl, ub, acc);
  }
  __syncthreads();
  float* sy = reinterpret_cast<float*>(smem);
#pragma unroll
  for (int t = 0; t < 8; ++t) {
#pragma unroll
    for (int j = 0; j < 4; ++j) {
      const int l = 256 * t + 128 * ap + rho + 8 * (4 * g + j);
      sy[wid * L_ + l] = acc[t][j];
    }
  }
  __syncthreads();
  const float dsk = bf2f(a.Dskip[d]);
  {
    const int l0 = q * 8;
    float y[8];
#pragma unroll
    for (int hh = 0; hh < 2; ++hh) {
      f32x4 a0 = *reinterpret_cast<const f32x4*>(sy + l0 + 4 * hh);
      f32x4 a1 = *reinterpret_cast<const f32x4*>(sy + L_ + l0 + 4 * hh);
      f32x4 a2 = *reinterpret_cast<const f32x4*>(sy + 2 * L_ + l0 + 4 * hh);
      f32x4 a3 = *reinterpret_cast<const f32x4*>(sy + 3 * L_ + l0 + 4 * hh);
#pragma unroll
      for (int j = 0; j < 4; ++j) y[4 * hh + j] = (a0[j] + a1[j]) + (a2[j] + a3[j]);
    }
    u32 o[4];
#pragma unroll
    for (int k = 0; k < 4; ++k) {
      const float t0 = rbf(lo_f(w0[k]) * dsk), t1 = rbf(hi_f(w0[k]) * dsk);
      o[k] = pack2(rbf(y[2 * k]) + t0, rbf(y[2 * k + 1]) + t1);
    }
    *reinterpret_cast<u32x4*>(a.sT + (size_t)d * LT_ + l0) = (u32x4){o[0], o[1], o[2], o[3]};
  }
}

struct LN2Args {
  const u16* sT; const u16* x; u16* h; u16* xni;
  const u16* wg; const u16* wu; const u16* wd; u16* w1i; u16* wdf;
};
constexpr int LN2_BLOCKS = L_ / 16;
constexpr int LN2_W_BLOCKS = (K1_W1_TILES + K1_WD_TILES) / 4;

__global__ __launch_bounds__(1024) void k_ln2(LN2Args a) {
  __shared__ __attribute__((aligned(16))) u16 ss[16 * 648];
  const int tid = threadIdx.x, lane = tid & 63, wid = tid >> 6;
  const u16* __restrict__ sT = a.sT; const u16* __restrict__ x = a.x;
  u16* __restrict__ h = a.h; u16* __restrict__ xni = a.xni;
  const int row0 = blockIdx.x * 16;
  const int row = row0 + wid;
  u32 xv[5];
  {
    const u32* xr = reinterpret_cast<const u32*>(x + (size_t)row * D_);
#pragma unroll
    for (int i = 0; i < 5; ++i) xv[i] = xr[lane + 64 * i];
  }
  if (tid < D_) {
    const int d = tid;
    const u32x4* src = reinterpret_cast<const u32x4*>(sT + (size_t)d * LT_ + row0);
    const u32x4 a0 = src[0], a1 = src[1];
#pragma unroll
    for (int k = 0; k < 4; ++k) {
      ss[(2 * k) * 648 + d] = (u16)(a0[k] & 0xffffu);
      ss[(2 * k + 1) * 648 + d] = (u16)(a0[k] >> 16);
      ss[(8 + 2 * k) * 648 + d] = (u16)(a1[k] & 0xffffu);
      ss[(8 + 2 * k + 1) * 648 + d] = (u16)(a1[k] >> 16);
    }
  }
  __syncthreads();
  {
    float f[10], o[10];
    u32* hrow = reinterpret_cast<u32*>(h + (size_t)row * D_);
#pragma unroll
    for (int i = 0; i < 5; ++i) {
      const u32 sv = *reinterpret_cast<const u32*>(ss + wid * 648 + 2 * (lane + 64 * i));
      const u32 hv = pack2(lo_f(xv[i]) + lo_f(sv), hi_f(xv[i]) + hi_f(sv));
      hrow[lane + 64 * i] = hv;
      f[2 * i] = lo_f(hv); f[2 * i + 1] = hi_f(hv);
    }
    ln_row(f, o);
    const int mt = row >> 7, r = row & 127;
#pragma unroll
    for (int i = 0; i < 5; ++i) {
      const int c = 2 * (lane + 64 * i);
      const int kt = c >> 6, cc = c & 63;
      const int off = ((mt * 10 + kt) * 128 + r) * 64 + (((cc >> 3) ^ (r & 7)) << 3) + (cc & 7);
      *reinterpret_cast<u32*>(xni + off) = pack2(o[2 * i], o[2 * i + 1]);
    }
  }
}

__device__ __forceinline__ float gelu_tanh(float x) {
  const float c0 = -2.f * 0.7978845608028654f * 1.4426950408889634f;
  const float c1 = c0 * 0.044715f;
  const float e = __builtin_amdgcn_exp2f(x * fmaf(x * x, c1, c0));
  return x * __builtin_amdgcn_rcpf(1.f + e);
}

constexpr int G1_A_BYTES = 16384;
constexpr int G1_B_BYTES = 7 * 4096;
constexpr int G1_STAGE = G1_A_BYTES + G1_B_BYTES;
constexpr int G1_NSTG = 3;

template <int NG>
__device__ __forceinline__ void g1_ldfrag(const unsigned char* sb, int arow, int brow, int cpos,
                                          bf16x8 (&xf)[2], bf16x8 (&wf)[8]) {
#pragma unroll
  for (int m = 0; m < 2; ++m)
    xf[m] = *reinterpret_cast<const bf16x8*>(sb + (arow + m * 16) * 128 + cpos);
#pragma unroll
  for (int n = 0; n < 2 * NG; ++n)
    wf[n] = *reinterpret_cast<const bf16x8*>(sb + G1_A_BYTES + (brow + n * 16) * 128 + cpos);
}
template <int NG>
__device__ __forceinline__ void g1_mma(const bf16x8 (&xf)[2], const bf16x8 (&wf)[8], f32x4 (&acc)[2][8]) {
#pragma unroll
  for (int n = 0; n < 2 * NG; ++n)
#pragma unroll
    for (int m = 0; m < 2; ++m)
      acc[m][n] = __builtin_amdgcn_mfma_f32_16x16x32_bf16(wf[n], xf[m], acc[m][n], 0, 0, 0);
}

template <int NG>
__device__ __forceinline__ void g1_main(unsigned char* smem, const unsigned char* Ag, const unsigned char* Bg,
                                        int g0, int wid, int lane, int arow, int brow, int cpos0, int cpos1,
                                        f32x4 (&acc)[2][8]) {
#define G1_ISSUE(kt, buf)                                                                              \
  {                                                                                                    \
    unsigned char* sb_ = smem + (buf) * G1_STAGE;                                                      \
    _Pragma("unroll") for (int i = 0; i < 2; ++i) {                                                    \
      const int piece = wid + 8 * i;                                                                   \
      __builtin_amdgcn_global_load_lds(                                                                \
          (const __attribute__((address_space(1))) void*)(Ag + (size_t)(kt) * G1_A_BYTES + piece * 1024 + lane * 16), \
          (__attribute__((address_space(3))) void*)(sb_ + piece * 1024), 16, 0, 0);                    \
    }                                                                                                  \
    _Pragma("unroll") for (int i = 0; i < 4; ++i) {                                                    \
      int pb = wid + 8 * i; if (pb >= 28) pb -= 4;                                                     \
      const int gi = pb >> 2;                                                                          \
      const int grp = (g0 + gi < 108) ? g0 + gi : 107;                                                 \
      __builtin_amdgcn_global_load_lds(                                                                \
          (const __attribute__((address_space(1))) void*)(Bg + ((size_t)((kt) * 108 + grp) * 4096) + (pb & 3) * 1024 + lane * 16), \
          (__attribute__((address_space(3))) void*)(sb_ + G1_A_BYTES + pb * 1024), 16, 0, 0);          \
    }                                                                                                  \
  }
  bf16x8 xf0[2], wf0[8], xf1[2], wf1[8];
  G1_ISSUE(0, 0);
  G1_ISSUE(1, 1);
  G1_ISSUE(2, 2);
  asm volatile("s_waitcnt vmcnt(12)" ::: "memory");
  __builtin_amdgcn_s_barrier();
  asm volatile("" ::: "memory");
  g1_ldfrag<NG>(smem, arow, brow, cpos0, xf0, wf0);
#pragma unroll
  for (int kt = 0; kt < 10; ++kt) {
    const int buf = kt % 3;
    const unsigned char* sb = smem + buf * G1_STAGE;
    const int nbuf = (buf == 2) ? 0 : buf + 1;
    g1_ldfrag<NG>(sb, arow, brow, cpos1, xf1, wf1);
    g1_mma<NG>(xf0, wf0, acc);
    if (kt < 9) {
      if (kt < 8) asm volatile("s_waitcnt vmcnt(6) lgkmcnt(0)" ::: "memory");
      else asm volatile("s_waitcnt vmcnt(0) lgkmcnt(0)" ::: "memory");
      __builtin_amdgcn_s_barrier();
      asm volatile("" ::: "memory");
      g1_ldfrag<NG>(smem + nbuf * G1_STAGE, arow, brow, cpos0, xf0, wf0);
      __builtin_amdgcn_sched_barrier(0);
      if (kt + 3 < 10) G1_ISSUE(kt + 3, buf);
    }
    g1_mma<NG>(xf1, wf1, acc);
  }
}

__global__ __launch_bounds__(512) void k3_gemm1(const u16* __restrict__ xni, const u16* __restrict__ w1i,
                                                u16* __restrict__ hmf) {
  __shared__ __attribute__((aligned(16))) unsigned char smem[G1_NSTG * G1_STAGE];
  const int tid = threadIdx.x, lane = tid & 63;
  const int wid = __builtin_amdgcn_readfirstlane(tid >> 6);
  const int wm = wid & 3, wn = wid >> 2;
  const int fr = lane & 15, fq = lane >> 4;
  const int bid = blockIdx.x;
  const int xcd = bid & 7, idx = bid >> 3;
  const int mt = (xcd & 1) * 8 + (idx & 7);
  const int nb = (xcd >> 1) * 4 + (idx >> 3);
  const int g0 = 27 * (nb >> 2) + 7 * (nb & 3);
  const int ng = ((nb & 3) == 3) ? 6 : 7;
  const int gw0 = wn * 4;
  const int gwn = wn ? ng - 4 : 4;
  const unsigned char* Ag = reinterpret_cast<const unsigned char*>(xni) + (size_t)mt * 10 * G1_A_BYTES;
  const unsigned char* Bg = reinterpret_cast<const unsigned char*>(w1i);
  f32x4 acc[2][8];
#pragma unroll
  for (int m = 0; m < 2; ++m)
#pragma unroll
    for (int n = 0; n < 8; ++n) acc[m][n] = (f32x4){0, 0, 0, 0};
  const int swz = fr & 7;
  const int cpos0 = ((0 + fq) ^ swz) << 4, cpos1 = ((4 + fq) ^ swz) << 4;
  const int arow = wm * 32 + fr, brow = gw0 * 32 + fr;
  if (wn == 0) g1_main<4>(smem, Ag, Bg, g0, wid, lane, arow, brow, cpos0, cpos1, acc);
  else g1_main<3>(smem, Ag, Bg, g0, wid, lane, arow, brow, cpos0, cpos1, acc);
#pragma unroll
  for (int m = 0; m < 2; ++m) {
    const int mt16 = mt * 8 + wm * 2 + m;
#pragma unroll
    for (int gI = 0; gI < 4; ++gI) {
      if (gI < gwn) {
        float o[4];
#pragma unroll
        for (int j = 0; j < 4; ++j) {
          const float gv = rbf(acc[m][2 * gI][j]);
          const float uv = rbf(acc[m][2 * gI + 1][j]);
          o[j] = rbf(gelu_tanh(gv)) * uv;
        }
        const int f = 16 * (g0 + gw0 + gI) + 4 * fq;
        const int ks = f >> 5, fq2 = (f & 31) >> 3, j0 = f & 7;
        u16* dst = hmf + ((size_t)(mt16 * 54 + ks) * 64 + fr + 16 * fq2) * 8 + j0;
        *reinterpret_cast<u32x2*>(dst) = (u32x2){pack2(o[0], o[1]), pack2(o[2], o[3])};
      }
    }
  }
}

__global__ __launch_bounds__(256) void k4_gemm2(const u16* __restrict__ hmf, const u16* __restrict__ wdf,
                                                const u16* __restrict__ h, u16* __restrict__ out) {
  __shared__ __attribute__((aligned(16))) float sred[4 * 64 * 84];
  const int tid = threadIdx.x, lane = tid & 63;
  const int wk = __builtin_amdgcn_readfirstlane(tid >> 6);
  const int fr = lane & 15, fq = lane >> 4;
  const int bid = blockIdx.x;
  const int xcd = bid & 7, idx = bid >> 3;
  const int mt = (xcd >> 1) * 8 + (idx & 7);
  const int nb = (xcd & 1) * 4 + (idx >> 3);
  const int ks0 = (wk < 2) ? wk * 14 : 28 + (wk - 2) * 13;
  const int nks = (wk < 2) ? 14 : 13;
  const bf16x8* Ap = reinterpret_cast<const bf16x8*>(hmf) + (size_t)(mt * 4) * 54 * 64 + lane;
  const bf16x8* Bp = reinterpret_cast<const bf16x8*>(wdf) + (size_t)(nb * 5) * 54 * 64 + lane;
  const int rl = tid >> 2, c0 = (tid & 3) * 20;
  const int row = mt * 64 + rl;
  u32x2 hres[5];
  {
    const u16* hr = h + (size_t)row * D_ + nb * 80 + c0;
#pragma unroll
    for (int i = 0; i < 5; ++i) hres[i] = *reinterpret_cast<const u32x2*>(hr + 4 * i);
  }
  f32x4 acc[4][5];
#pragma unroll
  for (int m = 0; m < 4; ++m)
#pragma unroll
    for (int n = 0; n < 5; ++n) acc[m][n] = (f32x4){0, 0, 0, 0};
  bf16x8 xa[4][4], wb[4][5];
#define G2_LOAD(slot, kidx)                                                              \
  {                                                                                      \
    const int kq_ = ks0 + (((kidx) < nks) ? (kidx) : nks - 1);                           \
    _Pragma("unroll") for (int m = 0; m < 4; ++m) xa[slot][m] = Ap[(m * 54 + kq_) * 64];   \
    _Pragma("unroll") for (int n = 0; n < 5; ++n) wb[slot][n] = Bp[(n * 54 + kq_) * 64];   \
  }
#define G2_MMA(slot)                                                                     \
  {                                                                                      \
    _Pragma("unroll") for (int n = 0; n < 5; ++n)                                        \
    _Pragma("unroll") for (int m = 0; m < 4; ++m)                                        \
      acc[m][n] = __builtin_amdgcn_mfma_f32_16x16x32_bf16(wb[slot][n], xa[slot][m], acc[m][n], 0, 0, 0); \
  }
  G2_LOAD(0, 0);
  G2_LOAD(1, 1);
  G2_LOAD(2, 2);
  __builtin_amdgcn_sched_barrier(0);
#pragma unroll
  for (int i = 0; i < 13; ++i) {
    if (i + 3 < 14) G2_LOAD((i + 3) & 3, i + 3);
    __builtin_amdgcn_sched_barrier(0);
    G2_MMA(i & 3);
    __builtin_amdgcn_sched_barrier(0);
  }
  if (nks == 14) G2_MMA(13 & 3);
  float* sw = sred + wk * 64 * 84;
#pragma unroll
  for (int m = 0; m < 4; ++m)
#pragma unroll
    for (int n = 0; n < 5; ++n)
      *reinterpret_cast<f32x4*>(sw + (m * 16 + fr) * 84 + n * 16 + 4 * fq) = acc[m][n];
  __syncthreads();
  {
    u16* orow = out + (size_t)row * D_ + nb * 80 + c0;
#pragma unroll
    for (int i = 0; i < 5; ++i) {
      const float* p = sred + rl * 84 + c0 + 4 * i;
      const f32x4 a0 = *reinterpret_cast<const f32x4*>(p);
      const f32x4 a1 = *reinterpret_cast<const f32x4*>(p + 64 * 84);
      const f32x4 a2 = *reinterpret_cast<const f32x4*>(p + 2 * 64 * 84);
      const f32x4 a3 = *reinterpret_cast<const f32x4*>(p + 3 * 64 * 84);
      float o[4];
#pragma unroll
      for (int j = 0; j < 4; ++j) o[j] = rbf((a0[j] + a1[j]) + (a2[j] + a3[j]));
      const u32 r0 = pack2(lo_f(hres[i][0]) + o[0], hi_f(hres[i][0]) + o[1]);
      const u32 r1 = pack2(lo_f(hres[i][1]) + o[2], hi_f(hres[i][1]) + o[3]);
      *reinterpret_cast<u32x2*>(orow + 4 * i) = (u32x2){r0, r1};
    }
  }
}

extern "C" void kernel_launch(void* const* d_in, const int* in_sizes, int n_in,
                              void* d_out, int out_size, void* d_ws, size_t ws_size,
                              hipStream_t stream) {
  const u16* x = (const u16*)d_in[0];
  const u16* logA = (const u16*)d_in[1];
  const u16* Aim = (const u16*)d_in[2];
  const u16* Cr = (const u16*)d_in[3];
  const u16* Ci = (const u16*)d_in[4];
  const u16* logdt = (const u16*)d_in[5];
  const u16* Dskip = (const u16*)d_in[6];
  const u16* wg = (const u16*)d_in[8];
  const u16* wu = (const u16*)d_in[9];
  const u16* wd = (const u16*)d_in[10];
  u16* out = (u16*)d_out;

  char* ws = (char*)d_ws;
  size_t off = 0;
  auto alloc = [&](size_t bytes) { char* p = ws + off; off += (bytes + 255) & ~(size_t)255; return p; };
  u16* uT = (u16*)alloc((size_t)D_ * LT_ * 2);
  u16* sT = (u16*)alloc((size_t)D_ * LT_ * 2);
  u16* h = (u16*)alloc((size_t)L_ * D_ * 2);
  u16* xni = (u16*)alloc((size_t)L_ * D_ * 2);
  u16* hmf = (u16*)alloc((size_t)L_ * F_ * 2);
  u16* w1i = (u16*)alloc((size_t)2 * F_ * D_ * 2);
  u16* wdf = (u16*)alloc((size_t)D_ * F_ * 2);
  float* Kc = (float*)alloc((size_t)2 * D_ * KC_STRIDE * 4);

  K1Args a;
  a.x = x; a.wg = wg; a.wu = wu; a.wd = wd;
  a.logA = logA; a.Aim = Aim; a.Cr = Cr; a.Ci = Ci; a.logdt = logdt;
  a.uT = uT; a.w1i = w1i; a.wdf = wdf; a.Kc = Kc;
  k1_kernel<<<K1_BLOCKS, 256, 0, stream>>>(a);
  K2Args a2;
  a2.Kc = Kc; a2.Dskip = Dskip; a2.uT = uT; a2.sT = sT;
  k2_conv<<<D_, 256, 0, stream>>>(a2);
  LN2Args a3;
  a3.sT = sT; a3.x = x; a3.h = h; a3.xni = xni; a3.wg = wg; a3.wu = wu; a3.wd = wd; a3.w1i = w1i; a3.wdf = wdf;
  k_ln2<<<LN2_BLOCKS, 1024, 0, stream>>>(a3);
  k3_gemm1<<<256, 512, 0, stream>>>(xni, w1i, hmf);
  k4_gemm2<<<256, 256, 0, stream>>>(hmf, wdf, h, out);
}
```

```cpp
#include <hip/hip_runtime.h>
#include <hip/hip_bf16.h>
#include <stdint.h>

constexpr int L_ = 2048;
constexpr int D_ = 640;
constexpr int N_ = 128;
constexpr int F_ = 1728;
constexpr int LT_ = 2112;
constexpr int KC_STRIDE = 648;

using bf16x8 = __attribute__((ext_vector_type(8))) short;
using f32x4 = __attribute__((ext_vector_type(4))) float;
using u32x4 = __attribute__((ext_vector_type(4))) unsigned int;
using u32x2 = __attribute__((ext_vector_type(2))) unsigned int;
using f32x2 = __attribute__((ext_vector_type(2))) float;
typedef unsigned short u16;
typedef unsigned int u32;

__device__ __forceinline__ float bf2f(u16 v) { return __uint_as_float(((u32)v) << 16); }
__device__ __forceinline__ u16 f2bf(float f) {
  __hip_bfloat16 b = __float2bfloat16(f);
  return *reinterpret_cast<u16*>(&b);
}
__device__ __forceinline__ float rbf(float f) { return bf2f(f2bf(f)); }
__device__ __forceinline__ float lo_f(u32 v) { return __uint_as_float(v << 16); }
__device__ __forceinline__ float hi_f(u32 v) { return __uint_as_float(v & 0xffff0000u); }
__device__ __forceinline__ u32 pack2(float a, float b) { return (u32)f2bf(a) | ((u32)f2bf(b) << 16); }

template <int CTRL, int RMASK>
__device__ __forceinline__ float dpp_add(float v) {
  const int t = __builtin_amdgcn_update_dpp(0, __float_as_int(v), CTRL, RMASK, 0xf, false);
  return v + __int_as_float(t);
}
__device__ __forceinline__ float wave_sum(float v) {
  v = dpp_add<0x111, 0xf>(v);
  v = dpp_add<0x112, 0xf>(v);
  v = dpp_add<0x114, 0xf>(v);
  v = dpp_add<0x118, 0xf>(v);
  v = dpp_add<0x142, 0xa>(v);
  v = dpp_add<0x143, 0xc>(v);
  return __int_as_float(__builtin_amdgcn_readlane(__float_as_int(v), 63));
}

__device__ __forceinline__ void cexpp(float re2, float imr, float p, float& outr, float& outi) {
  const float mag = __builtin_amdgcn_exp2f(re2 * p);
  const float t = __builtin_amdgcn_fractf(imr * p);
  outr = mag * __builtin_amdgcn_cosf(t);
  outi = mag * __builtin_amdgcn_sinf(t);
}

__device__ __forceinline__ void ln_row(const float (&f)[10], float (&o)[10]) {
  float s = 0.f;
#pragma unroll
  for (int i = 0; i < 10; ++i) s += f[i];
  const float mean = wave_sum(s) * (1.f / D_);
  float q = 0.f;
#pragma unroll
  for (int i = 0; i < 10; ++i) { const float t = f[i] - mean; q += t * t; }
  const float var = wave_sum(q) * (1.f / D_);
  const float mb = rbf(mean);
  const float sd = rbf(sqrtf(rbf(rbf(var) + 1e-5f)));
  const float rs = 1.f / sd;
#pragma unroll
  for (int i = 0; i < 10; ++i) {
    const float t = rbf(f[i] - mb);
    const float q0 = t * rs;
    o[i] = fmaf(fmaf(-q0, sd, t), rs, q0);
  }
}

constexpr int K1_KG_BLOCKS = 2 * D_;
constexpr int K1_LN_BLOCKS = L_ / 16;
constexpr int K1_W1_TILES = (D_ / 64) * (F_ / 64);
constexpr int K1_WD_TILES = (F_ / 64) * (D_ / 64);
constexpr int K1_W_BLOCKS = (K1_W1_TILES + K1_WD_TILES);
constexpr int K1_BLOCKS = K1_KG_BLOCKS + K1_LN_BLOCKS + K1_W_BLOCKS;

__device__ __forceinline__ void wtile_load(int tile, int tid, u16* st, const u16* wg, const u16* wu, const u16* wd) {
  if (tile < K1_W1_TILES) {
    const int kt = tile / 27, cc0 = (tile % 27) * 64;
#pragma unroll
    for (int i = 0; i < 4; ++i) {
      const int idx = tid + 256 * i;
      const int mat = idx >> 9, r = (idx >> 3) & 63, c = (idx & 7) * 8;
      const u16* src = mat ? wu : wg;
      const u32x4 v = *reinterpret_cast<const u32x4*>(src + (size_t)(kt * 64 + r) * F_ + cc0 + c);
      *reinterpret_cast<u32x4*>(st + (mat * 64 + r) * 72 + c) = v;
    }
  } else {
    const int t2 = tile - K1_W1_TILES;
    const int k0 = (t2 / 10) * 64, n0 = (t2 % 10) * 64;
#pragma unroll
    for (int i = 0; i < 2; ++i) {
      const int idx = tid + 256 * i;
      const int r = idx >> 3, c = (idx & 7) * 8;
      const u32x4 v = *reinterpret_cast<const u32x4*>(wd + (size_t)(k0 + r) * D_ + n0 + c);
      *reinterpret_cast<u32x4*>(st + r * 72 + c) = v;
    }
  }
}
__device__ __forceinline__ void wtile_store(int tile, int tid, const u16* st, u16* w1i, u16* wdf) {
  if (tile < K1_W1_TILES) {
    const int kt = tile / 27, cc0 = (tile % 27) * 64;
#pragma unroll
    for (int i = 0; i < 4; ++i) {
      const int idx = tid + 256 * i;
      const int gl = idx >> 8, rr = (idx >> 3) & 31, pc = idx & 7;
      const int ch = pc ^ (rr & 7);
      const int mat = rr >> 4, col = gl * 16 + (rr & 15);
      u32 p[4];
#pragma unroll
      for (int j = 0; j < 4; ++j)
        p[j] = (u32)st[(mat * 64 + ch * 8 + 2 * j) * 72 + col] | ((u32)st[(mat * 64 + ch * 8 + 2 * j + 1) * 72 + col] << 16);
      const int grp = (cc0 >> 4) + gl;
      u16* dst = w1i + ((size_t)(kt * 108 + grp) * 2048) + rr * 64 + pc * 8;
      *reinterpret_cast<u32x4*>(dst) = (u32x4){p[0], p[1], p[2], p[3]};
    }
  } else {
    const int t2 = tile - K1_W1_TILES;
    const int k0 = (t2 / 10) * 64, n0 = (t2 % 10) * 64;
#pragma unroll
    for (int i = 0; i < 2; ++i) {
      const int idx = tid + 256 * i;
      const int ntl = idx >> 7, ksl = (idx >> 6) & 1, ln = idx & 63;
      const int fr = ln & 15, fq = ln >> 4;
      const int kb = ksl * 32 + fq * 8, col = ntl * 16 + fr;
      u32 p[4];
#pragma unroll
      for (int j = 0; j < 4; ++j)
        p[j] = (u32)st[(kb + 2 * j) * 72 + col] | ((u32)st[(kb + 2 * j + 1) * 72 + col] << 16);
      const int nt = (n0 >> 4) + ntl, ks = (k0 >> 5) + ksl;
      u16* dst = wdf + ((size_t)(nt * 54 + ks) * 64 + ln) * 8;
      *reinterpret_cast<u32x4*>(dst) = (u32x4){p[0], p[1], p[2], p[3]};
    }
  }
}

struct K1Args {
  const u16* x; const u16* wg; const u16* wu; const u16* wd;
  const u16* logA; const u16* Aim; const u16* Cr; const u16* Ci; const u16* logdt;
  u16* uT; u16* w1i; u16* wdf; float* Kc;
};

__global__ __launch_bounds__(256) void k1_kernel(K1Args a) {
  __shared__ __attribute__((aligned(16))) unsigned char smem_raw[16 * 648 * 2];
  const int tid = threadIdx.x, lane = tid & 63, wid = tid >> 6;
  int b = blockIdx.x;
  if (b < K1_LN_BLOCKS) {
    u16* su = reinterpret_cast<u16*>(smem_raw);
    const int row0 = b * 16;
    u32 v[4][5];
#pragma unroll
    for (int rr = 0; rr < 4; ++rr) {
      const u32* xr = reinterpret_cast<const u32*>(a.x + (size_t)(row0 + wid * 4 + rr) * D_);
#pragma unroll
      for (int i = 0; i < 5; ++i) v[rr][i] = xr[lane + 64 * i];
    }
#pragma unroll
    for (int rr = 0; rr < 4; ++rr) {
      float f[10], o[10];
#pragma unroll
      for (int i = 0; i < 5; ++i) { f[2 * i] = lo_f(v[rr][i]); f[2 * i + 1] = hi_f(v[rr][i]); }
      ln_row(f, o);
#pragma unroll
      for (int i = 0; i < 5; ++i)
        *reinterpret_cast<u32*>(su + (wid * 4 + rr) * 648 + 2 * (lane + 64 * i)) = pack2(o[2 * i], o[2 * i + 1]);
    }
    __syncthreads();
#pragma unroll
    for (int it = 0; it < 3; ++it) {
      const int d = tid + 256 * it;
      if (d < D_) {
        u32 pu[8];
#pragma unroll
        for (int r = 0; r < 8; ++r)
          pu[r] = (u32)su[(2 * r) * 648 + d] | ((u32)su[(2 * r + 1) * 648 + d] << 16);
        u32x4* du = reinterpret_cast<u32x4*>(a.uT + (size_t)d * LT_ + row0);
        du[0] = (u32x4){pu[0], pu[1], pu[2], pu[3]};
        du[1] = (u32x4){pu[4], pu[5], pu[6], pu[7]};
      }
    }
    return;
  }
  b -= K1_LN_BLOCKS;
  if (b < K1_KG_BLOCKS) {
    const int d = b >> 1, half = b & 1;
    float* sp = reinterpret_cast<float*>(smem_raw);
    float* sr = sp + 512;
    const float dt = rbf(__expf(bf2f(a.logdt[d])));
    if (tid < 64) {
      const int n = half * 64 + tid;
      const float la = bf2f(a.logA[d * N_ + n]);
      f32x4 pv;
      pv[0] = (-rbf(expf(la)) * dt) * 1.4426950408889634f;
      pv[1] = (bf2f(a.Aim[d * N_ + n]) * dt) * 0.15915494309189535f;
      pv[2] = bf2f(a.Cr[d * N_ + n]) * dt;
      pv[3] = bf2f(a.Ci[d * N_ + n]) * dt;
      *reinterpret_cast<f32x4*>(sp + 4 * tid) = pv;
    }
    __syncthreads();
    const int rc = lane & 15;
    const int kk = lane >> 4;
    f32x4 acc0 = {0, 0, 0, 0}, acc1 = {0, 0, 0, 0}, acc2 = {0, 0, 0, 0}, acc3 = {0, 0, 0, 0};
    const float pA = (float)(16 * rc);
    const float pB = (float)(rc + 1);
#pragma unroll
    for (int i = 0; i < 2; ++i) {
      const f32x4 p0 = *reinterpret_cast<const f32x4*>(sp + 4 * (wid * 16 + 8 * i + kk));
      const f32x4 p1 = *reinterpret_cast<const f32x4*>(sp + 4 * (wid * 16 + 8 * i + 4 + kk));
      const f32x2 cr = {p0[2], p1[2]}, ci = {p0[3], p1[3]};
      f32x2 Ar, Ai, Br, Bi;
      {
        float r0, i0, r1, i1;
        cexpp(p0[0], p0[1], pA, r0, i0); cexpp(p1[0], p1[1], pA, r1, i1);
        Ar = (f32x2){r0, r1}; Ai = (f32x2){i0, i1};
        cexpp(p0[0], p0[1], pB, r0, i0); cexpp(p1[0], p1[1], pB, r1, i1);
        Br = (f32x2){r0, r1}; Bi = (f32x2){i0, i1};
      }
#define KG_STEP(ACC)                                                              \
      {                                                                             \
        const f32x2 tr = __builtin_elementwise_fma(Ar, cr, -(Ai * ci));             \
        const f32x2 ti = __builtin_elementwise_fma(-Ar, ci, -(Ai * cr));            \
        ACC = __builtin_amdgcn_mfma_f32_16x16x4f32(tr[0], Br[0], ACC, 0, 0, 0);     \
        ACC = __builtin_amdgcn_mfma_f32_16x16x4f32(ti[0], Bi[0], ACC, 0, 0, 0);     \
        ACC = __builtin_amdgcn_mfma_f32_16x16x4f32(tr[1], Br[1], ACC, 0, 0, 0);     \
        ACC = __builtin_amdgcn_mfma_f32_16x16x4f32(ti[1], Bi[1], ACC, 0, 0, 0);     \
      }
#define KG_SQ()                                                                   \
      {                                                                             \
        const f32x2 t0 = __builtin_elementwise_fma(Ar, Ar, -(Ai * Ai));             \
        Ai = (Ar + Ar) * Ai; Ar = t0;                                               \
        const f32x2 t1 = __builtin_elementwise_fma(Br, Br, -(Bi * Bi));             \
        Bi = (Br + Br) * Bi; Br = t1;                                               \
      }
      KG_STEP(acc0); KG_SQ();
      KG_STEP(acc1); KG_SQ();
      KG_STEP(acc2); KG_SQ();
      KG_STEP(acc3);
    }
    {
      float* w = sr + wid * 1024;
#pragma unroll
      for (int j = 0; j < 4; ++j) {
        const int e = (4 * kk + j) * 16 + rc;
        w[e] = acc0[j]; w[256 + e] = acc1[j]; w[512 + e] = acc2[j]; w[768 + e] = acc3[j];
      }
    }
    __syncthreads();
    float* Kd = a.Kc + (size_t)(d * 2 + half) * KC_STRIDE;
    {
      Kd[tid + 1] = 2.f * ((sr[tid] + sr[1024 + tid]) + (sr[2048 + tid] + sr[3072 + tid]));
      if (tid >= 128) {
#pragma unroll
        for (int gI = 1; gI < 4; ++gI) {
          const int e = gI * 256 + tid;
          Kd[gI * 128 + tid + 1] = 2.f * ((sr[e] + sr[1024 + e]) + (sr[2048 + e] + sr[3072 + e]));
        }
      }
      if (wid == 0) {
        const float s0 = 2.f * wave_sum(sp[4 * lane + 2]);
        if (lane == 0) Kd[0] = s0;
      }
    }
    return;
  }
  b -= K1_KG_BLOCKS;
  {
    u16* st = reinterpret_cast<u16*>(smem_raw);
    wtile_load(b, tid, st, a.wg, a.wu, a.wd);
    __syncthreads();
    wtile_store(b, tid, st, a.w1i, a.wdf);
  }
}

constexpr int CV_KLEN = 2208;
constexpr int CV_USTRIDE = 2320;
constexpr int CV_OFF_U = 2 * CV_KLEN * 2;
constexpr int CV_LDS_BYTES = CV_OFF_U + 8 * CV_USTRIDE * 2;

template <int TMIN>
__device__ __forceinline__ void conv_steps(int s0, int s1, const u16* kh, const u16* kl, const u16* ub,
                                           f32x4 (&acc)[8]) {
  bf16x8 ah = *reinterpret_cast<const bf16x8*>(kh + 32 * s0);
  bf16x8 al = *reinterpret_cast<const bf16x8*>(kl + 32 * s0);
  bf16x8 bv[8];
#pragma unroll
  for (int t = TMIN; t < 8; ++t) bv[t] = *reinterpret_cast<const bf16x8*>(ub + 32 * s0 - 256 * t);
  for (int s = s0; s < s1; ++s) {
    const bf16x8 ah2 = *reinterpret_cast<const bf16x8*>(kh + 32 * s + 32);
    const bf16x8 al2 = *reinterpret_cast<const bf16x8*>(kl + 32 * s + 32);
    bf16x8 bv2[8];
#pragma unroll
    for (int t = TMIN; t < 8; ++t) bv2[t] = *reinterpret_cast<const bf16x8*>(ub + 32 * s + 32 - 256 * t);
    __builtin_amdgcn_sched_barrier(0);
#pragma unroll
    for (int t = TMIN; t < 8; ++t) {
      acc[t] = __builtin_amdgcn_mfma_f32_16x16x32_bf16(ah, bv[t], acc[t], 0, 0, 0);
      acc[t] = __builtin_amdgcn_mfma_f32_16x16x32_bf16(al, bv[t], acc[t], 0, 0, 0);
    }
    __builtin_amdgcn_sched_barrier(0);
    ah = ah2; al = al2;
#pragma unroll
    for (int t = TMIN; t < 8; ++t) bv[t] = bv2[t];
  }
}

struct K2Args {
  const float* Kc; const u16* Dskip; const u16* uT; u16* sT;
};

__device__ __forceinline__ void build_u_chunk(const u32 (&W)[8], u16* dst) {
#pragma unroll
  for (int rho = 0; rho < 8; ++rho) {
    u32 o[4];
#pragma unroll
    for (int k = 0; k < 4; ++k) {
      if ((rho & 1) == 0) {
        const int aI = (rho + 8 - 2 * k) >> 1;
        o[k] = (W[aI] & 0xffffu) | (W[aI - 1] & 0xffff0000u);
      } else {
        const int aI = (rho + 7 - 2 * k) >> 1;
        o[k] = (W[aI] >> 16) | (W[aI] << 16);
      }
    }
    *reinterpret_cast<u32x4*>(dst + rho * CV_USTRIDE) = (u32x4){o[0], o[1], o[2], o[3]};
  }
}

__global__ __launch_bounds__(256, 3) void k2_conv(K2Args a) {
  __shared__ __attribute__((aligned(16))) unsigned char smem[CV_LDS_BYTES];
  u16* sKh = reinterpret_cast<u16*>(smem);
  u16* sKl = sKh + CV_KLEN;
  u16* sU = reinterpret_cast<u16*>(smem + CV_OFF_U);
  const int tid = threadIdx.x, lane = tid & 63, wid = tid >> 6;
  const int d = blockIdx.x;
  const u16* ud = a.uT + (size_t)d * LT_;
  const float* Kd = a.Kc + (size_t)(2 * d) * KC_STRIDE;
  const int q = 255 - tid;
  const u32x4 w0 = *reinterpret_cast<const u32x4*>(ud + 8 * q);
  u32x4 w1 = {0, 0, 0, 0};
  if (q < 255) w1 = *reinterpret_cast<const u32x4*>(ud + 8 * q + 8);
  float kv[8];
  {
    const int j0 = 8 * tid;
    const int e = (j0 < 256) ? 0 : (j0 < 512) ? 1 : (j0 < 1024) ? 2 : 3;
    const int base = 128 * e;
    const int hm1 = ((1 << e) >> 1) - 1;
#pragma unroll
    for (int jj = 0; jj < 8; ++jj) {
      const int j = j0 + jj;
      const int pq = (e == 0) ? j : ((j + hm1 + ((j >> e) & 1)) >> e);
      kv[jj] = Kd[base + pq] + Kd[KC_STRIDE + base + pq];
    }
  }
  {
    const u32 W[8] = {w0[0], w0[1], w0[2], w0[3], w1[0], w1[1], w1[2], w1[3]};
    build_u_chunk(W, sU + 8 * tid);
    if (tid == 255) {
      const u32 W2[8] = {0, 0, 0, 0, w0[0], w0[1], w0[2], w0[3]};
      build_u_chunk(W2, sU + 8 * 256);
    }
    if (tid < 8 * 19) {
      const int rho = tid / 19, m = 257 + tid % 19;
      *reinterpret_cast<u32x4*>(sU + rho * CV_USTRIDE + 8 * m) = (u32x4){0, 0, 0, 0};
    } else if (tid >= 160 && tid < 200) {
      const int t = tid - 160;
      const int m = (t % 20 < 16) ? t % 20 : 256 + t % 20;
      u16* dst = (t < 20) ? sKh : sKl;
      *reinterpret_cast<u32x4*>(dst + 8 * m) = (u32x4){0, 0, 0, 0};
    }
    u32x4 ph, pl;
#pragma unroll
    for (int k = 0; k < 4; ++k) {
      const u32 hh = pack2(kv[2 * k], kv[2 * k + 1]);
      ph[k] = hh;
      pl[k] = pack2(kv[2 * k] - lo_f(hh), kv[2 * k + 1] - hi_f(hh));
    }
    *reinterpret_cast<u32x4*>(sKh + 128 + 8 * tid) = ph;
    *reinterpret_cast<u32x4*>(sKl + 128 + 8 * tid) = pl;
  }
  __syncthreads();
  const int r = lane & 15, g = lane >> 4;
  const int ap = (lane & 15) >> 3, rho = lane & 7;
  f32x4 acc[8];
#pragma unroll
  for (int t = 0; t < 8; ++t) acc[t] = (f32x4){0, 0, 0, 0};
  const u16* kh = sKh + 128 + 8 * r + 8 * g;
  const u16* kl = sKl + 128 + 8 * r + 8 * g;
  const u16* ub = sU + rho * CV_USTRIDE + 8 * g - 128 * ap + 2048;
  if (wid == 0) {
    conv_steps<0>(-4, 5, kh, kl, ub, acc);
  } else if (wid == 1) {
    conv_steps<1>(5, 13, kh, kl, ub, acc);
    conv_steps<2>(13, 16, kh, kl, ub, acc);
  } else if (wid == 2) {
    conv_steps<2>(16, 21, kh, kl, ub, acc);
    conv_steps<3>(21, 29, kh, kl, ub, acc);
    conv_steps<4>(29, 30, kh, kl, ub, acc);
  } else {
    conv_steps<4>(30, 37, kh, kl, ub, acc);
    conv_steps<5>(37, 45, kh, kl, ub, acc);
    conv_steps<6>(45, 53, kh, kl, ub, acc);
    conv_steps<7>(53, 61, kh, kl, ub, acc);
  }
  __syncthreads();
  float* sy = reinterpret_cast<float*>(smem);
#pragma unroll
  for (int t = 0; t < 8; ++t) {
#pragma unroll
    for (int j = 0; j < 4; ++j) {
      const int l = 256 * t + 128 * ap + rho + 8 * (4 * g + j);
      sy[wid * L_ + l] = acc[t][j];
    }
  }
  __syncthreads();
  const float dsk = bf2f(a.Dskip[d]);
  {
    const int l0 = q * 8;
    float y[8];
#pragma unroll
    for (int hh = 0; hh < 2; ++hh) {
      f32x4 a0 = *reinterpret_cast<const f32x4*>(sy + l0 + 4 * hh);
      f32x4 a1 = *reinterpret_cast<const f32x4*>(sy + L_ + l0 + 4 * hh);
      f32x4 a2 = *reinterpret_cast<const f32x4*>(sy + 2 * L_ + l0 + 4 * hh);
      f32x4 a3 = *reinterpret_cast<const f32x4*>(sy + 3 * L_ + l0 + 4 * hh);
#pragma unroll
      for (int j = 0; j < 4; ++j) y[4 * hh + j] = (a0[j] + a1[j]) + (a2[j] + a3[j]);
    }
    u32 o[4];
#pragma unroll
    for (int k = 0; k < 4; ++k) {
      const float t0 = rbf(lo_f(w0[k]) * dsk), t1 = rbf(hi_f(w0[k]) * dsk);
      o[k] = pack2(rbf(y[2 * k]) + t0, rbf(y[2 * k + 1]) + t1);
    }
    *reinterpret_cast<u32x4*>(a.sT + (size_t)d * LT_ + l0) = (u32x4){o[0], o[1], o[2], o[3]};
  }
}

struct LN2Args {
  const u16* sT; const u16* x; u16* h; u16* xni;
  const u16* wg; const u16* wu; const u16* wd; u16* w1i; u16* wdf;
};
constexpr int LN2_BLOCKS = L_ / 16;
constexpr int LN2_W_BLOCKS = (K1_W1_TILES + K1_WD_TILES) / 4;

__global__ __launch_bounds__(1024) void k_ln2(LN2Args a) {
  __shared__ __attribute__((aligned(16))) u16 ss[16 * 648];
  const int tid = threadIdx.x, lane = tid & 63, wid = tid >> 6;
  const u16* __restrict__ sT = a.sT; const u16* __restrict__ x = a.x;
  u16* __restrict__ h = a.h; u16* __restrict__ xni = a.xni;
  const int row0 = blockIdx.x * 16;
  const int row = row0 + wid;
  u32 xv[5];
  {
    const u32* xr = reinterpret_cast<const u32*>(x + (size_t)row * D_);
#pragma unroll
    for (int i = 0; i < 5; ++i) xv[i] = xr[lane + 64 * i];
  }
  if (tid < D_) {
    const int d = tid;
    const u32x4* src = reinterpret_cast<const u32x4*>(sT + (size_t)d * LT_ + row0);
    const u32x4 a0 = src[0], a1 = src[1];
#pragma unroll
    for (int k = 0; k < 4; ++k) {
      ss[(2 * k) * 648 + d] = (u16)(a0[k] & 0xffffu);
      ss[(2 * k + 1) * 648 + d] = (u16)(a0[k] >> 16);
      ss[(8 + 2 * k) * 648 + d] = (u16)(a1[k] & 0xffffu);
      ss[(8 + 2 * k + 1) * 648 + d] = (u16)(a1[k] >> 16);
    }
  }
  __syncthreads();
  {
    float f[10], o[10];
    u32* hrow = reinterpret_cast<u32*>(h + (size_t)row * D_);
#pragma unroll
    for (int i = 0; i < 5; ++i) {
      const u32 sv = *reinterpret_cast<const u32*>(ss + wid * 648 + 2 * (lane + 64 * i));
      const u32 hv = pack2(lo_f(xv[i]) + lo_f(sv), hi_f(xv[i]) + hi_f(sv));
      hrow[lane + 64 * i] = hv;
      f[2 * i] = lo_f(hv); f[2 * i + 1] = hi_f(hv);
    }
    ln_row(f, o);
    const int mt = row >> 7, r = row & 127;
#pragma unroll
    for (int i = 0; i < 5; ++i) {
      const int c = 2 * (lane + 64 * i);
      const int kt = c >> 6, cc = c & 63;
      const int off = ((mt * 10 + kt) * 128 + r) * 64 + (((cc >> 3) ^ (r & 7)) << 3) + (cc & 7);
      *reinterpret_cast<u32*>(xni + off) = pack2(o[2 * i], o[2 * i + 1]);
    }
  }
}

__device__ __forceinline__ float gelu_tanh(float x) {
  const float c0 = -2.f * 0.7978845608028654f * 1.4426950408889634f;
  const float c1 = c0 * 0.044715f;
  const float e = __builtin_amdgcn_exp2f(x * fmaf(x * x, c1, c0));
  return x * __builtin_amdgcn_rcpf(1.f + e);
}

constexpr int G1_A_BYTES = 16384;
constexpr int G1_B_BYTES = 7 * 4096;
constexpr int G1_STAGE = G1_A_BYTES + G1_B_BYTES;
constexpr int G1_NSTG = 3;

template <int NG>
__device__ __forceinline__ void g1_ldfrag(const unsigned char* sb, int arow, int brow, int cpos,
                                          bf16x8 (&xf)[2], bf16x8 (&wf)[8]) {
#pragma unroll
  for (int m = 0; m < 2; ++m)
    xf[m] = *reinterpret_cast<const bf16x8*>(sb + (arow + m * 16) * 128 + cpos);
#pragma unroll
  for (int n = 0; n < 2 * NG; ++n)
    wf[n] = *reinterpret_cast<const bf16x8*>(sb + G1_A_BYTES + (brow + n * 16) * 128 + cpos);
}
template <int NG>
__device__ __forceinline__ void g1_mma(const bf16x8 (&xf)[2], const bf16x8 (&wf)[8], f32x4 (&acc)[2][8]) {
#pragma unroll
  for (int n = 0; n < 2 * NG; ++n)
#pragma unroll
    for (int m = 0; m < 2; ++m)
      acc[m][n] = __builtin_amdgcn_mfma_f32_16x16x32_bf16(wf[n], xf[m], acc[m][n], 0, 0, 0);
}

template <int NG>
__device__ __forceinline__ void g1_main(unsigned char* smem, const unsigned char* Ag, const unsigned char* Bg,
                                        int g0, int wid, int lane, int arow, int brow, int cpos0, int cpos1,
                                        f32x4 (&acc)[2][8]) {
#define G1_ISSUE(kt, buf)                                                                              \
  {                                                                                                    \
    unsigned char* sb_ = smem + (buf) * G1_STAGE;                                                      \
    _Pragma("unroll") for (int i = 0; i < 2; ++i) {                                                    \
      const int piece = wid + 8 * i;                                                                   \
      __builtin_amdgcn_global_load_lds(                                                                \
          (const __attribute__((address_space(1))) void*)(Ag + (size_t)(kt) * G1_A_BYTES + piece * 1024 + lane * 16), \
          (__attribute__((address_space(3))) void*)(sb_ + piece * 1024), 16, 0, 0);                    \
    }                                                                                                  \
    _Pragma("unroll") for (int i = 0; i < 4; ++i) {                                                    \
      int pb = wid + 8 * i; if (pb >= 28) pb -= 4;                                                     \
      const int gi = pb >> 2;                                                                          \
      const int grp = (g0 + gi < 108) ? g0 + gi : 107;                                                 \
      __builtin_amdgcn_global_load_lds(                                                                \
          (const __attribute__((address_space(1))) void*)(Bg + ((size_t)((kt) * 108 + grp) * 4096) + (pb & 3) * 1024 + lane * 16), \
          (__attribute__((address_space(3))) void*)(sb_ + G1_A_BYTES + pb * 1024), 16, 0, 0);          \
    }                                                                                                  \
  }
  bf16x8 xf0[2], wf0[8], xf1[2], wf1[8];
  G1_ISSUE(0, 0);
  G1_ISSUE(1, 1);
  G1_ISSUE(2, 2);
  asm volatile("s_waitcnt vmcnt(12)" ::: "memory");
  __builtin_amdgcn_s_barrier();
  asm volatile("" ::: "memory");
  g1_ldfrag<NG>(smem, arow, brow, cpos0, xf0, wf0);
#pragma unroll
  for (int kt = 0; kt < 10; ++kt) {
    const int buf = kt % 3;
    const unsigned char* sb = smem + buf * G1_STAGE;
    const int nbuf = (buf == 2) ? 0 : buf + 1;
    g1_ldfrag<NG>(sb, arow, brow, cpos1, xf1, wf1);
    g1_mma<NG>(xf0, wf0, acc);
    if (kt < 9) {
      if (kt < 8) asm volatile("s_waitcnt vmcnt(6) lgkmcnt(0)" ::: "memory");
      else asm volatile("s_waitcnt vmcnt(0) lgkmcnt(0)" ::: "memory");
      __builtin_amdgcn_s_barrier();
      asm volatile("" ::: "memory");
      g1_ldfrag<NG>(smem + nbuf * G1_STAGE, arow, brow, cpos0, xf0, wf0);
      __builtin_amdgcn_sched_barrier(0);
      if (kt + 3 < 10) G1_ISSUE(kt + 3, buf);
    }
    g1_mma<NG>(xf1, wf1, acc);
  }
}

__global__ __launch_bounds__(512) void k3_gemm1(const u16* __restrict__ xni, const u16* __restrict__ w1i,
                                                u16* __restrict__ hmf) {
  __shared__ __attribute__((aligned(16))) unsigned char smem[G1_NSTG * G1_STAGE];
  const int tid = threadIdx.x, lane = tid & 63;
  const int wid = __builtin_amdgcn_readfirstlane(tid >> 6);
  const int wm = wid & 3, wn = wid >> 2;
  const int fr = lane & 15, fq = lane >> 4;
  const int bid = blockIdx.x;
  const int xcd = bid & 7, idx = bid >> 3;
  const int mt = (xcd & 1) * 8 + (idx & 7);
  const int nb = (xcd >> 1) * 4 + (idx >> 3);
  const int g0 = 27 * (nb >> 2) + 7 * (nb & 3);
  const int ng = ((nb & 3) == 3) ? 6 : 7;
  const int gw0 = wn * 4;
  const int gwn = wn ? ng - 4 : 4;
  const unsigned char* Ag = reinterpret_cast<const unsigned char*>(xni) + (size_t)mt * 10 * G1_A_BYTES;
  const unsigned char* Bg = reinterpret_cast<const unsigned char*>(w1i);
  f32x4 acc[2][8];
#pragma unroll
  for (int m = 0; m < 2; ++m)
#pragma unroll
    for (int n = 0; n < 8; ++n) acc[m][n] = (f32x4){0, 0, 0, 0};
  const int swz = fr & 7;
  const int cpos0 = ((0 + fq) ^ swz) << 4, cpos1 = ((4 + fq) ^ swz) << 4;
  const int arow = wm * 32 + fr, brow = gw0 * 32 + fr;
  if (wn == 0) g1_main<4>(smem, Ag, Bg, g0, wid, lane, arow, brow, cpos0, cpos1, acc);
  else g1_main<3>(smem, Ag, Bg, g0, wid, lane, arow, brow, cpos0, cpos1, acc);
#pragma unroll
  for (int m = 0; m < 2; ++m) {
    const int mt16 = mt * 8 + wm * 2 + m;
#pragma unroll
    for (int gI = 0; gI < 4; ++gI) {
      if (gI < gwn) {
        float o[4];
#pragma unroll
        for (int j = 0; j < 4; ++j) {
          const float gv = rbf(acc[m][2 * gI][j]);
          const float uv = rbf(acc[m][2 * gI + 1][j]);
          o[j] = rbf(gelu_tanh(gv)) * uv;
        }
        const int f = 16 * (g0 + gw0 + gI) + 4 * fq;
        const int ks = f >> 5, fq2 = (f & 31) >> 3, j0 = f & 7;
        u16* dst = hmf + ((size_t)(mt16 * 54 + ks) * 64 + fr + 16 * fq2) * 8 + j0;
        *reinterpret_cast<u32x2*>(dst) = (u32x2){pack2(o[0], o[1]), pack2(o[2], o[3])};
      }
    }
  }
}

__global__ __launch_bounds__(256) void k4_gemm2(const u16* __restrict__ hmf, const u16* __restrict__ wdf,
                                                const u16* __restrict__ h, u16* __restrict__ out) {
  __shared__ __attribute__((aligned(16))) float sred[4 * 64 * 84];
  const int tid = threadIdx.x, lane = tid & 63;
  const int wk = __builtin_amdgcn_readfirstlane(tid >> 6);
  const int fr = lane & 15, fq = lane >> 4;
  const int bid = blockIdx.x;
  const int xcd = bid & 7, idx = bid >> 3;
  const int mt = (xcd >> 1) * 8 + (idx & 7);
  const int nb = (xcd & 1) * 4 + (idx >> 3);
  const int ks0 = (wk < 2) ? wk * 14 : 28 + (wk - 2) * 13;
  const int nks = (wk < 2) ? 14 : 13;
  const bf16x8* Ap = reinterpret_cast<const bf16x8*>(hmf) + (size_t)(mt * 4) * 54 * 64 + lane;
  const bf16x8* Bp = reinterpret_cast<const bf16x8*>(wdf) + (size_t)(nb * 5) * 54 * 64 + lane;
  const int rl = tid >> 2, c0 = (tid & 3) * 20;
  const int row = mt * 64 + rl;
  u32x2 hres[5];
  {
    const u16* hr = h + (size_t)row * D_ + nb * 80 + c0;
#pragma unroll
    for (int i = 0; i < 5; ++i) hres[i] = *reinterpret_cast<const u32x2*>(hr + 4 * i);
  }
  f32x4 acc[4][5];
#pragma unroll
  for (int m = 0; m < 4; ++m)
#pragma unroll
    for (int n = 0; n < 5; ++n) acc[m][n] = (f32x4){0, 0, 0, 0};
  bf16x8 xa[4][4], wb[4][5];
#define G2_LOAD(slot, kidx)                                                              \
  {                                                                                      \
    const int kq_ = ks0 + (((kidx) < nks) ? (kidx) : nks - 1);                           \
    _Pragma("unroll") for (int m = 0; m < 4; ++m) xa[slot][m] = Ap[(m * 54 + kq_) * 64];   \
    _Pragma("unroll") for (int n = 0; n < 5; ++n) wb[slot][n] = Bp[(n * 54 + kq_) * 64];   \
  }
#define G2_MMA(slot)                                                                     \
  {                                                                                      \
    _Pragma("unroll") for (int n = 0; n < 5; ++n)                                        \
    _Pragma("unroll") for (int m = 0; m < 4; ++m)                                        \
      acc[m][n] = __builtin_amdgcn_mfma_f32_16x16x32_bf16(wb[slot][n], xa[slot][m], acc[m][n], 0, 0, 0); \
  }
  G2_LOAD(0, 0);
  G2_LOAD(1, 1);
  G2_LOAD(2, 2);
  __builtin_amdgcn_sched_barrier(0);
#pragma unroll
  for (int i = 0; i < 13; ++i) {
    if (i + 3 < 14) G2_LOAD((i + 3) & 3, i + 3);
    __builtin_amdgcn_sched_barrier(0);
    G2_MMA(i & 3);
    __builtin_amdgcn_sched_barrier(0);
  }
  if (nks == 14) G2_MMA(13 & 3);
  float* sw = sred + wk * 64 * 84;
#pragma unroll
  for (int m = 0; m < 4; ++m)
#pragma unroll
    for (int n = 0; n < 5; ++n)
      *reinterpret_cast<f32x4*>(sw + (m * 16 + fr) * 84 + n * 16 + 4 * fq) = acc[m][n];
  __syncthreads();
  {
    u16* orow = out + (size_t)row * D_ + nb * 80 + c0;
#pragma unroll
    for (int i = 0; i < 5; ++i) {
      const float* p = sred + rl * 84 + c0 + 4 * i;
      const f32x4 a0 = *reinterpret_cast<const f32x4*>(p);
      const f32x4 a1 = *reinterpret_cast<const f32x4*>(p + 64 * 84);
      const f32x4 a2 = *reinterpret_cast<const f32x4*>(p + 2 * 64 * 84);
      const f32x4 a3 = *reinterpret_cast<const f32x4*>(p + 3 * 64 * 84);
      float o[4];
#pragma unroll
      for (int j = 0; j < 4; ++j) o[j] = rbf((a0[j] + a1[j]) + (a2[j] + a3[j]));
      const u32 r0 = pack2(lo_f(hres[i][0]) + o[0], hi_f(hres[i][0]) + o[1]);
      const u32 r1 = pack2(lo_f(hres[i][1]) + o[2], hi_f(hres[i][1]) + o[3]);
      *reinterpret_cast<u32x2*>(orow + 4 * i) = (u32x2){r0, r1};
    }
  }
}

extern "C" void kernel_launch(void* const* d_in, const int* in_sizes, int n_in,
                              void* d_out, int out_size, void* d_ws, size_t ws_size,
                              hipStream_t stream) {
  const u16* x = (const u16*)d_in[0];
  const u16* logA = (const u16*)d_in[1];
  const u16* Aim = (const u16*)d_in[2];
  const u16* Cr = (const u16*)d_in[3];
  const u16* Ci = (const u16*)d_in[4];
  const u16* logdt = (const u16*)d_in[5];
  const u16* Dskip = (const u16*)d_in[6];
  const u16* wg = (const u16*)d_in[8];
  const u16* wu = (const u16*)d_in[9];
  const u16* wd = (const u16*)d_in[10];
  u16* out = (u16*)d_out;

  char* ws = (char*)d_ws;
  size_t off = 0;
  auto alloc = [&](size_t bytes) { char* p = ws + off; off += (bytes + 255) & ~(size_t)255; return p; };
  u16* uT = (u16*)alloc((size_t)D_ * LT_ * 2);
  u16* sT = (u16*)alloc((size_t)D_ * LT_ * 2);
  u16* h = (u16*)alloc((size_t)L_ * D_ * 2);
  u16* xni = (u16*)alloc((size_t)L_ * D_ * 2);
  u16* hmf = (u16*)alloc((size_t)L_ * F_ * 2);
  u16* w1i = (u16*)alloc((size_t)2 * F_ * D_ * 2);
  u16* wdf = (u16*)alloc((size_t)D_ * F_ * 2);
  float* Kc = (float*)alloc((size_t)2 * D_ * KC_STRIDE * 4);

  K1Args a;
  a.x = x; a.wg = wg; a.wu = wu; a.wd = wd;
  a.logA = logA; a.Aim = Aim; a.Cr = Cr; a.Ci = Ci; a.logdt = logdt;
  a.uT = uT; a.w1i = w1i; a.wdf = wdf; a.Kc = Kc;
  k1_kernel<<<K1_BLOCKS, 256, 0, stream>>>(a);
  K2Args a2;
  a2.Kc = Kc; a2.Dskip = Dskip; a2.uT = uT; a2.sT = sT;
  k2_conv<<<D_, 256, 0, stream>>>(a2);
  LN2Args a3;
  a3.sT = sT; a3.x = x; a3.h = h; a3.xni = xni; a3.wg = wg; a3.wu = wu; a3.wd = wd; a3.w1i = w1i; a3.wdf = wdf;
  k_ln2<<<LN2_BLOCKS, 1024, 0, stream>>>(a3);
  k3_gemm1<<<256, 512, 0, stream>>>(xni, w1i, hmf);
  k4_gemm2<<<256, 256, 0, stream>>>(hmf, wdf, h, out);
}
```

```cpp
#include <hip/hip_runtime.h>
#include <hip/hip_bf16.h>
#include <stdint.h>

constexpr int L_ = 2048;
constexpr int D_ = 640;
constexpr int N_ = 128;
constexpr int F_ = 1728;
constexpr int LT_ = 2112;
constexpr int KC_STRIDE = 648;

using bf16x8 = __attribute__((ext_vector_type(8))) short;
using f32x4 = __attribute__((ext_vector_type(4))) float;
using u32x4 = __attribute__((ext_vector_type(4))) unsigned int;
using u32x2 = __attribute__((ext_vector_type(2))) unsigned int;
using f32x2 = __attribute__((ext_vector_type(2))) float;
typedef unsigned short u16;
typedef unsigned int u32;

__device__ __forceinline__ float bf2f(u16 v) { return __uint_as_float(((u32)v) << 16); }
__device__ __forceinline__ u16 f2bf(float f) {
  __hip_bfloat16 b = __float2bfloat16(f);
  return *reinterpret_cast<u16*>(&b);
}
__device__ __forceinline__ float rbf(float f) { return bf2f(f2bf(f)); }
__device__ __forceinline__ float lo_f(u32 v) { return __uint_as_float(v << 16); }
__device__ __forceinline__ float hi_f(u32 v) { return __uint_as_float(v & 0xffff0000u); }
__device__ __forceinline__ u32 pack2(float a, float b) { return (u32)f2bf(a) | ((u32)f2bf(b) << 16); }

template <int CTRL, int RMASK>
__device__ __forceinline__ float dpp_add(float v) {
  const int t = __builtin_amdgcn_update_dpp(0, __float_as_int(v), CTRL, RMASK, 0xf, false);
  return v + __int_as_float(t);
}
__device__ __forceinline__ float wave_sum(float v) {
  v = dpp_add<0x111, 0xf>(v);
  v = dpp_add<0x112, 0xf>(v);
  v = dpp_add<0x114, 0xf>(v);
  v = dpp_add<0x118, 0xf>(v);
  v = dpp_add<0x142, 0xa>(v);
  v = dpp_add<0x143, 0xc>(v);
  return __int_as_float(__builtin_amdgcn_readlane(__float_as_int(v), 63));
}

__device__ __forceinline__ void cexpp(float re2, float imr, float p, float& outr, float& outi) {
  const float mag = __builtin_amdgcn_exp2f(re2 * p);
  const float t = __builtin_amdgcn_fractf(imr * p);
  outr = mag * __builtin_amdgcn_cosf(t);
  outi = mag * __builtin_amdgcn_sinf(t);
}

__device__ __forceinline__ void ln_row(const float (&f)[10], float (&o)[10]) {
  float s = 0.f;
#pragma unroll
  for (int i = 0; i < 10; ++i) s += f[i];
  const float mean = wave_sum(s) * (1.f / D_);
  float q = 0.f;
#pragma unroll
  for (int i = 0; i < 10; ++i) { const float t = f[i] - mean; q += t * t; }
  const float var = wave_sum(q) * (1.f / D_);
  const float mb = rbf(mean);
  const float sd = rbf(sqrtf(rbf(rbf(var) + 1e-5f)));
  const float rs = 1.f / sd;
#pragma unroll
  for (int i = 0; i < 10; ++i) {
    const float t = rbf(f[i] - mb);
    const float q0 = t * rs;
    o[i] = fmaf(fmaf(-q0, sd, t), rs, q0);
  }
}

constexpr int K1_KG_BLOCKS = 2 * D_;
constexpr int K1_LN_BLOCKS = L_ / 16;
constexpr int K1_W1_TILES = (D_ / 64) * (F_ / 64);
constexpr int K1_WD_TILES = (F_ / 64) * (D_ / 64);
constexpr int K1_W_BLOCKS = (K1_W1_TILES + K1_WD_TILES);
constexpr int K1_BLOCKS = K1_KG_BLOCKS + K1_LN_BLOCKS + K1_W_BLOCKS;

__device__ __forceinline__ void wtile_load(int tile, int tid, u16* st, const u16* wg, const u16* wu, const u16* wd) {
  if (tile < K1_W1_TILES) {
    const int kt = tile / 27, cc0 = (tile % 27) * 64;
#pragma unroll
    for (int i = 0; i < 4; ++i) {
      const int idx = tid + 256 * i;
      const int mat = idx >> 9, r = (idx >> 3) & 63, c = (idx & 7) * 8;
      const u16* src = mat ? wu : wg;
      const u32x4 v = *reinterpret_cast<const u32x4*>(src + (size_t)(kt * 64 + r) * F_ + cc0 + c);
      *reinterpret_cast<u32x4*>(st + (mat * 64 + r) * 72 + c) = v;
    }
  } else {
    const int t2 = tile - K1_W1_TILES;
    const int k0 = (t2 / 10) * 64, n0 = (t2 % 10) * 64;
#pragma unroll
    for (int i = 0; i < 2; ++i) {
      const int idx = tid + 256 * i;
      const int r = idx >> 3, c = (idx & 7) * 8;
      const u32x4 v = *reinterpret_cast<const u32x4*>(wd + (size_t)(k0 + r) * D_ + n0 + c);
      *reinterpret_cast<u32x4*>(st + r * 72 + c) = v;
    }
  }
}
__device__ __forceinline__ void wtile_store(int tile, int tid, const u16* st, u16* w1i, u16* wdf) {
  if (tile < K1_W1_TILES) {
    const int kt = tile / 27, cc0 = (tile % 27) * 64;
#pragma unroll
    for (int i = 0; i < 4; ++i) {
      const int idx = tid + 256 * i;
      const int gl = idx >> 8, rr = (idx >> 3) & 31, pc = idx & 7;
      const int ch = pc ^ (rr & 7);
      const int mat = rr >> 4, col = gl * 16 + (rr & 15);
      u32 p[4];
#pragma unroll
      for (int j = 0; j < 4; ++j)
        p[j] = (u32)st[(mat * 64 + ch * 8 + 2 * j) * 72 + col] | ((u32)st[(mat * 64 + ch * 8 + 2 * j + 1) * 72 + col] << 16);
      const int grp = (cc0 >> 4) + gl;
      u16* dst = w1i + ((size_t)(kt * 108 + grp) * 2048) + rr * 64 + pc * 8;
      *reinterpret_cast<u32x4*>(dst) = (u32x4){p[0], p[1], p[2], p[3]};
    }
  } else {
    const int t2 = tile - K1_W1_TILES;
    const int k0 = (t2 / 10) * 64, n0 = (t2 % 10) * 64;
#pragma unroll
    for (int i = 0; i < 2; ++i) {
      const int idx = tid + 256 * i;
      const int ntl = idx >> 7, ksl = (idx >> 6) & 1, ln = idx & 63;
      const int fr = ln & 15, fq = ln >> 4;
      const int kb = ksl * 32 + fq * 8, col = ntl * 16 + fr;
      u32 p[4];
#pragma unroll
      for (int j = 0; j < 4; ++j)
        p[j] = (u32)st[(kb + 2 * j) * 72 + col] | ((u32)st[(kb + 2 * j + 1) * 72 + col] << 16);
      const int nt = (n0 >> 4) + ntl, ks = (k0 >> 5) + ksl;
      u16* dst = wdf + ((size_t)(nt * 54 + ks) * 64 + ln) * 8;
      *reinterpret_cast<u32x4*>(dst) = (u32x4){p[0], p[1], p[2], p[3]};
    }
  }
}

struct K1Args {
  const u16* x; const u16* wg; const u16* wu; const u16* wd;
  const u16* logA; const u16* Aim; const u16* Cr; const u16* Ci; const u16* logdt;
  u16* uT; u16* w1i; u16* wdf; float* Kc;
};

__global__ __launch_bounds__(256) void k1_kernel(K1Args a) {
  __shared__ __attribute__((aligned(16))) unsigned char smem_raw[16 * 648 * 2];
  const int tid = threadIdx.x, lane = tid & 63, wid = tid >> 6;
  int b = blockIdx.x;
  if (b < K1_LN_BLOCKS) {
    u16* su = reinterpret_cast<u16*>(smem_raw);
    const int row0 = b * 16;
    u32 v[4][5];
#pragma unroll
    for (int rr = 0; rr < 4; ++rr) {
      const u32* xr = reinterpret_cast<const u32*>(a.x + (size_t)(row0 + wid * 4 + rr) * D_);
#pragma unroll
      for (int i = 0; i < 5; ++i) v[rr][i] = xr[lane + 64 * i];
    }
#pragma unroll
    for (int rr = 0; rr < 4; ++rr) {
      float f[10], o[10];
#pragma unroll
      for (int i = 0; i < 5; ++i) { f[2 * i] = lo_f(v[rr][i]); f[2 * i + 1] = hi_f(v[rr][i]); }
      ln_row(f, o);
#pragma unroll
      for (int i = 0; i < 5; ++i)
        *reinterpret_cast<u32*>(su + (wid * 4 + rr) * 648 + 2 * (lane + 64 * i)) = pack2(o[2 * i], o[2 * i + 1]);
    }
    __syncthreads();
#pragma unroll
    for (int it = 0; it < 3; ++it) {
      const int d = tid + 256 * it;
      if (d < D_) {
        u32 pu[8];
#pragma unroll
        for (int r = 0; r < 8; ++r)
          pu[r] = (u32)su[(2 * r) * 648 + d] | ((u32)su[(2 * r + 1) * 648 + d] << 16);
        u32x4* du = reinterpret_cast<u32x4*>(a.uT + (size_t)d * LT_ + row0);
        du[0] = (u32x4){pu[0], pu[1], pu[2], pu[3]};
        du[1] = (u32x4){pu[4], pu[5], pu[6], pu[7]};
      }
    }
    return;
  }
  b -= K1_LN_BLOCKS;
  if (b < K1_KG_BLOCKS) {
    const int d = b >> 1, half = b & 1;
    float* sp = reinterpret_cast<float*>(smem_raw);
    float* sr = sp + 512;
    const float dt = rbf(expf(bf2f(a.logdt[d])));
    if (tid < 64) {
      const int n = half * 64 + tid;
      const float la = bf2f(a.logA[d * N_ + n]);
      f32x4 pv;
      pv[0] = (-rbf(expf(la)) * dt) * 1.4426950408889634f;
      pv[1] = (bf2f(a.Aim[d * N_ + n]) * dt) * 0.15915494309189535f;
      pv[2] = bf2f(a.Cr[d * N_ + n]) * dt;
      pv[3] = bf2f(a.Ci[d * N_ + n]) * dt;
      *reinterpret_cast<f32x4*>(sp + 4 * tid) = pv;
    }
    __syncthreads();
    const int rc = lane & 15;
    const int kk = lane >> 4;
    f32x4 acc0 = {0, 0, 0, 0}, acc1 = {0, 0, 0, 0}, acc2 = {0, 0, 0, 0}, acc3 = {0, 0, 0, 0};
    const float pA = (float)(16 * rc);
    const float pB = (float)(rc + 1);
#pragma unroll
    for (int i = 0; i < 2; ++i) {
      const f32x4 p0 = *reinterpret_cast<const f32x4*>(sp + 4 * (wid * 16 + 8 * i + kk));
      const f32x4 p1 = *reinterpret_cast<const f32x4*>(sp + 4 * (wid * 16 + 8 * i + 4 + kk));
      const f32x2 cr = {p0[2], p1[2]}, ci = {p0[3], p1[3]};
      f32x2 Ar, Ai, Br, Bi;
      {
        float r0, i0, r1, i1;
        cexpp(p0[0], p0[1], pA, r0, i0); cexpp(p1[0], p1[1], pA, r1, i1);
        Ar = (f32x2){r0, r1}; Ai = (f32x2){i0, i1};
        cexpp(p0[0], p0[1], pB, r0, i0); cexpp(p1[0], p1[1], pB, r1, i1);
        Br = (f32x2){r0, r1}; Bi = (f32x2){i0, i1};
      }
#define KG_STEP(ACC)                                                              \
      {                                                                             \
        const f32x2 tr = __builtin_elementwise_fma(Ar, cr, -(Ai * ci));             \
        const f32x2 ti = __builtin_elementwise_fma(-Ar, ci, -(Ai * cr));            \
        ACC = __builtin_amdgcn_mfma_f32_16x16x4f32(tr[0], Br[0], ACC, 0, 0, 0);     \
        ACC = __builtin_amdgcn_mfma_f32_16x16x4f32(ti[0], Bi[0], ACC, 0, 0, 0);     \
        ACC = __builtin_amdgcn_mfma_f32_16x16x4f32(tr[1], Br[1], ACC, 0, 0, 0);     \
        ACC = __builtin_amdgcn_mfma_f32_16x16x4f32(ti[1], Bi[1], ACC, 0, 0, 0);     \
      }
#define KG_SQ()                                                                   \
      {                                                                             \
        const f32x2 t0 = __builtin_elementwise_fma(Ar, Ar, -(Ai * Ai));             \
        Ai = (Ar + Ar) * Ai; Ar = t0;                                               \
        const f32x2 t1 = __builtin_elementwise_fma(Br, Br, -(Bi * Bi));             \
        Bi = (Br + Br) * Bi; Br = t1;                                               \
      }
      KG_STEP(acc0); KG_SQ();
      KG_STEP(acc1); KG_SQ();
      KG_STEP(acc2); KG_SQ();
      KG_STEP(acc3);
    }
    {
      float* w = sr + wid * 1024;
#pragma unroll
      for (int j = 0; j < 4; ++j) {
        const int e = (4 * kk + j) * 16 + rc;
        w[e] = acc0[j]; w[256 + e] = acc1[j]; w[512 + e] = acc2[j]; w[768 + e] = acc3[j];
      }
    }
    __syncthreads();
    float* Kd = a.Kc + (size_t)(d * 2 + half) * KC_STRIDE;
    {
      Kd[tid + 1] = 2.f * ((sr[tid] + sr[1024 + tid]) + (sr[2048 + tid] + sr[3072 + tid]));
      if (tid >= 128) {
#pragma unroll
        for (int gI = 1; gI < 4; ++gI) {
          const int e = gI * 256 + tid;
          Kd[gI * 128 + tid + 1] = 2.f * ((sr[e] + sr[1024 + e]) + (sr[2048 + e] + sr[3072 + e]));
        }
      }
      if (wid == 0) {
        const float s0 = 2.f * wave_sum(sp[4 * lane + 2]);
        if (lane == 0) Kd[0] = s0;
      }
    }
    return;
  }
  b -= K1_KG_BLOCKS;
  {
    u16* st = reinterpret_cast<u16*>(smem_raw);
    wtile_load(b, tid, st, a.wg, a.wu, a.wd);
    __syncthreads();
    wtile_store(b, tid, st, a.w1i, a.wdf);
  }
}

constexpr int CV_KLEN = 2208;
constexpr int CV_USTRIDE = 2320;
constexpr int CV_OFF_U = 2 * CV_KLEN * 2;
constexpr int CV_LDS_BYTES = CV_OFF_U + 8 * CV_USTRIDE * 2;

template <int MASK>
__device__ __forceinline__ void conv_steps(int s0, int s1, const u16* kh, const u16* kl, const u16* ub,
                                           f32x4 (&acc)[8]) {
  bf16x8 ah = *reinterpret_cast<const bf16x8*>(kh + 32 * s0);
  bf16x8 al = *reinterpret_cast<const bf16x8*>(kl + 32 * s0);
  bf16x8 bv[8];
#pragma unroll
  for (int t = 0; t < 8; ++t) if ((MASK >> t) & 1) bv[t] = *reinterpret_cast<const bf16x8*>(ub + 32 * s0 - 256 * t);
  for (int s = s0; s < s1; ++s) {
    const bf16x8 ah2 = *reinterpret_cast<const bf16x8*>(kh + 32 * s + 32);
    const bf16x8 al2 = *reinterpret_cast<const bf16x8*>(kl + 32 * s + 32);
    bf16x8 bv2[8];
#pragma unroll
    for (int t = 0; t < 8; ++t) if ((MASK >> t) & 1) bv2[t] = *reinterpret_cast<const bf16x8*>(ub + 32 * s + 32 - 256 * t);
    __builtin_amdgcn_sched_barrier(0);
#pragma unroll
    for (int t = 0; t < 8; ++t) {
      if ((MASK >> t) & 1) {
        acc[t] = __builtin_amdgcn_mfma_f32_16x16x32_bf16(ah, bv[t], acc[t], 0, 0, 0);
        acc[t] = __builtin_amdgcn_mfma_f32_16x16x32_bf16(al, bv[t], acc[t], 0, 0, 0);
      }
    }
    __builtin_amdgcn_sched_barrier(0);
    ah = ah2; al = al2;
#pragma unroll
    for (int t = 0; t < 8; ++t) if ((MASK >> t) & 1) bv[t] = bv2[t];
  }
}

struct K2Args {
  const float* Kc; const u16* Dskip; const u16* uT; u16* sT;
};

__device__ __forceinline__ void build_u_chunk(const u32 (&W)[8], u16* dst) {
#pragma unroll
  for (int rho = 0; rho < 8; ++rho) {
    u32 o[4];
#pragma unroll
    for (int k = 0; k < 4; ++k) {
      if ((rho & 1) == 0) {
        const int aI = (rho + 8 - 2 * k) >> 1;
        o[k] = (W[aI] & 0xffffu) | (W[aI - 1] & 0xffff0000u);
      } else {
        const int aI = (rho + 7 - 2 * k) >> 1;
        o[k] = (W[aI] >> 16) | (W[aI] << 16);
      }
    }
    *reinterpret_cast<u32x4*>(dst + rho * CV_USTRIDE) = (u32x4){o[0], o[1], o[2], o[3]};
  }
}

__global__ __launch_bounds__(256, 3) void k2_conv(K2Args a) {
  __shared__ __attribute__((aligned(16))) unsigned char smem[CV_LDS_BYTES];
  u16* sKh = reinterpret_cast<u16*>(smem);
  u16* sKl = sKh + CV_KLEN;
  u16* sU = reinterpret_cast<u16*>(smem + CV_OFF_U);
  const int tid = threadIdx.x, lane = tid & 63, wid = tid >> 6;
  const int d = blockIdx.x >> 1, half = blockIdx.x & 1;
  const u16* ud = a.uT + (size_t)d * LT_;
  const float* Kd = a.Kc + (size_t)(2 * d) * KC_STRIDE;
  const int q = 255 - tid;
  const u32x4 w0 = *reinterpret_cast<const u32x4*>(ud + 8 * q);
  u32x4 w1 = {0, 0, 0, 0};
  if (q < 255) w1 = *reinterpret_cast<const u32x4*>(ud + 8 * q + 8);
  float kv[8];
  {
    const int j0 = 8 * tid;
    const int e = (j0 < 256) ? 0 : (j0 < 512) ? 1 : (j0 < 1024) ? 2 : 3;
    const int base = 128 * e;
    const int hm1 = ((1 << e) >> 1) - 1;
#pragma unroll
    for (int jj = 0; jj < 8; ++jj) {
      const int j = j0 + jj;
      const int pq = (e == 0) ? j : ((j + hm1 + ((j >> e) & 1)) >> e);
      kv[jj] = Kd[base + pq] + Kd[KC_STRIDE + base + pq];
    }
  }
  {
    const u32 W[8] = {w0[0], w0[1], w0[2], w0[3], w1[0], w1[1], w1[2], w1[3]};
    build_u_chunk(W, sU + 8 * tid);
    if (tid == 255) {
      const u32 W2[8] = {0, 0, 0, 0, w0[0], w0[1], w0[2], w0[3]};
      build_u_chunk(W2, sU + 8 * 256);
    }
    if (tid < 8 * 19) {
      const int rho = tid / 19, m = 257 + tid % 19;
      *reinterpret_cast<u32x4*>(sU + rho * CV_USTRIDE + 8 * m) = (u32x4){0, 0, 0, 0};
    } else if (tid >= 160 && tid < 200) {
      const int t = tid - 160;
      const int m = (t % 20 < 16) ? t % 20 : 256 + t % 20;
      u16* dst = (t < 20) ? sKh : sKl;
      *reinterpret_cast<u32x4*>(dst + 8 * m) = (u32x4){0, 0, 0, 0};
    }
    u32x4 ph, pl;
#pragma unroll
    for (int k = 0; k < 4; ++k) {
      const u32 hh = pack2(kv[2 * k], kv[2 * k + 1]);
      ph[k] = hh;
      pl[k] = pack2(kv[2 * k] - lo_f(hh), kv[2 * k + 1] - hi_f(hh));
    }
    *reinterpret_cast<u32x4*>(sKh + 128 + 8 * tid) = ph;
    *reinterpret_cast<u32x4*>(sKl + 128 + 8 * tid) = pl;
  }
  __syncthreads();
  const int r = lane & 15, g = lane >> 4;
  const int ap = (lane & 15) >> 3, rho = lane & 7;
  f32x4 acc[8];
#pragma unroll
  for (int t = 0; t < 8; ++t) acc[t] = (f32x4){0, 0, 0, 0};
  const u16* kh = sKh + 128 + 8 * r + 8 * g;
  const u16* kl = sKl + 128 + 8 * r + 8 * g;
  const u16* ub = sU + rho * CV_USTRIDE + 8 * g - 128 * ap + 2048;
  if (half == 0) {
    if (wid == 0) conv_steps<0xC4>(-4, 9, kh, kl, ub, acc);
    else if (wid == 1) conv_steps<0xC4>(9, 21, kh, kl, ub, acc);
    else if (wid == 2) conv_steps<0xC0>(21, 39, kh, kl, ub, acc);
    else { conv_steps<0xC0>(39, 53, kh, kl, ub, acc); conv_steps<0x80>(53, 61, kh, kl, ub, acc); }
  } else {
    if (wid == 0) conv_steps<0x3B>(-4, 4, kh, kl, ub, acc);
    else if (wid == 1) { conv_steps<0x3B>(4, 5, kh, kl, ub, acc); conv_steps<0x3A>(5, 13, kh, kl, ub, acc); }
    else if (wid == 2) conv_steps<0x38>(13, 25, kh, kl, ub, acc);
    else {
      conv_steps<0x38>(25, 29, kh, kl, ub, acc); conv_steps<0x30>(29, 37, kh, kl, ub, acc);
      conv_steps<0x20>(37, 45, kh, kl, ub, acc);
    }
  }
  const int tmask = half ? 0x3B : 0xC4;
  __syncthreads();
  float* sy = reinterpret_cast<float*>(smem);
#pragma unroll
  for (int t = 0; t < 8; ++t) {
    if ((tmask >> t) & 1) {
#pragma unroll
      for (int j = 0; j < 4; ++j) {
        const int l = 256 * t + 128 * ap + rho + 8 * (4 * g + j);
        sy[wid * L_ + l] = acc[t][j];
      }
    }
  }
  __syncthreads();
  const float dsk = bf2f(a.Dskip[d]);
  if ((tmask >> (q >> 5)) & 1) {
    const int l0 = q * 8;
    float y[8];
#pragma unroll
    for (int hh = 0; hh < 2; ++hh) {
      f32x4 a0 = *reinterpret_cast<const f32x4*>(sy + l0 + 4 * hh);
      f32x4 a1 = *reinterpret_cast<const f32x4*>(sy + L_ + l0 + 4 * hh);
      f32x4 a2 = *reinterpret_cast<const f32x4*>(sy + 2 * L_ + l0 + 4 * hh);
      f32x4 a3 = *reinterpret_cast<const f32x4*>(sy + 3 * L_ + l0 + 4 * hh);
#pragma unroll
      for (int j = 0; j < 4; ++j) y[4 * hh + j] = (a0[j] + a1[j]) + (a2[j] + a3[j]);
    }
    u32 o[4];
#pragma unroll
    for (int k = 0; k < 4; ++k) {
      const float t0 = rbf(lo_f(w0[k]) * dsk), t1 = rbf(hi_f(w0[k]) * dsk);
      o[k] = pack2(rbf(y[2 * k]) + t0, rbf(y[2 * k + 1]) + t1);
    }
    *reinterpret_cast<u32x4*>(a.sT + (size_t)d * LT_ + l0) = (u32x4){o[0], o[1], o[2], o[3]};
  }
}

struct LN2Args {
  const u16* sT; const u16* x; u16* h; u16* xni;
  const u16* wg; const u16* wu; const u16* wd; u16* w1i; u16* wdf;
};
constexpr int LN2_BLOCKS = L_ / 16;
constexpr int LN2_W_BLOCKS = (K1_W1_TILES + K1_WD_TILES) / 4;

__global__ __launch_bounds__(1024) void k_ln2(LN2Args a) {
  __shared__ __attribute__((aligned(16))) u16 ss[16 * 648];
  const int tid = threadIdx.x, lane = tid & 63, wid = tid >> 6;
  const u16* __restrict__ sT = a.sT; const u16* __restrict__ x = a.x;
  u16* __restrict__ h = a.h; u16* __restrict__ xni = a.xni;
  const int row0 = blockIdx.x * 16;
  const int row = row0 + wid;
  u32 xv[5];
  {
    const u32* xr = reinterpret_cast<const u32*>(x + (size_t)row * D_);
#pragma unroll
    for (int i = 0; i < 5; ++i) xv[i] = xr[lane + 64 * i];
  }
  if (tid < D_) {
    const int d = tid;
    const u32x4* src = reinterpret_cast<const u32x4*>(sT + (size_t)d * LT_ + row0);
    const u32x4 a0 = src[0], a1 = src[1];
#pragma unroll
    for (int k = 0; k < 4; ++k) {
      ss[(2 * k) * 648 + d] = (u16)(a0[k] & 0xffffu);
      ss[(2 * k + 1) * 648 + d] = (u16)(a0[k] >> 16);
      ss[(8 + 2 * k) * 648 + d] = (u16)(a1[k] & 0xffffu);
      ss[(8 + 2 * k + 1) * 648 + d] = (u16)(a1[k] >> 16);
    }
  }
  __syncthreads();
  {
    float f[10], o[10];
    u32* hrow = reinterpret_cast<u32*>(h + (size_t)row * D_);
#pragma unroll
    for (int i = 0; i < 5; ++i) {
      const u32 sv = *reinterpret_cast<const u32*>(ss + wid * 648 + 2 * (lane + 64 * i));
      const u32 hv = pack2(lo_f(xv[i]) + lo_f(sv), hi_f(xv[i]) + hi_f(sv));
      hrow[lane + 64 * i] = hv;
      f[2 * i] = lo_f(hv); f[2 * i + 1] = hi_f(hv);
    }
    ln_row(f, o);
    const int mt = row >> 7, r = row & 127;
#pragma unroll
    for (int i = 0; i < 5; ++i) {
      const int c = 2 * (lane + 64 * i);
      const int kt = c >> 6, cc = c & 63;
      const int off = ((mt * 10 + kt) * 128 + r) * 64 + (((cc >> 3) ^ (r & 7)) << 3) + (cc & 7);
      *reinterpret_cast<u32*>(xni + off) = pack2(o[2 * i], o[2 * i + 1]);
    }
  }
}

__device__ __forceinline__ float gelu_tanh(float x) {
  const float c0 = -2.f * 0.7978845608028654f * 1.4426950408889634f;
  const float c1 = c0 * 0.044715f;
  const float e = __builtin_amdgcn_exp2f(x * fmaf(x * x, c1, c0));
  return x * __builtin_amdgcn_rcpf(1.f + e);
}

constexpr int G1_A_BYTES = 16384;
constexpr int G1_B_BYTES = 7 * 4096;
constexpr int G1_STAGE = G1_A_BYTES + G1_B_BYTES;
constexpr int G1_NSTG = 3;

template <int NG>
__device__ __forceinline__ void g1_ldfrag(const unsigned char* sb, int arow, int brow, int cpos,
                                          bf16x8 (&xf)[2], bf16x8 (&wf)[8]) {
#pragma unroll
  for (int m = 0; m < 2; ++m)
    xf[m] = *reinterpret_cast<const bf16x8*>(sb + (arow + m * 16) * 128 + cpos);
#pragma unroll
  for (int n = 0; n < 2 * NG; ++n)
    wf[n] = *reinterpret_cast<const bf16x8*>(sb + G1_A_BYTES + (brow + n * 16) * 128 + cpos);
}
template <int NG>
__device__ __forceinline__ void g1_mma(const bf16x8 (&xf)[2], const bf16x8 (&wf)[8], f32x4 (&acc)[2][8]) {
#pragma unroll
  for (int n = 0; n < 2 * NG; ++n)
#pragma unroll
    for (int m = 0; m < 2; ++m)
      acc[m][n] = __builtin_amdgcn_mfma_f32_16x16x32_bf16(wf[n], xf[m], acc[m][n], 0, 0, 0);
}

template <int NG>
__device__ __forceinline__ void g1_main(unsigned char* smem, const unsigned char* Ag, const unsigned char* Bg,
                                        int g0, int wid, int lane, int arow, int brow, int cpos0, int cpos1,
                                        f32x4 (&acc)[2][8]) {
#define G1_ISSUE(kt, buf)                                                                              \
  {                                                                                                    \
    unsigned char* sb_ = smem + (buf) * G1_STAGE;                                                      \
    _Pragma("unroll") for (int i = 0; i < 2; ++i) {                                                    \
      const int piece = wid + 8 * i;                                                                   \
      __builtin_amdgcn_global_load_lds(                                                                \
          (const __attribute__((address_space(1))) void*)(Ag + (size_t)(kt) * G1_A_BYTES + piece * 1024 + lane * 16), \
          (__attribute__((address_space(3))) void*)(sb_ + piece * 1024), 16, 0, 0);                    \
    }                                                                                                  \
    _Pragma("unroll") for (int i = 0; i < 4; ++i) {                                                    \
      int pb = wid + 8 * i; if (pb >= 28) pb -= 4;                                                     \
      const int gi = pb >> 2;                                                                          \
      const int grp = (g0 + gi < 108) ? g0 + gi : 107;                                                 \
      __builtin_amdgcn_global_load_lds(                                                                \
          (const __attribute__((address_space(1))) void*)(Bg + ((size_t)((kt) * 108 + grp) * 4096) + (pb & 3) * 1024 + lane * 16), \
          (__attribute__((address_space(3))) void*)(sb_ + G1_A_BYTES + pb * 1024), 16, 0, 0);          \
    }                                                                                                  \
  }
  bf16x8 xf0[2], wf0[8], xf1[2], wf1[8];
  G1_ISSUE(0, 0);
  G1_ISSUE(1, 1);
  G1_ISSUE(2, 2);
  asm volatile("s_waitcnt vmcnt(12)" ::: "memory");
  __builtin_amdgcn_s_barrier();
  asm volatile("" ::: "memory");
  g1_ldfrag<NG>(smem, arow, brow, cpos0, xf0, wf0);
#pragma unroll
  for (int kt = 0; kt < 10; ++kt) {
    const int buf = kt % 3;
    const unsigned char* sb = smem + buf * G1_STAGE;
    const int nbuf = (buf == 2) ? 0 : buf + 1;
    g1_ldfrag<NG>(sb, arow, brow, cpos1, xf1, wf1);
    g1_mma<NG>(xf0, wf0, acc);
    if (kt < 9) {
      if (kt < 8) asm volatile("s_waitcnt vmcnt(6) lgkmcnt(0)" ::: "memory");
      else asm volatile("s_waitcnt vmcnt(0) lgkmcnt(0)" ::: "memory");
      __builtin_amdgcn_s_barrier();
      asm volatile("" ::: "memory");
      g1_ldfrag<NG>(smem + nbuf * G1_STAGE, arow, brow, cpos0, xf0, wf0);
      __builtin_amdgcn_sched_barrier(0);
      if (kt + 3 < 10) G1_ISSUE(kt + 3, buf);
    }
    g1_mma<NG>(xf1, wf1, acc);
  }
}

__global__ __launch_bounds__(512) void k3_gemm1(const u16* __restrict__ xni, const u16* __restrict__ w1i,
                                                u16* __restrict__ hmf) {
  __shared__ __attribute__((aligned(16))) unsigned char smem[G1_NSTG * G1_STAGE];
  const int tid = threadIdx.x, lane = tid & 63;
  const int wid = __builtin_amdgcn_readfirstlane(tid >> 6);
  const int wm = wid & 3, wn = wid >> 2;
  const int fr = lane & 15, fq = lane >> 4;
  const int bid = blockIdx.x;
  const int xcd = bid & 7, idx = bid >> 3;
  const int mt = (xcd & 1) * 8 + (idx & 7);
  const int nb = (xcd >> 1) * 4 + (idx >> 3);
  const int g0 = 27 * (nb >> 2) + 7 * (nb & 3);
  const int ng = ((nb & 3) == 3) ? 6 : 7;
  const int gw0 = wn * 4;
  const int gwn = wn ? ng - 4 : 4;
  const unsigned char* Ag = reinterpret_cast<const unsigned char*>(xni) + (size_t)mt * 10 * G1_A_BYTES;
  const unsigned char* Bg = reinterpret_cast<const unsigned char*>(w1i);
  f32x4 acc[2][8];
#pragma unroll
  for (int m = 0; m < 2; ++m)
#pragma unroll
    for (int n = 0; n < 8; ++n) acc[m][n] = (f32x4){0, 0, 0, 0};
  const int swz = fr & 7;
  const int cpos0 = ((0 + fq) ^ swz) << 4, cpos1 = ((4 + fq) ^ swz) << 4;
  const int arow = wm * 32 + fr, brow = gw0 * 32 + fr;
  if (wn == 0) g1_main<4>(smem, Ag, Bg, g0, wid, lane, arow, brow, cpos0, cpos1, acc);
  else g1_main<3>(smem, Ag, Bg, g0, wid, lane, arow, brow, cpos0, cpos1, acc);
#pragma unroll
  for (int m = 0; m < 2; ++m) {
    const int mt16 = mt * 8 + wm * 2 + m;
#pragma unroll
    for (int gI = 0; gI < 4; ++gI) {
      if (gI < gwn) {
        float o[4];
#pragma unroll
        for (int j = 0; j < 4; ++j) {
          const float gv = rbf(acc[m][2 * gI][j]);
          const float uv = rbf(acc[m][2 * gI + 1][j]);
          o[j] = rbf(gelu_tanh(gv)) * uv;
        }
        const int f = 16 * (g0 + gw0 + gI) + 4 * fq;
        const int ks = f >> 5, fq2 = (f & 31) >> 3, j0 = f & 7;
        u16* dst = hmf + ((size_t)(mt16 * 54 + ks) * 64 + fr + 16 * fq2) * 8 + j0;
        *reinterpret_cast<u32x2*>(dst) = (u32x2){pack2(o[0], o[1]), pack2(o[2], o[3])};
      }
    }
  }
}

__global__ __launch_bounds__(256) void k4_gemm2(const u16* __restrict__ hmf, const u16* __restrict__ wdf,
                                                const u16* __restrict__ h, u16* __restrict__ out) {
  __shared__ __attribute__((aligned(16))) float sred[4 * 64 * 84];
  const int tid = threadIdx.x, lane = tid & 63;
  const int wk = __builtin_amdgcn_readfirstlane(tid >> 6);
  const int fr = lane & 15, fq = lane >> 4;
  const int bid = blockIdx.x;
  const int xcd = bid & 7, idx = bid >> 3;
  const int mt = (xcd >> 1) * 8 + (idx & 7);
  const int nb = (xcd & 1) * 4 + (idx >> 3);
  const int ks0 = (wk < 2) ? wk * 14 : 28 + (wk - 2) * 13;
  const int nks = (wk < 2) ? 14 : 13;
  const bf16x8* Ap = reinterpret_cast<const bf16x8*>(hmf) + (size_t)(mt * 4) * 54 * 64 + lane;
  const bf16x8* Bp = reinterpret_cast<const bf16x8*>(wdf) + (size_t)(nb * 5) * 54 * 64 + lane;
  const int rl = tid >> 2, c0 = (tid & 3) * 20;
  const int row = mt * 64 + rl;
  u32x2 hres[5];
  {
    const u16* hr = h + (size_t)row * D_ + nb * 80 + c0;
#pragma unroll
    for (int i = 0; i < 5; ++i) hres[i] = *reinterpret_cast<const u32x2*>(hr + 4 * i);
  }
  f32x4 acc[4][5];
#pragma unroll
  for (int m = 0; m < 4; ++m)
#pragma unroll
    for (int n = 0; n < 5; ++n) acc[m][n] = (f32x4){0, 0, 0, 0};
  bf16x8 xa[4][4], wb[4][5];
#define G2_LOAD(slot, kidx)                                                              \
  {                                                                                      \
    const int kq_ = ks0 + (((kidx) < nks) ? (kidx) : nks - 1);                           \
    _Pragma("unroll") for (int m = 0; m < 4; ++m) xa[slot][m] = Ap[(m * 54 + kq_) * 64];   \
    _Pragma("unroll") for (int n = 0; n < 5; ++n) wb[slot][n] = Bp[(n * 54 + kq_) * 64];   \
  }
#define G2_MMA(slot)                                                                     \
  {                                                                                      \
    _Pragma("unroll") for (int n = 0; n < 5; ++n)                                        \
    _Pragma("unroll") for (int m = 0; m < 4; ++m)                                        \
      acc[m][n] = __builtin_amdgcn_mfma_f32_16x16x32_bf16(wb[slot][n], xa[slot][m], acc[m][n], 0, 0, 0); \
  }
  G2_LOAD(0, 0);
  G2_LOAD(1, 1);
  G2_LOAD(2, 2);
  __builtin_amdgcn_sched_barrier(0);
#pragma unroll
  for (int i = 0; i < 13; ++i) {
    if (i + 3 < 14) G2_LOAD((i + 3) & 3, i + 3);
    __builtin_amdgcn_sched_barrier(0);
    G2_MMA(i & 3);
    __builtin_amdgcn_sched_barrier(0);
  }
  if (nks == 14) G2_MMA(13 & 3);
  float* sw = sred + wk * 64 * 84;
#pragma unroll
  for (int m = 0; m < 4; ++m)
#pragma unroll
    for (int n = 0; n < 5; ++n)
      *reinterpret_cast<f32x4*>(sw + (m * 16 + fr) * 84 + n * 16 + 4 * fq) = acc[m][n];
  __syncthreads();
  {
    u16* orow = out + (size_t)row * D_ + nb * 80 + c0;
#pragma unroll
    for (int i = 0; i < 5; ++i) {
      const float* p = sred + rl * 84 + c0 + 4 * i;
      const f32x4 a0 = *reinterpret_cast<const f32x4*>(p);
      const f32x4 a1 = *reinterpret_cast<const f32x4*>(p + 64 * 84);
      const f32x4 a2 = *reinterpret_cast<const f32x4*>(p + 2 * 64 * 84);
      const f32x4 a3 = *reinterpret_cast<const f32x4*>(p + 3 * 64 * 84);
      float o[4];
#pragma unroll
      for (int j = 0; j < 4; ++j) o[j] = rbf((a0[j] + a1[j]) + (a2[j] + a3[j]));
      const u32 r0 = pack2(lo_f(hres[i][0]) + o[0], hi_f(hres[i][0]) + o[1]);
      const u32 r1 = pack2(lo_f(hres[i][1]) + o[2], hi_f(hres[i][1]) + o[3]);
      *reinterpret_cast<u32x2*>(orow + 4 * i) = (u32x2){r0, r1};
    }
  }
}

extern "C" void kernel_launch(void* const* d_in, const int* in_sizes, int n_in,
                              void* d_out, int out_size, void* d_ws, size_t ws_size,
                              hipStream_t stream) {
  const u16* x = (const u16*)d_in[0];
  const u16* logA = (const u16*)d_in[1];
  const u16* Aim = (const u16*)d_in[2];
  const u16* Cr = (const u16*)d_in[3];
  const u16* Ci = (const u16*)d_in[4];
  const u16* logdt = (const u16*)d_in[5];
  const u16* Dskip = (const u16*)d_in[6];
  const u16* wg = (const u16*)d_in[8];
  const u16* wu = (const u16*)d_in[9];
  const u16* wd = (const u16*)d_in[10];
  u16* out = (u16*)d_out;

  char* ws = (char*)d_ws;
  size_t off = 0;
  auto alloc = [&](size_t bytes) { char* p = ws + off; off += (bytes + 255) & ~(size_t)255; return p; };
  u16* uT = (u16*)alloc((size_t)D_ * LT_ * 2);
  u16* sT = (u16*)alloc((size_t)D_ * LT_ * 2);
  u16* h = (u16*)alloc((size_t)L_ * D_ * 2);
  u16* xni = (u16*)alloc((size_t)L_ * D_ * 2);
  u16* hmf = (u16*)alloc((size_t)L_ * F_ * 2);
  u16* w1i = (u16*)alloc((size_t)2 * F_ * D_ * 2);
  u16* wdf = (u16*)alloc((size_t)D_ * F_ * 2);
  float* Kc = (float*)alloc((size_t)2 * D_ * KC_STRIDE * 4);

  K1Args a;
  a.x = x; a.wg = wg; a.wu = wu; a.wd = wd;
  a.logA = logA; a.Aim = Aim; a.Cr = Cr; a.Ci = Ci; a.logdt = logdt;
  a.uT = uT; a.w1i = w1i; a.wdf = wdf; a.Kc = Kc;
  k1_kernel<<<K1_BLOCKS, 256, 0, stream>>>(a);
  K2Args a2;
  a2.Kc = Kc; a2.Dskip = Dskip; a2.uT = uT; a2.sT = sT;
  k2_conv<<<2 * D_, 256, 0, stream>>>(a2);
  LN2Args a3;
  a3.sT = sT; a3.x = x; a3.h = h; a3.xni = xni; a3.wg = wg; a3.wu = wu; a3.wd = wd; a3.w1i = w1i; a3.wdf = wdf;
  k_ln2<<<LN2_BLOCKS, 1024, 0, stream>>>(a3);
  k3_gemm1<<<256, 512, 0, stream>>>(xni, w1i, hmf);
  k4_gemm2<<<256, 256, 0, stream>>>(hmf, wdf, h, out);
}
```

```cpp
#include <hip/hip_runtime.h>
#include <hip/hip_bf16.h>
#include <stdint.h>

constexpr int L_ = 2048;
constexpr int D_ = 640;
constexpr int N_ = 128;
constexpr int F_ = 1728;
constexpr int LT_ = 2112;
constexpr int KC_STRIDE = 648;

using bf16x8 = __attribute__((ext_vector_type(8))) short;
using f32x4 = __attribute__((ext_vector_type(4))) float;
using u32x4 = __attribute__((ext_vector_type(4))) unsigned int;
using u32x2 = __attribute__((ext_vector_type(2))) unsigned int;
using f32x2 = __attribute__((ext_vector_type(2))) float;
typedef unsigned short u16;
typedef unsigned int u32;

__device__ __forceinline__ float bf2f(u16 v) { return __uint_as_float(((u32)v) << 16); }
__device__ __forceinline__ u16 f2bf(float f) {
  __hip_bfloat16 b = __float2bfloat16(f);
  return *reinterpret_cast<u16*>(&b);
}
__device__ __forceinline__ float rbf(float f) { return bf2f(f2bf(f)); }
__device__ __forceinline__ float lo_f(u32 v) { return __uint_as_float(v << 16); }
__device__ __forceinline__ float hi_f(u32 v) { return __uint_as_float(v & 0xffff0000u); }
__device__ __forceinline__ u32 pack2(float a, float b) { return (u32)f2bf(a) | ((u32)f2bf(b) << 16); }

template <int CTRL, int RMASK>
__device__ __forceinline__ float dpp_add(float v) {
  const int t = __builtin_amdgcn_update_dpp(0, __float_as_int(v), CTRL, RMASK, 0xf, false);
  return v + __int_as_float(t);
}
__device__ __forceinline__ float wave_sum(float v) {
  v = dpp_add<0x111, 0xf>(v);
  v = dpp_add<0x112, 0xf>(v);
  v = dpp_add<0x114, 0xf>(v);
  v = dpp_add<0x118, 0xf>(v);
  v = dpp_add<0x142, 0xa>(v);
  v = dpp_add<0x143, 0xc>(v);
  return __int_as_float(__builtin_amdgcn_readlane(__float_as_int(v), 63));
}

__device__ __forceinline__ void cexpp(float re2, float imr, float p, float& outr, float& outi) {
  const float mag = __builtin_amdgcn_exp2f(re2 * p);
  const float t = __builtin_amdgcn_fractf(imr * p);
  outr = mag * __builtin_amdgcn_cosf(t);
  outi = mag * __builtin_amdgcn_sinf(t);
}

__device__ __forceinline__ void ln_row(const float (&f)[10], float (&o)[10]) {
  float s = 0.f;
#pragma unroll
  for (int i = 0; i < 10; ++i) s += f[i];
  const float mean = wave_sum(s) * (1.f / D_);
  float q = 0.f;
#pragma unroll
  for (int i = 0; i < 10; ++i) { const float t = f[i] - mean; q += t * t; }
  const float var = wave_sum(q) * (1.f / D_);
  const float mb = rbf(mean);
  const float sd = rbf(sqrtf(rbf(rbf(var) + 1e-5f)));
  const float rs = 1.f / sd;
#pragma unroll
  for (int i = 0; i < 10; ++i) {
    const float t = rbf(f[i] - mb);
    const float q0 = t * rs;
    o[i] = fmaf(fmaf(-q0, sd, t), rs, q0);
  }
}

constexpr int K1_KG_BLOCKS = 2 * D_;
constexpr int K1_LN_BLOCKS = L_ / 16;
constexpr int K1_W1_TILES = (D_ / 64) * (F_ / 64);
constexpr int K1_WD_TILES = (F_ / 64) * (D_ / 64);
constexpr int K1_W_BLOCKS = (K1_W1_TILES + K1_WD_TILES);
constexpr int K1_BLOCKS = K1_KG_BLOCKS + K1_LN_BLOCKS + K1_W_BLOCKS;

__device__ __forceinline__ void wtile_load(int tile, int tid, u16* st, const u16* wg, const u16* wu, const u16* wd) {
  if (tile < K1_W1_TILES) {
    const int kt = tile / 27, cc0 = (tile % 27) * 64;
#pragma unroll
    for (int i = 0; i < 4; ++i) {
      const int idx = tid + 256 * i;
      const int mat = idx >> 9, r = (idx >> 3) & 63, c = (idx & 7) * 8;
      const u16* src = mat ? wu : wg;
      const u32x4 v = *reinterpret_cast<const u32x4*>(src + (size_t)(kt * 64 + r) * F_ + cc0 + c);
      *reinterpret_cast<u32x4*>(st + (mat * 64 + r) * 72 + c) = v;
    }
  } else {
    const int t2 = tile - K1_W1_TILES;
    const int k0 = (t2 / 10) * 64, n0 = (t2 % 10) * 64;
#pragma unroll
    for (int i = 0; i < 2; ++i) {
      const int idx = tid + 256 * i;
      const int r = idx >> 3, c = (idx & 7) * 8;
      const u32x4 v = *reinterpret_cast<const u32x4*>(wd + (size_t)(k0 + r) * D_ + n0 + c);
      *reinterpret_cast<u32x4*>(st + r * 72 + c) = v;
    }
  }
}
__device__ __forceinline__ void wtile_store(int tile, int tid, const u16* st, u16* w1i, u16* wdf) {
  if (tile < K1_W1_TILES) {
    const int kt = tile / 27, cc0 = (tile % 27) * 64;
#pragma unroll
    for (int i = 0; i < 4; ++i) {
      const int idx = tid + 256 * i;
      const int gl = idx >> 8, rr = (idx >> 3) & 31, pc = idx & 7;
      const int ch = pc ^ (rr & 7);
      const int mat = rr >> 4, col = gl * 16 + (rr & 15);
      u32 p[4];
#pragma unroll
      for (int j = 0; j < 4; ++j)
        p[j] = (u32)st[(mat * 64 + ch * 8 + 2 * j) * 72 + col] | ((u32)st[(mat * 64 + ch * 8 + 2 * j + 1) * 72 + col] << 16);
      const int grp = (cc0 >> 4) + gl;
      u16* dst = w1i + ((size_t)(kt * 108 + grp) * 2048) + rr * 64 + pc * 8;
      *reinterpret_cast<u32x4*>(dst) = (u32x4){p[0], p[1], p[2], p[3]};
    }
  } else {
    const int t2 = tile - K1_W1_TILES;
    const int k0 = (t2 / 10) * 64, n0 = (t2 % 10) * 64;
#pragma unroll
    for (int i = 0; i < 2; ++i) {
      const int idx = tid + 256 * i;
      const int ntl = idx >> 7, ksl = (idx >> 6) & 1, ln = idx & 63;
      const int fr = ln & 15, fq = ln >> 4;
      const int kb = ksl * 32 + fq * 8, col = ntl * 16 + fr;
      u32 p[4];
#pragma unroll
      for (int j = 0; j < 4; ++j)
        p[j] = (u32)st[(kb + 2 * j) * 72 + col] | ((u32)st[(kb + 2 * j + 1) * 72 + col] << 16);
      const int nt = (n0 >> 4) + ntl, ks = (k0 >> 5) + ksl;
      u16* dst = wdf + ((size_t)(nt * 54 + ks) * 64 + ln) * 8;
      *reinterpret_cast<u32x4*>(dst) = (u32x4){p[0], p[1], p[2], p[3]};
    }
  }
}

struct K1Args {
  const u16* x; const u16* wg; const u16* wu; const u16* wd;
  const u16* logA; const u16* Aim; const u16* Cr; const u16* Ci; const u16* logdt;
  u16* uT; u16* w1i; u16* wdf; float* Kc;
};

__global__ __launch_bounds__(256) void k1_kernel(K1Args a) {
  __shared__ __attribute__((aligned(16))) unsigned char smem_raw[16 * 648 * 2];
  const int tid = threadIdx.x, lane = tid & 63, wid = tid >> 6;
  int b = blockIdx.x;
  if (b < K1_LN_BLOCKS) {
    u16* su = reinterpret_cast<u16*>(smem_raw);
    const int row0 = b * 16;
    u32 v[4][5];
#pragma unroll
    for (int rr = 0; rr < 4; ++rr) {
      const u32* xr = reinterpret_cast<const u32*>(a.x + (size_t)(row0 + wid * 4 + rr) * D_);
#pragma unroll
      for (int i = 0; i < 5; ++i) v[rr][i] = xr[lane + 64 * i];
    }
#pragma unroll
    for (int rr = 0; rr < 4; ++rr) {
      float f[10], o[10];
#pragma unroll
      for (int i = 0; i < 5; ++i) { f[2 * i] = lo_f(v[rr][i]); f[2 * i + 1] = hi_f(v[rr][i]); }
      ln_row(f, o);
#pragma unroll
      for (int i = 0; i < 5; ++i)
        *reinterpret_cast<u32*>(su + (wid * 4 + rr) * 648 + 2 * (lane + 64 * i)) = pack2(o[2 * i], o[2 * i + 1]);
    }
    __syncthreads();
#pragma unroll
    for (int it = 0; it < 3; ++it) {
      const int d = tid + 256 * it;
      if (d < D_) {
        u32 pu[8];
#pragma unroll
        for (int r = 0; r < 8; ++r)
          pu[r] = (u32)su[(2 * r) * 648 + d] | ((u32)su[(2 * r + 1) * 648 + d] << 16);
        u32x4* du = reinterpret_cast<u32x4*>(a.uT + (size_t)d * LT_ + row0);
        du[0] = (u32x4){pu[0], pu[1], pu[2], pu[3]};
        du[1] = (u32x4){pu[4], pu[5], pu[6], pu[7]};
      }
    }
    return;
  }
  b -= K1_LN_BLOCKS;
  if (b < K1_KG_BLOCKS) {
    const int d = b >> 1, half = b & 1;
    float* sp = reinterpret_cast<float*>(smem_raw);
    float* sr = sp + 512;
    const float dt = rbf(expf(bf2f(a.logdt[d])));
    if (tid < 64) {
      const int n = half * 64 + tid;
      const float la = bf2f(a.logA[d * N_ + n]);
      f32x4 pv;
      pv[0] = (-rbf(expf(la)) * dt) * 1.4426950408889634f;
      pv[1] = (bf2f(a.Aim[d * N_ + n]) * dt) * 0.15915494309189535f;
      pv[2] = bf2f(a.Cr[d * N_ + n]) * dt;
      pv[3] = bf2f(a.Ci[d * N_ + n]) * dt;
      *reinterpret_cast<f32x4*>(sp + 4 * tid) = pv;
    }
    __syncthreads();
    const int rc = lane & 15;
    const int kk = lane >> 4;
    f32x4 acc0 = {0, 0, 0, 0}, acc1 = {0, 0, 0, 0}, acc2 = {0, 0, 0, 0}, acc3 = {0, 0, 0, 0};
    const float pA = (float)(16 * rc);
    const float pB = (float)(rc + 1);
#pragma unroll
    for (int i = 0; i < 2; ++i) {
      const f32x4 p0 = *reinterpret_cast<const f32x4*>(sp + 4 * (wid * 16 + 8 * i + kk));
      const f32x4 p1 = *reinterpret_cast<const f32x4*>(sp + 4 * (wid * 16 + 8 * i + 4 + kk));
      const f32x2 cr = {p0[2], p1[2]}, ci = {p0[3], p1[3]};
      f32x2 Ar, Ai, Br, Bi;
      {
        float r0, i0, r1, i1;
        cexpp(p0[0], p0[1], pA, r0, i0); cexpp(p1[0], p1[1], pA, r1, i1);
        Ar = (f32x2){r0, r1}; Ai = (f32x2){i0, i1};
        cexpp(p0[0], p0[1], pB, r0, i0); cexpp(p1[0], p1[1], pB, r1, i1);
        Br = (f32x2){r0, r1}; Bi = (f32x2){i0, i1};
      }
#define KG_STEP(ACC)                                                              \
      {                                                                             \
        const f32x2 tr = __builtin_elementwise_fma(Ar, cr, -(Ai * ci));             \
        const f32x2 ti = __builtin_elementwise_fma(-Ar, ci, -(Ai * cr));            \
        ACC = __builtin_amdgcn_mfma_f32_16x16x4f32(tr[0], Br[0], ACC, 0, 0, 0);     \
        ACC = __builtin_amdgcn_mfma_f32_16x16x4f32(ti[0], Bi[0], ACC, 0, 0, 0);     \
        ACC = __builtin_amdgcn_mfma_f32_16x16x4f32(tr[1], Br[1], ACC, 0, 0, 0);     \
        ACC = __builtin_amdgcn_mfma_f32_16x16x4f32(ti[1], Bi[1], ACC, 0, 0, 0);     \
      }
#define KG_SQ()                                                                   \
      {                                                                             \
        const f32x2 t0 = __builtin_elementwise_fma(Ar, Ar, -(Ai * Ai));             \
        Ai = (Ar + Ar) * Ai; Ar = t0;                                               \
        const f32x2 t1 = __builtin_elementwise_fma(Br, Br, -(Bi * Bi));             \
        Bi = (Br + Br) * Bi; Br = t1;                                               \
      }
      KG_STEP(acc0); KG_SQ();
      KG_STEP(acc1); KG_SQ();
      KG_STEP(acc2); KG_SQ();
      KG_STEP(acc3);
    }
    {
      float* w = sr + wid * 1024;
#pragma unroll
      for (int j = 0; j < 4; ++j) {
        const int e = (4 * kk + j) * 16 + rc;
        w[e] = acc0[j]; w[256 + e] = acc1[j]; w[512 + e] = acc2[j]; w[768 + e] = acc3[j];
      }
    }
    __syncthreads();
    float* Kd = a.Kc + (size_t)(d * 2 + half) * KC_STRIDE;
    {
      Kd[tid + 1] = 2.f * ((sr[tid] + sr[1024 + tid]) + (sr[2048 + tid] + sr[3072 + tid]));
      if (tid >= 128) {
#pragma unroll
        for (int gI = 1; gI < 4; ++gI) {
          const int e = gI * 256 + tid;
          Kd[gI * 128 + tid + 1] = 2.f * ((sr[e] + sr[1024 + e]) + (sr[2048 + e] + sr[3072 + e]));
        }
      }
      if (wid == 0) {
        const float s0 = 2.f * wave_sum(sp[4 * lane + 2]);
        if (lane == 0) Kd[0] = s0;
      }
    }
    return;
  }
  b -= K1_KG_BLOCKS;
  {
    u16* st = reinterpret_cast<u16*>(smem_raw);
    wtile_load(b, tid, st, a.wg, a.wu, a.wd);
    __syncthreads();
    wtile_store(b, tid, st, a.w1i, a.wdf);
  }
}

constexpr int CV_KLEN = 2208;
constexpr int CV_USTRIDE = 2320;
constexpr int CV_OFF_U = 2 * CV_KLEN * 2;
constexpr int CV_LDS_BYTES = CV_OFF_U + 8 * CV_USTRIDE * 2;

template <int TMIN>
__device__ __forceinline__ void conv_steps(int s0, int s1, const u16* kh, const u16* kl, const u16* ub,
                                           f32x4 (&acc)[8]) {
  bf16x8 ah = *reinterpret_cast<const bf16x8*>(kh + 32 * s0);
  bf16x8 al = *reinterpret_cast<const bf16x8*>(kl + 32 * s0);
  bf16x8 bv[8];
#pragma unroll
  for (int t = TMIN; t < 8; ++t) bv[t] = *reinterpret_cast<const bf16x8*>(ub + 32 * s0 - 256 * t);
  for (int s = s0; s < s1; ++s) {
    const bf16x8 ah2 = *reinterpret_cast<const bf16x8*>(kh + 32 * s + 32);
    const bf16x8 al2 = *reinterpret_cast<const bf16x8*>(kl + 32 * s + 32);
    bf16x8 bv2[8];
#pragma unroll
    for (int t = TMIN; t < 8; ++t) bv2[t] = *reinterpret_cast<const bf16x8*>(ub + 32 * s + 32 - 256 * t);
    __builtin_amdgcn_sched_barrier(0);
#pragma unroll
    for (int t = TMIN; t < 8; ++t) {
      acc[t] = __builtin_amdgcn_mfma_f32_16x16x32_bf16(ah, bv[t], acc[t], 0, 0, 0);
      acc[t] = __builtin_amdgcn_mfma_f32_16x16x32_bf16(al, bv[t], acc[t], 0, 0, 0);
    }
    __builtin_amdgcn_sched_barrier(0);
    ah = ah2; al = al2;
#pragma unroll
    for (int t = TMIN; t < 8; ++t) bv[t] = bv2[t];
  }
}

struct K2Args {
  const float* Kc; const u16* Dskip; const u16* uT; u16* sT;
};

__device__ __forceinline__ void build_u_chunk(const u32 (&W)[8], u16* dst) {
#pragma unroll
  for (int rho = 0; rho < 8; ++rho) {
    u32 o[4];
#pragma unroll
    for (int k = 0; k < 4; ++k) {
      if ((rho & 1) == 0) {
        const int aI = (rho + 8 - 2 * k) >> 1;
        o[k] = (W[aI] & 0xffffu) | (W[aI - 1] & 0xffff0000u);
      } else {
        const int aI = (rho + 7 - 2 * k) >> 1;
        o[k] = (W[aI] >> 16) | (W[aI] << 16);
      }
    }
    *reinterpret_cast<u32x4*>(dst + rho * CV_USTRIDE) = (u32x4){o[0], o[1], o[2], o[3]};
  }
}

__global__ __launch_bounds__(256, 3) void k2_conv(K2Args a) {
  __shared__ __attribute__((aligned(16))) unsigned char smem[CV_LDS_BYTES];
  u16* sKh = reinterpret_cast<u16*>(smem);
  u16* sKl = sKh + CV_KLEN;
  u16* sU = reinterpret_cast<u16*>(smem + CV_OFF_U);
  const int tid = threadIdx.x, lane = tid & 63, wid = tid >> 6;
  const int d = blockIdx.x;
  const u16* ud = a.uT + (size_t)d * LT_;
  const float* Kd = a.Kc + (size_t)(2 * d) * KC_STRIDE;
  const int q = 255 - tid;
  const u32x4 w0 = *reinterpret_cast<const u32x4*>(ud + 8 * q);
  u32x4 w1 = {0, 0, 0, 0};
  if (q < 255) w1 = *reinterpret_cast<const u32x4*>(ud + 8 * q + 8);
  float kv[8];
  {
    const int j0 = 8 * tid;
    const int e = (j0 < 256) ? 0 : (j0 < 512) ? 1 : (j0 < 1024) ? 2 : 3;
    const int base = 128 * e;
    const int hm1 = ((1 << e) >> 1) - 1;
#pragma unroll
    for (int jj = 0; jj < 8; ++jj) {
      const int j = j0 + jj;
      const int pq = (e == 0) ? j : ((j + hm1 + ((j >> e) & 1)) >> e);
      kv[jj] = Kd[base + pq] + Kd[KC_STRIDE + base + pq];
    }
  }
  {
    const u32 W[8] = {w0[0], w0[1], w0[2], w0[3], w1[0], w1[1], w1[2], w1[3]};
    build_u_chunk(W, sU + 8 * tid);
    if (tid == 255) {
      const u32 W2[8] = {0, 0, 0, 0, w0[0], w0[1], w0[2], w0[3]};
      build_u_chunk(W2, sU + 8 * 256);
    }
    if (tid < 8 * 19) {
      const int rho = tid / 19, m = 257 + tid % 19;
      *reinterpret_cast<u32x4*>(sU + rho * CV_USTRIDE + 8 * m) = (u32x4){0, 0, 0, 0};
    } else if (tid >= 160 && tid < 200) {
      const int t = tid - 160;
      const int m = (t % 20 < 16) ? t % 20 : 256 + t % 20;
      u16* dst = (t < 20) ? sKh : sKl;
      *reinterpret_cast<u32x4*>(dst + 8 * m) = (u32x4){0, 0, 0, 0};
    }
    u32x4 ph, pl;
#pragma unroll
    for (int k = 0; k < 4; ++k) {
      const u32 hh = pack2(kv[2 * k], kv[2 * k + 1]);
      ph[k] = hh;
      pl[k] = pack2(kv[2 * k] - lo_f(hh), kv[2 * k + 1] - hi_f(hh));
    }
    *reinterpret_cast<u32x4*>(sKh + 128 + 8 * tid) = ph;
    *reinterpret_cast<u32x4*>(sKl + 128 + 8 * tid) = pl;
  }
  __syncthreads();
  const int r = lane & 15, g = lane >> 4;
  const int ap = (lane & 15) >> 3, rho = lane & 7;
  f32x4 acc[8];
#pragma unroll
  for (int t = 0; t < 8; ++t) acc[t] = (f32x4){0, 0, 0, 0};
  const u16* kh = sKh + 128 + 8 * r + 8 * g;
  const u16* kl = sKl + 128 + 8 * r + 8 * g;
  const u16* ub = sU + rho * CV_USTRIDE + 8 * g - 128 * ap + 2048;
  if (wid == 0) {
    conv_steps<0>(-4, 5, kh, kl, ub, acc);
  } else if (wid == 1) {
    conv_steps<1>(5, 13, kh, kl, ub, acc);
    conv_steps<2>(13, 16, kh, kl, ub, acc);
  } else if (wid == 2) {
    conv_steps<2>(16, 21, kh, kl, ub, acc);
    conv_steps<3>(21, 29, kh, kl, ub, acc);
    conv_steps<4>(29, 30, kh, kl, ub, acc);
  } else {
    conv_steps<4>(30, 37, kh, kl, ub, acc);
    conv_steps<5>(37, 45, kh, kl, ub, acc);
    conv_steps<6>(45, 53, kh, kl, ub, acc);
    conv_steps<7>(53, 61, kh, kl, ub, acc);
  }
  __syncthreads();
  float* sy = reinterpret_cast<float*>(smem);
#pragma unroll
  for (int t = 0; t < 8; ++t) {
#pragma unroll
    for (int j = 0; j < 4; ++j) {
      const int l = 256 * t + 128 * ap + rho + 8 * (4 * g + j);
      sy[wid * L_ + l] = acc[t][j];
    }
  }
  __syncthreads();
  const float dsk = bf2f(a.Dskip[d]);
  {
    const int l0 = q * 8;
    float y[8];
#pragma unroll
    for (int hh = 0; hh < 2; ++hh) {
      f32x4 a0 = *reinterpret_cast<const f32x4*>(sy + l0 + 4 * hh);
      f32x4 a1 = *reinterpret_cast<const f32x4*>(sy + L_ + l0 + 4 * hh);
      f32x4 a2 = *reinterpret_cast<const f32x4*>(sy + 2 * L_ + l0 + 4 * hh);
      f32x4 a3 = *reinterpret_cast<const f32x4*>(sy + 3 * L_ + l0 + 4 * hh);
#pragma unroll
      for (int j = 0; j < 4; ++j) y[4 * hh + j] = (a0[j] + a1[j]) + (a2[j] + a3[j]);
    }
    u32 o[4];
#pragma unroll
    for (int k = 0; k < 4; ++k) {
      const float t0 = rbf(lo_f(w0[k]) * dsk), t1 = rbf(hi_f(w0[k]) * dsk);
      o[k] = pack2(rbf(y[2 * k]) + t0, rbf(y[2 * k + 1]) + t1);
    }
    *reinterpret_cast<u32x4*>(a.sT + (size_t)d * LT_ + l0) = (u32x4){o[0], o[1], o[2], o[3]};
  }
}

struct LN2Args {
  const u16* sT; const u16* x; u16* h; u16* xni;
};
constexpr int LN2_BLOCKS = L_ / 16;

__global__ __launch_bounds__(1024) void k_ln2(LN2Args a) {
  __shared__ __attribute__((aligned(16))) u16 ss[16 * 648];
  const int tid = threadIdx.x, lane = tid & 63, wid = tid >> 6;
  const u16* __restrict__ sT = a.sT; const u16* __restrict__ x = a.x;
  u16* __restrict__ h = a.h; u16* __restrict__ xni = a.xni;
  const int row0 = blockIdx.x * 16;
  const int row = row0 + wid;
  u32 xv[5];
  {
    const u32* xr = reinterpret_cast<const u32*>(x + (size_t)row * D_);
#pragma unroll
    for (int i = 0; i < 5; ++i) xv[i] = xr[lane + 64 * i];
  }
  if (tid < D_) {
    const int d = tid;
    const u32x4* src = reinterpret_cast<const u32x4*>(sT + (size_t)d * LT_ + row0);
    const u32x4 a0 = src[0], a1 = src[1];
#pragma unroll
    for (int k = 0; k < 4; ++k) {
      ss[(2 * k) * 648 + d] = (u16)(a0[k] & 0xffffu);
      ss[(2 * k + 1) * 648 + d] = (u16)(a0[k] >> 16);
      ss[(8 + 2 * k) * 648 + d] = (u16)(a1[k] & 0xffffu);
      ss[(8 + 2 * k + 1) * 648 + d] = (u16)(a1[k] >> 16);
    }
  }
  __syncthreads();
  {
    float f[10], o[10];
    u32* hrow = reinterpret_cast<u32*>(h + (size_t)row * D_);
#pragma unroll
    for (int i = 0; i < 5; ++i) {
      const u32 sv = *reinterpret_cast<const u32*>(ss + wid * 648 + 2 * (lane + 64 * i));
      const u32 hv = pack2(lo_f(xv[i]) + lo_f(sv), hi_f(xv[i]) + hi_f(sv));
      hrow[lane + 64 * i] = hv;
      f[2 * i] = lo_f(hv); f[2 * i + 1] = hi_f(hv);
    }
    ln_row(f, o);
    const int mt = row >> 7, r = row & 127;
#pragma unroll
    for (int i = 0; i < 5; ++i) {
      const int c = 2 * (lane + 64 * i);
      const int kt = c >> 6, cc = c & 63;
      const int off = ((mt * 10 + kt) * 128 + r) * 64 + (((cc >> 3) ^ (r & 7)) << 3) + (cc & 7);
      *reinterpret_cast<u32*>(xni + off) = pack2(o[2 * i], o[2 * i + 1]);
    }
  }
}

__device__ __forceinline__ float gelu_tanh(float x) {
  const float c0 = -2.f * 0.7978845608028654f * 1.4426950408889634f;
  const float c1 = c0 * 0.044715f;
  const float e = __builtin_amdgcn_exp2f(x * fmaf(x * x, c1, c0));
  return x * __builtin_amdgcn_rcpf(1.f + e);
}

constexpr int G1_A_BYTES = 16384;
constexpr int G1_B_BYTES = 7 * 4096;
constexpr int G1_STAGE = G1_A_BYTES + G1_B_BYTES;
constexpr int G1_NSTG = 3;

template <int NG>
__device__ __forceinline__ void g1_ldfrag(const unsigned char* sb, int arow, int brow, int cpos,
                                          bf16x8 (&xf)[2], bf16x8 (&wf)[8]) {
#pragma unroll
  for (int m = 0; m < 2; ++m)
    xf[m] = *reinterpret_cast<const bf16x8*>(sb + (arow + m * 16) * 128 + cpos);
#pragma unroll
  for (int n = 0; n < 2 * NG; ++n)
    wf[n] = *reinterpret_cast<const bf16x8*>(sb + G1_A_BYTES + (brow + n * 16) * 128 + cpos);
}
template <int NG>
__device__ __forceinline__ void g1_mma(const bf16x8 (&xf)[2], const bf16x8 (&wf)[8], f32x4 (&acc)[2][8]) {
#pragma unroll
  for (int n = 0; n < 2 * NG; ++n)
#pragma unroll
    for (int m = 0; m < 2; ++m)
      acc[m][n] = __builtin_amdgcn_mfma_f32_16x16x32_bf16(wf[n], xf[m], acc[m][n], 0, 0, 0);
}

template <int NG>
__device__ __forceinline__ void g1_main(unsigned char* smem, const unsigned char* Ag, const unsigned char* Bg,
                                        int g0, int wid, int lane, int arow, int brow, int cpos0, int cpos1,
                                        f32x4 (&acc)[2][8]) {
#define G1_ISSUE(kt, buf)                                                                              \
  {                                                                                                    \
    unsigned char* sb_ = smem + (buf) * G1_STAGE;                                                      \
    _Pragma("unroll") for (int i = 0; i < 2; ++i) {                                                    \
      const int piece = wid + 8 * i;                                                                   \
      __builtin_amdgcn_global_load_lds(                                                                \
          (const __attribute__((address_space(1))) void*)(Ag + (size_t)(kt) * G1_A_BYTES + piece * 1024 + lane * 16), \
          (__attribute__((address_space(3))) void*)(sb_ + piece * 1024), 16, 0, 0);                    \
    }                                                                                                  \
    _Pragma("unroll") for (int i = 0; i < 4; ++i) {                                                    \
      int pb = wid + 8 * i; if (pb >= 28) pb -= 4;                                                     \
      const int gi = pb >> 2;                                                                          \
      const int grp = (g0 + gi < 108) ? g0 + gi : 107;                                                 \
      __builtin_amdgcn_global_load_lds(                                                                \
          (const __attribute__((address_space(1))) void*)(Bg + ((size_t)((kt) * 108 + grp) * 4096) + (pb & 3) * 1024 + lane * 16), \
          (__attribute__((address_space(3))) void*)(sb_ + G1_A_BYTES + pb * 1024), 16, 0, 0);          \
    }                                                                                                  \
  }
  bf16x8 xf0[2], wf0[8], xf1[2], wf1[8];
  G1_ISSUE(0, 0);
  G1_ISSUE(1, 1);
  G1_ISSUE(2, 2);
  asm volatile("s_waitcnt vmcnt(12)" ::: "memory");
  __builtin_amdgcn_s_barrier();
  asm volatile("" ::: "memory");
  g1_ldfrag<NG>(smem, arow, brow, cpos0, xf0, wf0);
#pragma unroll
  for (int kt = 0; kt < 10; ++kt) {
    const int buf = kt % 3;
    const unsigned char* sb = smem + buf * G1_STAGE;
    const int nbuf = (buf == 2) ? 0 : buf + 1;
    g1_ldfrag<NG>(sb, arow, brow, cpos1, xf1, wf1);
    g1_mma<NG>(xf0, wf0, acc);
    if (kt < 9) {
      if (kt < 8) asm volatile("s_waitcnt vmcnt(6) lgkmcnt(0)" ::: "memory");
      else asm volatile("s_waitcnt vmcnt(0) lgkmcnt(0)" ::: "memory");
      __builtin_amdgcn_s_barrier();
      asm volatile("" ::: "memory");
      g1_ldfrag<NG>(smem + nbuf * G1_STAGE, arow, brow, cpos0, xf0, wf0);
      __builtin_amdgcn_sched_barrier(0);
      if (kt + 3 < 10) G1_ISSUE(kt + 3, buf);
    }
    g1_mma<NG>(xf1, wf1, acc);
  }
}

__global__ __launch_bounds__(512) void k3_gemm1(const u16* __restrict__ xni, const u16* __restrict__ w1i,
                                                u16* __restrict__ hmf) {
  __shared__ __attribute__((aligned(16))) unsigned char smem[G1_NSTG * G1_STAGE];
  const int tid = threadIdx.x, lane = tid & 63;
  const int wid = __builtin_amdgcn_readfirstlane(tid >> 6);
  const int wm = wid & 3, wn = wid >> 2;
  const int fr = lane & 15, fq = lane >> 4;
  const int bid = blockIdx.x;
  const int xcd = bid & 7, idx = bid >> 3;
  const int mt = (xcd & 1) * 8 + (idx & 7);
  const int nb = (xcd >> 1) * 4 + (idx >> 3);
  const int g0 = 27 * (nb >> 2) + 7 * (nb & 3);
  const int ng = ((nb & 3) == 3) ? 6 : 7;
  const int gw0 = wn * 4;
  const int gwn = wn ? ng - 4 : 4;
  const unsigned char* Ag = reinterpret_cast<const unsigned char*>(xni) + (size_t)mt * 10 * G1_A_BYTES;
  const unsigned char* Bg = reinterpret_cast<const unsigned char*>(w1i);
  f32x4 acc[2][8];
#pragma unroll
  for (int m = 0; m < 2; ++m)
#pragma unroll
    for (int n = 0; n < 8; ++n) acc[m][n] = (f32x4){0, 0, 0, 0};
  const int swz = fr & 7;
  const int cpos0 = ((0 + fq) ^ swz) << 4, cpos1 = ((4 + fq) ^ swz) << 4;
  const int arow = wm * 32 + fr, brow = gw0 * 32 + fr;
  if (wn == 0) g1_main<4>(smem, Ag, Bg, g0, wid, lane, arow, brow, cpos0, cpos1, acc);
  else g1_main<3>(smem, Ag, Bg, g0, wid, lane, arow, brow, cpos0, cpos1, acc);
#pragma unroll
  for (int m = 0; m < 2; ++m) {
    const int mt16 = mt * 8 + wm * 2 + m;
#pragma unroll
    for (int gI = 0; gI < 4; ++gI) {
      if (gI < gwn) {
        float o[4];
#pragma unroll
        for (int j = 0; j < 4; ++j) {
          const float gv = rbf(acc[m][2 * gI][j]);
          const float uv = rbf(acc[m][2 * gI + 1][j]);
          o[j] = rbf(gelu_tanh(gv)) * uv;
        }
        const int f = 16 * (g0 + gw0 + gI) + 4 * fq;
        const int ks = f >> 5, fq2 = (f & 31) >> 3, j0 = f & 7;
        u16* dst = hmf + ((size_t)(mt16 * 54 + ks) * 64 + fr + 16 * fq2) * 8 + j0;
        *reinterpret_cast<u32x2*>(dst) = (u32x2){pack2(o[0], o[1]), pack2(o[2], o[3])};
      }
    }
  }
}

__global__ __launch_bounds__(256) void k4_gemm2(const u16* __restrict__ hmf, const u16* __restrict__ wdf,
                                                const u16* __restrict__ h, u16* __restrict__ out) {
  __shared__ __attribute__((aligned(16))) float sred[4 * 64 * 84];
  const int tid = threadIdx.x, lane = tid & 63;
  const int wk = __builtin_amdgcn_readfirstlane(tid >> 6);
  const int fr = lane & 15, fq = lane >> 4;
  const int bid = blockIdx.x;
  const int xcd = bid & 7, idx = bid >> 3;
  const int mt = (xcd >> 1) * 8 + (idx & 7);
  const int nb = (xcd & 1) * 4 + (idx >> 3);
  const int ks0 = (wk < 2) ? wk * 14 : 28 + (wk - 2) * 13;
  const int nks = (wk < 2) ? 14 : 13;
  const bf16x8* Ap = reinterpret_cast<const bf16x8*>(hmf) + (size_t)(mt * 4) * 54 * 64 + lane;
  const bf16x8* Bp = reinterpret_cast<const bf16x8*>(wdf) + (size_t)(nb * 5) * 54 * 64 + lane;
  const int rl = tid >> 2, c0 = (tid & 3) * 20;
  const int row = mt * 64 + rl;
  u32x2 hres[5];
  {
    const u16* hr = h + (size_t)row * D_ + nb * 80 + c0;
#pragma unroll
    for (int i = 0; i < 5; ++i) hres[i] = *reinterpret_cast<const u32x2*>(hr + 4 * i);
  }
  f32x4 acc[4][5];
#pragma unroll
  for (int m = 0; m < 4; ++m)
#pragma unroll
    for (int n = 0; n < 5; ++n) acc[m][n] = (f32x4){0, 0, 0, 0};
  bf16x8 xa[4][4], wb[4][5];
#define G2_LOAD(slot, kidx)                                                              \
  {                                                                                      \
    const int kq_ = ks0 + (((kidx) < nks) ? (kidx) : nks - 1);                           \
    _Pragma("unroll") for (int m = 0; m < 4; ++m) xa[slot][m] = Ap[(m * 54 + kq_) * 64];   \
    _Pragma("unroll") for (int n = 0; n < 5; ++n) wb[slot][n] = Bp[(n * 54 + kq_) * 64];   \
  }
#define G2_MMA(slot)                                                                     \
  {                                                                                      \
    _Pragma("unroll") for (int n = 0; n < 5; ++n)                                        \
    _Pragma("unroll") for (int m = 0; m < 4; ++m)                                        \
      acc[m][n] = __builtin_amdgcn_mfma_f32_16x16x32_bf16(wb[slot][n], xa[slot][m], acc[m][n], 0, 0, 0); \
  }
  G2_LOAD(0, 0);
  G2_LOAD(1, 1);
  G2_LOAD(2, 2);
  __builtin_amdgcn_sched_barrier(0);
#pragma unroll
  for (int i = 0; i < 13; ++i) {
    if (i + 3 < 14) G2_LOAD((i + 3) & 3, i + 3);
    __builtin_amdgcn_sched_barrier(0);
    G2_MMA(i & 3);
    __builtin_amdgcn_sched_barrier(0);
  }
  if (nks == 14) G2_MMA(13 & 3);
  float* sw = sred + wk * 64 * 84;
#pragma unroll
  for (int m = 0; m < 4; ++m)
#pragma unroll
    for (int n = 0; n < 5; ++n)
      *reinterpret_cast<f32x4*>(sw + (m * 16 + fr) * 84 + n * 16 + 4 * fq) = acc[m][n];
  __syncthreads();
  {
    u16* orow = out + (size_t)row * D_ + nb * 80 + c0;
#pragma unroll
    for (int i = 0; i < 5; ++i) {
      const float* p = sred + rl * 84 + c0 + 4 * i;
      const f32x4 a0 = *reinterpret_cast<const f32x4*>(p);
      const f32x4 a1 = *reinterpret_cast<const f32x4*>(p + 64 * 84);
      const f32x4 a2 = *reinterpret_cast<const f32x4*>(p + 2 * 64 * 84);
      const f32x4 a3 = *reinterpret_cast<const f32x4*>(p + 3 * 64 * 84);
      float o[4];
#pragma unroll
      for (int j = 0; j < 4; ++j) o[j] = rbf((a0[j] + a1[j]) + (a2[j] + a3[j]));
      const u32 r0 = pack2(lo_f(hres[i][0]) + o[0], hi_f(hres[i][0]) + o[1]);
      const u32 r1 = pack2(lo_f(hres[i][1]) + o[2], hi_f(hres[i][1]) + o[3]);
      *reinterpret_cast<u32x2*>(orow + 4 * i) = (u32x2){r0, r1};
    }
  }
}

extern "C" void kernel_launch(void* const* d_in, const int* in_sizes, int n_in,
                              void* d_out, int out_size, void* d_ws, size_t ws_size,
                              hipStream_t stream) {
  const u16* x = (const u16*)d_in[0];
  const u16* logA = (const u16*)d_in[1];
  const u16* Aim = (const u16*)d_in[2];
  const u16* Cr = (const u16*)d_in[3];
  const u16* Ci = (const u16*)d_in[4];
  const u16* logdt = (const u16*)d_in[5];
  const u16* Dskip = (const u16*)d_in[6];
  const u16* wg = (const u16*)d_in[8];
  const u16* wu = (const u16*)d_in[9];
  const u16* wd = (const u16*)d_in[10];
  u16* out = (u16*)d_out;

  char* ws = (char*)d_ws;
  size_t off = 0;
  auto alloc = [&](size_t bytes) { char* p = ws + off; off += (bytes + 255) & ~(size_t)255; return p; };
  u16* uT = (u16*)alloc((size_t)D_ * LT_ * 2);
  u16* sT = (u16*)alloc((size_t)D_ * LT_ * 2);
  u16* h = (u16*)alloc((size_t)L_ * D_ * 2);
  u16* xni = (u16*)alloc((size_t)L_ * D_ * 2);
  u16* hmf = (u16*)alloc((size_t)L_ * F_ * 2);
  u16* w1i = (u16*)alloc((size_t)2 * F_ * D_ * 2);
  u16* wdf = (u16*)alloc((size_t)D_ * F_ * 2);
  float* Kc = (float*)alloc((size_t)2 * D_ * KC_STRIDE * 4);

  K1Args a;
  a.x = x; a.wg = wg; a.wu = wu; a.wd = wd;
  a.logA = logA; a.Aim = Aim; a.Cr = Cr; a.Ci = Ci; a.logdt = logdt;
  a.uT = uT; a.w1i = w1i; a.wdf = wdf; a.Kc = Kc;
  k1_kernel<<<K1_BLOCKS, 256, 0, stream>>>(a);
  K2Args a2;
  a2.Kc = Kc; a2.Dskip = Dskip; a2.uT = uT; a2.sT = sT;
  k2_conv<<<D_, 256, 0, stream>>>(a2);
  LN2Args a3;
  a3.sT = sT; a3.x = x; a3.h = h; a3.xni = xni;
  k_ln2<<<LN2_BLOCKS, 1024, 0, stream>>>(a3);
  k3_gemm1<<<256, 512, 0, stream>>>(xni, w1i, hmf);
  k4_gemm2<<<256, 256, 0, stream>>>(hmf, wdf, h, out);
}
```

```cpp
#include <hip/hip_runtime.h>
#include <hip/hip_bf16.h>
#include <stdint.h>

constexpr int L_ = 2048;
constexpr int D_ = 640;
constexpr int N_ = 128;
constexpr int F_ = 1728;
constexpr int LT_ = 2112;
constexpr int KC_STRIDE = 648;

using bf16x8 = __attribute__((ext_vector_type(8))) short;
using f32x4 = __attribute__((ext_vector_type(4))) float;
using u32x4 = __attribute__((ext_vector_type(4))) unsigned int;
using u32x2 = __attribute__((ext_vector_type(2))) unsigned int;
using f32x2 = __attribute__((ext_vector_type(2))) float;
typedef unsigned short u16;
typedef unsigned int u32;

__device__ __forceinline__ float bf2f(u16 v) { return __uint_as_float(((u32)v) << 16); }
__device__ __forceinline__ u16 f2bf(float f) {
  __hip_bfloat16 b = __float2bfloat16(f);
  return *reinterpret_cast<u16*>(&b);
}
__device__ __forceinline__ float rbf(float f) { return bf2f(f2bf(f)); }
__device__ __forceinline__ float lo_f(u32 v) { return __uint_as_float(v << 16); }
__device__ __forceinline__ float hi_f(u32 v) { return __uint_as_float(v & 0xffff0000u); }
__device__ __forceinline__ u32 pack2(float a, float b) { return (u32)f2bf(a) | ((u32)f2bf(b) << 16); }

template <int CTRL, int RMASK>
__device__ __forceinline__ float dpp_add(float v) {
  const int t = __builtin_amdgcn_update_dpp(0, __float_as_int(v), CTRL, RMASK, 0xf, false);
  return v + __int_as_float(t);
}
__device__ __forceinline__ float wave_sum(float v) {
  v = dpp_add<0x111, 0xf>(v);
  v = dpp_add<0x112, 0xf>(v);
  v = dpp_add<0x114, 0xf>(v);
  v = dpp_add<0x118, 0xf>(v);
  v = dpp_add<0x142, 0xa>(v);
  v = dpp_add<0x143, 0xc>(v);
  return __int_as_float(__builtin_amdgcn_readlane(__float_as_int(v), 63));
}

__device__ __forceinline__ void cexpp(float re2, float imr, float p, float& outr, float& outi) {
  const float mag = __builtin_amdgcn_exp2f(re2 * p);
  const float t = __builtin_amdgcn_fractf(imr * p);
  outr = mag * __builtin_amdgcn_cosf(t);
  outi = mag * __builtin_amdgcn_sinf(t);
}

__device__ __forceinline__ void ln_row(const float (&f)[10], float (&o)[10]) {
  float s = 0.f;
#pragma unroll
  for (int i = 0; i < 10; ++i) s += f[i];
  const float mean = wave_sum(s) * (1.f / D_);
  float q = 0.f;
#pragma unroll
  for (int i = 0; i < 10; ++i) { const float t = f[i] - mean; q += t * t; }
  const float var = wave_sum(q) * (1.f / D_);
  const float mb = rbf(mean);
  const float sd = rbf(sqrtf(rbf(rbf(var) + 1e-5f)));
  const float rs = 1.f / sd;
#pragma unroll
  for (int i = 0; i < 10; ++i) {
    const float t = rbf(f[i] - mb);
    const float q0 = t * rs;
    o[i] = fmaf(fmaf(-q0, sd, t), rs, q0);
  }
}

constexpr int K1_KG_BLOCKS = 2 * D_;
constexpr int K1_LN_BLOCKS = L_ / 16;
constexpr int K1_W1_TILES = (D_ / 64) * (F_ / 64);
constexpr int K1_WD_TILES = (F_ / 64) * (D_ / 64);
constexpr int K1_W_BLOCKS = (K1_W1_TILES + K1_WD_TILES);
constexpr int K1_BLOCKS = K1_LN_BLOCKS + K1_W_BLOCKS;

__device__ __forceinline__ void wtile_load(int tile, int tid, u16* st, const u16* wg, const u16* wu, const u16* wd) {
  if (tile < K1_W1_TILES) {
    const int kt = tile / 27, cc0 = (tile % 27) * 64;
#pragma unroll
    for (int i = 0; i < 4; ++i) {
      const int idx = tid + 256 * i;
      const int mat = idx >> 9, r = (idx >> 3) & 63, c = (idx & 7) * 8;
      const u16* src = mat ? wu : wg;
      const u32x4 v = *reinterpret_cast<const u32x4*>(src + (size_t)(kt * 64 + r) * F_ + cc0 + c);
      *reinterpret_cast<u32x4*>(st + (mat * 64 + r) * 72 + c) = v;
    }
  } else {
    const int t2 = tile - K1_W1_TILES;
    const int k0 = (t2 / 10) * 64, n0 = (t2 % 10) * 64;
#pragma unroll
    for (int i = 0; i < 2; ++i) {
      const int idx = tid + 256 * i;
      const int r = idx >> 3, c = (idx & 7) * 8;
      const u32x4 v = *reinterpret_cast<const u32x4*>(wd + (size_t)(k0 + r) * D_ + n0 + c);
      *reinterpret_cast<u32x4*>(st + r * 72 + c) = v;
    }
  }
}
__device__ __forceinline__ void wtile_store(int tile, int tid, const u16* st, u16* w1i, u16* wdf) {
  if (tile < K1_W1_TILES) {
    const int kt = tile / 27, cc0 = (tile % 27) * 64;
#pragma unroll
    for (int i = 0; i < 4; ++i) {
      const int idx = tid + 256 * i;
      const int gl = idx >> 8, rr = (idx >> 3) & 31, pc = idx & 7;
      const int ch = pc ^ (rr & 7);
      const int mat = rr >> 4, col = gl * 16 + (rr & 15);
      u32 p[4];
#pragma unroll
      for (int j = 0; j < 4; ++j)
        p[j] = (u32)st[(mat * 64 + ch * 8 + 2 * j) * 72 + col] | ((u32)st[(mat * 64 + ch * 8 + 2 * j + 1) * 72 + col] << 16);
      const int grp = (cc0 >> 4) + gl;
      u16* dst = w1i + ((size_t)(kt * 108 + grp) * 2048) + rr * 64 + pc * 8;
      *reinterpret_cast<u32x4*>(dst) = (u32x4){p[0], p[1], p[2], p[3]};
    }
  } else {
    const int t2 = tile - K1_W1_TILES;
    const int k0 = (t2 / 10) * 64, n0 = (t2 % 10) * 64;
#pragma unroll
    for (int i = 0; i < 2; ++i) {
      const int idx = tid + 256 * i;
      const int ntl = idx >> 7, ksl = (idx >> 6) & 1, ln = idx & 63;
      const int fr = ln & 15, fq = ln >> 4;
      const int kb = ksl * 32 + fq * 8, col = ntl * 16 + fr;
      u32 p[4];
#pragma unroll
      for (int j = 0; j < 4; ++j)
        p[j] = (u32)st[(kb + 2 * j) * 72 + col] | ((u32)st[(kb + 2 * j + 1) * 72 + col] << 16);
      const int nt = (n0 >> 4) + ntl, ks = (k0 >> 5) + ksl;
      u16* dst = wdf + ((size_t)(nt * 54 + ks) * 64 + ln) * 8;
      *reinterpret_cast<u32x4*>(dst) = (u32x4){p[0], p[1], p[2], p[3]};
    }
  }
}

struct K1Args {
  const u16* x; const u16* wg; const u16* wu; const u16* wd;
  const u16* logA; const u16* Aim; const u16* Cr; const u16* Ci; const u16* logdt;
  u16* uT; u16* w1i; u16* wdf; float* Kc;
};

__global__ __launch_bounds__(256) void k1_kernel(K1Args a) {
  __shared__ __attribute__((aligned(16))) unsigned char smem_raw[16 * 648 * 2];
  const int tid = threadIdx.x, lane = tid & 63, wid = tid >> 6;
  int b = blockIdx.x;
  if (b < K1_LN_BLOCKS) {
    u16* su = reinterpret_cast<u16*>(smem_raw);
    const int row0 = b * 16;
    u32 v[4][5];
#pragma unroll
    for (int rr = 0; rr < 4; ++rr) {
      const u32* xr = reinterpret_cast<const u32*>(a.x + (size_t)(row0 + wid * 4 + rr) * D_);
#pragma unroll
      for (int i = 0; i < 5; ++i) v[rr][i] = xr[lane + 64 * i];
    }
#pragma unroll
    for (int rr = 0; rr < 4; ++rr) {
      float f[10], o[10];
#pragma unroll
      for (int i = 0; i < 5; ++i) { f[2 * i] = lo_f(v[rr][i]); f[2 * i + 1] = hi_f(v[rr][i]); }
      ln_row(f, o);
#pragma unroll
      for (int i = 0; i < 5; ++i)
        *reinterpret_cast<u32*>(su + (wid * 4 + rr) * 648 + 2 * (lane + 64 * i)) = pack2(o[2 * i], o[2 * i + 1]);
    }
    __syncthreads();
#pragma unroll
    for (int it = 0; it < 3; ++it) {
      const int d = tid + 256 * it;
      if (d < D_) {
        u32 pu[8];
#pragma unroll
        for (int r = 0; r < 8; ++r)
          pu[r] = (u32)su[(2 * r) * 648 + d] | ((u32)su[(2 * r + 1) * 648 + d] << 16);
        u32x4* du = reinterpret_cast<u32x4*>(a.uT + (size_t)d * LT_ + row0);
        du[0] = (u32x4){pu[0], pu[1], pu[2], pu[3]};
        du[1] = (u32x4){pu[4], pu[5], pu[6], pu[7]};
      }
    }
    return;
  }
  b -= K1_LN_BLOCKS;
  {
    u16* st = reinterpret_cast<u16*>(smem_raw);
    wtile_load(b, tid, st, a.wg, a.wu, a.wd);
    __syncthreads();
    wtile_store(b, tid, st, a.w1i, a.wdf);
  }
}

constexpr int CV_KLEN = 2208;
constexpr int CV_USTRIDE = 2320;
constexpr int CV_OFF_U = 2 * CV_KLEN * 2;
constexpr int CV_OFF_KC = CV_OFF_U + 8 * CV_USTRIDE * 2;
constexpr int CV_OFF_SP = CV_OFF_KC + 648 * 4;
constexpr int CV_LDS_BYTES = CV_OFF_SP + 128 * 16;

template <int TMIN>
__device__ __forceinline__ void conv_steps(int s0, int s1, const u16* kh, const u16* kl, const u16* ub,
                                           f32x4 (&acc)[8]) {
  bf16x8 ah = *reinterpret_cast<const bf16x8*>(kh + 32 * s0);
  bf16x8 al = *reinterpret_cast<const bf16x8*>(kl + 32 * s0);
  bf16x8 bv[8];
#pragma unroll
  for (int t = TMIN; t < 8; ++t) bv[t] = *reinterpret_cast<const bf16x8*>(ub + 32 * s0 - 256 * t);
  for (int s = s0; s < s1; ++s) {
    const bf16x8 ah2 = *reinterpret_cast<const bf16x8*>(kh + 32 * s + 32);
    const bf16x8 al2 = *reinterpret_cast<const bf16x8*>(kl + 32 * s + 32);
    bf16x8 bv2[8];
#pragma unroll
    for (int t = TMIN; t < 8; ++t) bv2[t] = *reinterpret_cast<const bf16x8*>(ub + 32 * s + 32 - 256 * t);
    __builtin_amdgcn_sched_barrier(0);
#pragma unroll
    for (int t = TMIN; t < 8; ++t) {
      acc[t] = __builtin_amdgcn_mfma_f32_16x16x32_bf16(ah, bv[t], acc[t], 0, 0, 0);
      acc[t] = __builtin_amdgcn_mfma_f32_16x16x32_bf16(al, bv[t], acc[t], 0, 0, 0);
    }
    __builtin_amdgcn_sched_barrier(0);
    ah = ah2; al = al2;
#pragma unroll
    for (int t = TMIN; t < 8; ++t) bv[t] = bv2[t];
  }
}

struct K2Args {
  const u16* logA; const u16* Aim; const u16* Cr; const u16* Ci; const u16* logdt;
  const u16* Dskip; const u16* uT; u16* sT;
};

__device__ __forceinline__ void build_u_chunk(const u32 (&W)[8], u16* dst) {
#pragma unroll
  for (int rho = 0; rho < 8; ++rho) {
    u32 o[4];
#pragma unroll
    for (int k = 0; k < 4; ++k) {
      if ((rho & 1) == 0) {
        const int aI = (rho + 8 - 2 * k) >> 1;
        o[k] = (W[aI] & 0xffffu) | (W[aI - 1] & 0xffff0000u);
      } else {
        const int aI = (rho + 7 - 2 * k) >> 1;
        o[k] = (W[aI] >> 16) | (W[aI] << 16);
      }
    }
    *reinterpret_cast<u32x4*>(dst + rho * CV_USTRIDE) = (u32x4){o[0], o[1], o[2], o[3]};
  }
}

__global__ __launch_bounds__(256, 3) void k2_conv(K2Args a) {
  __shared__ __attribute__((aligned(16))) unsigned char smem[CV_LDS_BYTES];
  u16* sKh = reinterpret_cast<u16*>(smem);
  u16* sKl = sKh + CV_KLEN;
  u16* sU = reinterpret_cast<u16*>(smem + CV_OFF_U);
  const int tid = threadIdx.x, lane = tid & 63, wid = tid >> 6;
  const int d = blockIdx.x;
  const u16* ud = a.uT + (size_t)d * LT_;
  const int q = 255 - tid;
  const u32x4 w0 = *reinterpret_cast<const u32x4*>(ud + 8 * q);
  u32x4 w1 = {0, 0, 0, 0};
  if (q < 255) w1 = *reinterpret_cast<const u32x4*>(ud + 8 * q + 8);
  float* skc = reinterpret_cast<float*>(smem + CV_OFF_KC);
  {
    float* sp = reinterpret_cast<float*>(smem + CV_OFF_SP);
    float* sr = reinterpret_cast<float*>(smem + CV_OFF_U);
    const float dt = rbf(expf(bf2f(a.logdt[d])));
    if (tid < N_) {
      const int n = tid;
      const float la = bf2f(a.logA[d * N_ + n]);
      f32x4 pv;
      pv[0] = (-rbf(expf(la)) * dt) * 1.4426950408889634f;
      pv[1] = (bf2f(a.Aim[d * N_ + n]) * dt) * 0.15915494309189535f;
      pv[2] = bf2f(a.Cr[d * N_ + n]) * dt;
      pv[3] = bf2f(a.Ci[d * N_ + n]) * dt;
      *reinterpret_cast<f32x4*>(sp + 4 * n) = pv;
    }
    __syncthreads();
    const int rc = lane & 15, kk = lane >> 4;
    f32x4 acc0 = {0, 0, 0, 0}, acc1 = {0, 0, 0, 0}, acc2 = {0, 0, 0, 0}, acc3 = {0, 0, 0, 0};
    const float pA = (float)(16 * rc);
    const float pB = (float)(rc + 1);
#pragma unroll 2
    for (int i = 0; i < 4; ++i) {
      const f32x4 p0 = *reinterpret_cast<const f32x4*>(sp + 4 * (wid * 32 + 8 * i + kk));
      const f32x4 p1 = *reinterpret_cast<const f32x4*>(sp + 4 * (wid * 32 + 8 * i + 4 + kk));
      const f32x2 cr = {p0[2], p1[2]}, ci = {p0[3], p1[3]};
      f32x2 Ar, Ai, Br, Bi;
      {
        float r0, i0, r1, i1;
        cexpp(p0[0], p0[1], pA, r0, i0); cexpp(p1[0], p1[1], pA, r1, i1);
        Ar = (f32x2){r0, r1}; Ai = (f32x2){i0, i1};
        cexpp(p0[0], p0[1], pB, r0, i0); cexpp(p1[0], p1[1], pB, r1, i1);
        Br = (f32x2){r0, r1}; Bi = (f32x2){i0, i1};
      }
#define KG_STEP(ACC)                                                              \
      {                                                                             \
        const f32x2 tr = __builtin_elementwise_fma(Ar, cr, -(Ai * ci));             \
        const f32x2 ti = __builtin_elementwise_fma(-Ar, ci, -(Ai * cr));            \
        ACC = __builtin_amdgcn_mfma_f32_16x16x4f32(tr[0], Br[0], ACC, 0, 0, 0);     \
        ACC = __builtin_amdgcn_mfma_f32_16x16x4f32(ti[0], Bi[0], ACC, 0, 0, 0);     \
        ACC = __builtin_amdgcn_mfma_f32_16x16x4f32(tr[1], Br[1], ACC, 0, 0, 0);     \
        ACC = __builtin_amdgcn_mfma_f32_16x16x4f32(ti[1], Bi[1], ACC, 0, 0, 0);     \
      }
#define KG_SQ()                                                                   \
      {                                                                             \
        const f32x2 t0 = __builtin_elementwise_fma(Ar, Ar, -(Ai * Ai));             \
        Ai = (Ar + Ar) * Ai; Ar = t0;                                               \
        const f32x2 t1 = __builtin_elementwise_fma(Br, Br, -(Bi * Bi));             \
        Bi = (Br + Br) * Bi; Br = t1;                                               \
      }
      KG_STEP(acc0); KG_SQ();
      KG_STEP(acc1); KG_SQ();
      KG_STEP(acc2); KG_SQ();
      KG_STEP(acc3);
    }
    {
      float* w = sr + wid * 1024;
#pragma unroll
      for (int j = 0; j < 4; ++j) {
        const int e = (4 * kk + j) * 16 + rc;
        w[e] = acc0[j]; w[256 + e] = acc1[j]; w[512 + e] = acc2[j]; w[768 + e] = acc3[j];
      }
    }
    __syncthreads();
    skc[tid + 1] = 2.f * ((sr[tid] + sr[1024 + tid]) + (sr[2048 + tid] + sr[3072 + tid]));
    if (tid >= 128) {
#pragma unroll
      for (int gI = 1; gI < 4; ++gI) {
        const int e = gI * 256 + tid;
        skc[gI * 128 + tid + 1] = 2.f * ((sr[e] + sr[1024 + e]) + (sr[2048 + e] + sr[3072 + e]));
      }
    }
    if (wid == 0) {
      const float s0 = 2.f * wave_sum(sp[4 * lane + 2] + sp[4 * (lane + 64) + 2]);
      if (lane == 0) skc[0] = s0;
    }
    __syncthreads();
  }
  float kv[8];
  {
    const int j0 = 8 * tid;
    const int e = (j0 < 256) ? 0 : (j0 < 512) ? 1 : (j0 < 1024) ? 2 : 3;
    const int base = 128 * e;
    const int hm1 = ((1 << e) >> 1) - 1;
#pragma unroll
    for (int jj = 0; jj < 8; ++jj) {
      const int j = j0 + jj;
      const int pq = (e == 0) ? j : ((j + hm1 + ((j >> e) & 1)) >> e);
      kv[jj] = skc[base + pq];
    }
  }
  {
    const u32 W[8] = {w0[0], w0[1], w0[2], w0[3], w1[0], w1[1], w1[2], w1[3]};
    build_u_chunk(W, sU + 8 * tid);
    if (tid == 255) {
      const u32 W2[8] = {0, 0, 0, 0, w0[0], w0[1], w0[2], w0[3]};
      build_u_chunk(W2, sU + 8 * 256);
    }
    if (tid < 8 * 19) {
      const int rho = tid / 19, m = 257 + tid % 19;
      *reinterpret_cast<u32x4*>(sU + rho * CV_USTRIDE + 8 * m) = (u32x4){0, 0, 0, 0};
    } else if (tid >= 160 && tid < 200) {
      const int t = tid - 160;
      const int m = (t % 20 < 16) ? t % 20 : 256 + t % 20;
      u16* dst = (t < 20) ? sKh : sKl;
      *reinterpret_cast<u32x4*>(dst + 8 * m) = (u32x4){0, 0, 0, 0};
    }
    u32x4 ph, pl;
#pragma unroll
    for (int k = 0; k < 4; ++k) {
      const u32 hh = pack2(kv[2 * k], kv[2 * k + 1]);
      ph[k] = hh;
      pl[k] = pack2(kv[2 * k] - lo_f(hh), kv[2 * k + 1] - hi_f(hh));
    }
    *reinterpret_cast<u32x4*>(sKh + 128 + 8 * tid) = ph;
    *reinterpret_cast<u32x4*>(sKl + 128 + 8 * tid) = pl;
  }
  __syncthreads();
  const int r = lane & 15, g = lane >> 4;
  const int ap = (lane & 15) >> 3, rho = lane & 7;
  f32x4 acc[8];
#pragma unroll
  for (int t = 0; t < 8; ++t) acc[t] = (f32x4){0, 0, 0, 0};
  const u16* kh = sKh + 128 + 8 * r + 8 * g;
  const u16* kl = sKl + 128 + 8 * r + 8 * g;
  const u16* ub = sU + rho * CV_USTRIDE + 8 * g - 128 * ap + 2048;
  if (wid == 0) {
    conv_steps<0>(-4, 5, kh, kl, ub, acc);
  } else if (wid == 1) {
    conv_steps<1>(5, 13, kh, kl, ub, acc);
    conv_steps<2>(13, 16, kh, kl, ub, acc);
  } else if (wid == 2) {
    conv_steps<2>(16, 21, kh, kl, ub, acc);
    conv_steps<3>(21, 29, kh, kl, ub, acc);
    conv_steps<4>(29, 30, kh, kl, ub, acc);
  } else {
    conv_steps<4>(30, 37, kh, kl, ub, acc);
    conv_steps<5>(37, 45, kh, kl, ub, acc);
    conv_steps<6>(45, 53, kh, kl, ub, acc);
    conv_steps<7>(53, 61, kh, kl, ub, acc);
  }
  __syncthreads();
  float* sy = reinterpret_cast<float*>(smem);
#pragma unroll
  for (int t = 0; t < 8; ++t) {
#pragma unroll
    for (int j = 0; j < 4; ++j) {
      const int l = 256 * t + 128 * ap + rho + 8 * (4 * g + j);
      sy[wid * L_ + l] = acc[t][j];
    }
  }
  __syncthreads();
  const float dsk = bf2f(a.Dskip[d]);
  {
    const int l0 = q * 8;
    float y[8];
#pragma unroll
    for (int hh = 0; hh < 2; ++hh) {
      f32x4 a0 = *reinterpret_cast<const f32x4*>(sy + l0 + 4 * hh);
      f32x4 a1 = *reinterpret_cast<const f32x4*>(sy + L_ + l0 + 4 * hh);
      f32x4 a2 = *reinterpret_cast<const f32x4*>(sy + 2 * L_ + l0 + 4 * hh);
      f32x4 a3 = *reinterpret_cast<const f32x4*>(sy + 3 * L_ + l0 + 4 * hh);
#pragma unroll
      for (int j = 0; j < 4; ++j) y[4 * hh + j] = (a0[j] + a1[j]) + (a2[j] + a3[j]);
    }
    u32 o[4];
#pragma unroll
    for (int k = 0; k < 4; ++k) {
      const float t0 = rbf(lo_f(w0[k]) * dsk), t1 = rbf(hi_f(w0[k]) * dsk);
      o[k] = pack2(rbf(y[2 * k]) + t0, rbf(y[2 * k + 1]) + t1);
    }
    *reinterpret_cast<u32x4*>(a.sT + (size_t)d * LT_ + l0) = (u32x4){o[0], o[1], o[2], o[3]};
  }
}

struct LN2Args {
  const u16* sT; const u16* x; u16* h; u16* xni;
  const u16* wg; const u16* wu; const u16* wd; u16* w1i; u16* wdf;
};
constexpr int LN2_BLOCKS = L_ / 16;
constexpr int LN2_W_BLOCKS = (K1_W1_TILES + K1_WD_TILES) / 4;

__global__ __launch_bounds__(1024) void k_ln2(LN2Args a) {
  __shared__ __attribute__((aligned(16))) u16 ss[16 * 648];
  const int tid = threadIdx.x, lane = tid & 63, wid = tid >> 6;
  const u16* __restrict__ sT = a.sT; const u16* __restrict__ x = a.x;
  u16* __restrict__ h = a.h; u16* __restrict__ xni = a.xni;
  const int row0 = blockIdx.x * 16;
  const int row = row0 + wid;
  u32 xv[5];
  {
    const u32* xr = reinterpret_cast<const u32*>(x + (size_t)row * D_);
#pragma unroll
    for (int i = 0; i < 5; ++i) xv[i] = xr[lane + 64 * i];
  }
  if (tid < D_) {
    const int d = tid;
    const u32x4* src = reinterpret_cast<const u32x4*>(sT + (size_t)d * LT_ + row0);
    const u32x4 a0 = src[0], a1 = src[1];
#pragma unroll
    for (int k = 0; k < 4; ++k) {
      ss[(2 * k) * 648 + d] = (u16)(a0[k] & 0xffffu);
      ss[(2 * k + 1) * 648 + d] = (u16)(a0[k] >> 16);
      ss[(8 + 2 * k) * 648 + d] = (u16)(a1[k] & 0xffffu);
      ss[(8 + 2 * k + 1) * 648 + d] = (u16)(a1[k] >> 16);
    }
  }
  __syncthreads();
  {
    float f[10], o[10];
    u32* hrow = reinterpret_cast<u32*>(h + (size_t)row * D_);
#pragma unroll
    for (int i = 0; i < 5; ++i) {
      const u32 sv = *reinterpret_cast<const u32*>(ss + wid * 648 + 2 * (lane + 64 * i));
      const u32 hv = pack2(lo_f(xv[i]) + lo_f(sv), hi_f(xv[i]) + hi_f(sv));
      hrow[lane + 64 * i] = hv;
      f[2 * i] = lo_f(hv); f[2 * i + 1] = hi_f(hv);
    }
    ln_row(f, o);
    const int mt = row >> 7, r = row & 127;
#pragma unroll
    for (int i = 0; i < 5; ++i) {
      const int c = 2 * (lane + 64 * i);
      const int kt = c >> 6, cc = c & 63;
      const int off = ((mt * 10 + kt) * 128 + r) * 64 + (((cc >> 3) ^ (r & 7)) << 3) + (cc & 7);
      *reinterpret_cast<u32*>(xni + off) = pack2(o[2 * i], o[2 * i + 1]);
    }
  }
}

__device__ __forceinline__ float gelu_tanh(float x) {
  const float c0 = -2.f * 0.7978845608028654f * 1.4426950408889634f;
  const float c1 = c0 * 0.044715f;
  const float e = __builtin_amdgcn_exp2f(x * fmaf(x * x, c1, c0));
  return x * __builtin_amdgcn_rcpf(1.f + e);
}

constexpr int G1_A_BYTES = 16384;
constexpr int G1_B_BYTES = 7 * 4096;
constexpr int G1_STAGE = G1_A_BYTES + G1_B_BYTES;
constexpr int G1_NSTG = 3;

template <int NG>
__device__ __forceinline__ void g1_ldfrag(const unsigned char* sb, int arow, int brow, int cpos,
                                          bf16x8 (&xf)[2], bf16x8 (&wf)[8]) {
#pragma unroll
  for (int m = 0; m < 2; ++m)
    xf[m] = *reinterpret_cast<const bf16x8*>(sb + (arow + m * 16) * 128 + cpos);
#pragma unroll
  for (int n = 0; n < 2 * NG; ++n)
    wf[n] = *reinterpret_cast<const bf16x8*>(sb + G1_A_BYTES + (brow + n * 16) * 128 + cpos);
}
template <int NG>
__device__ __forceinline__ void g1_mma(const bf16x8 (&xf)[2], const bf16x8 (&wf)[8], f32x4 (&acc)[2][8]) {
#pragma unroll
  for (int n = 0; n < 2 * NG; ++n)
#pragma unroll
    for (int m = 0; m < 2; ++m)
      acc[m][n] = __builtin_amdgcn_mfma_f32_16x16x32_bf16(wf[n], xf[m], acc[m][n], 0, 0, 0);
}

template <int NG>
__device__ __forceinline__ void g1_main(unsigned char* smem, const unsigned char* Ag, const unsigned char* Bg,
                                        int g0, int wid, int lane, int arow, int brow, int cpos0, int cpos1,
                                        f32x4 (&acc)[2][8]) {
#define G1_ISSUE(kt, buf)                                                                              \
  {                                                                                                    \
    unsigned char* sb_ = smem + (buf) * G1_STAGE;                                                      \
    _Pragma("unroll") for (int i = 0; i < 2; ++i) {                                                    \
      const int piece = wid + 8 * i;                                                                   \
      __builtin_amdgcn_global_load_lds(                                                                \
          (const __attribute__((address_space(1))) void*)(Ag + (size_t)(kt) * G1_A_BYTES + piece * 1024 + lane * 16), \
          (__attribute__((address_space(3))) void*)(sb_ + piece * 1024), 16, 0, 0);                    \
    }                                                                                                  \
    _Pragma("unroll") for (int i = 0; i < 4; ++i) {                                                    \
      int pb = wid + 8 * i; if (pb >= 28) pb -= 4;                                                     \
      const int gi = pb >> 2;                                                                          \
      const int grp = (g0 + gi < 108) ? g0 + gi : 107;                                                 \
      __builtin_amdgcn_global_load_lds(                                                                \
          (const __attribute__((address_space(1))) void*)(Bg + ((size_t)((kt) * 108 + grp) * 4096) + (pb & 3) * 1024 + lane * 16), \
          (__attribute__((address_space(3))) void*)(sb_ + G1_A_BYTES + pb * 1024), 16, 0, 0);          \
    }                                                                                                  \
  }
  bf16x8 xf0[2], wf0[8], xf1[2], wf1[8];
  G1_ISSUE(0, 0);
  G1_ISSUE(1, 1);
  G1_ISSUE(2, 2);
  asm volatile("s_waitcnt vmcnt(12)" ::: "memory");
  __builtin_amdgcn_s_barrier();
  asm volatile("" ::: "memory");
  g1_ldfrag<NG>(smem, arow, brow, cpos0, xf0, wf0);
#pragma unroll
  for (int kt = 0; kt < 10; ++kt) {
    const int buf = kt % 3;
    const unsigned char* sb = smem + buf * G1_STAGE;
    const int nbuf = (buf == 2) ? 0 : buf + 1;
    g1_ldfrag<NG>(sb, arow, brow, cpos1, xf1, wf1);
    g1_mma<NG>(xf0, wf0, acc);
    if (kt < 9) {
      if (kt < 8) asm volatile("s_waitcnt vmcnt(6) lgkmcnt(0)" ::: "memory");
      else asm volatile("s_waitcnt vmcnt(0) lgkmcnt(0)" ::: "memory");
      __builtin_amdgcn_s_barrier();
      asm volatile("" ::: "memory");
      g1_ldfrag<NG>(smem + nbuf * G1_STAGE, arow, brow, cpos0, xf0, wf0);
      __builtin_amdgcn_sched_barrier(0);
      if (kt + 3 < 10) G1_ISSUE(kt + 3, buf);
    }
    g1_mma<NG>(xf1, wf1, acc);
  }
}

__global__ __launch_bounds__(512) void k3_gemm1(const u16* __restrict__ xni, const u16* __restrict__ w1i,
                                                u16* __restrict__ hmf) {
  __shared__ __attribute__((aligned(16))) unsigned char smem[G1_NSTG * G1_STAGE];
  const int tid = threadIdx.x, lane = tid & 63;
  const int wid = __builtin_amdgcn_readfirstlane(tid >> 6);
  const int wm = wid & 3, wn = wid >> 2;
  const int fr = lane & 15, fq = lane >> 4;
  const int bid = blockIdx.x;
  const int xcd = bid & 7, idx = bid >> 3;
  const int mt = (xcd & 1) * 8 + (idx & 7);
  const int nb = (xcd >> 1) * 4 + (idx >> 3);
  const int g0 = 27 * (nb >> 2) + 7 * (nb & 3);
  const int ng = ((nb & 3) == 3) ? 6 : 7;
  const int gw0 = wn * 4;
  const int gwn = wn ? ng - 4 : 4;
  const unsigned char* Ag = reinterpret_cast<const unsigned char*>(xni) + (size_t)mt * 10 * G1_A_BYTES;
  const unsigned char* Bg = reinterpret_cast<const unsigned char*>(w1i);
  f32x4 acc[2][8];
#pragma unroll
  for (int m = 0; m < 2; ++m)
#pragma unroll
    for (int n = 0; n < 8; ++n) acc[m][n] = (f32x4){0, 0, 0, 0};
  const int swz = fr & 7;
  const int cpos0 = ((0 + fq) ^ swz) << 4, cpos1 = ((4 + fq) ^ swz) << 4;
  const int arow = wm * 32 + fr, brow = gw0 * 32 + fr;
  if (wn == 0) g1_main<4>(smem, Ag, Bg, g0, wid, lane, arow, brow, cpos0, cpos1, acc);
  else g1_main<3>(smem, Ag, Bg, g0, wid, lane, arow, brow, cpos0, cpos1, acc);
#pragma unroll
  for (int m = 0; m < 2; ++m) {
    const int mt16 = mt * 8 + wm * 2 + m;
#pragma unroll
    for (int gI = 0; gI < 4; ++gI) {
      if (gI < gwn) {
        float o[4];
#pragma unroll
        for (int j = 0; j < 4; ++j) {
          const float gv = rbf(acc[m][2 * gI][j]);
          const float uv = rbf(acc[m][2 * gI + 1][j]);
          o[j] = rbf(gelu_tanh(gv)) * uv;
        }
        const int f = 16 * (g0 + gw0 + gI) + 4 * fq;
        const int ks = f >> 5, fq2 = (f & 31) >> 3, j0 = f & 7;
        u16* dst = hmf + ((size_t)(mt16 * 54 + ks) * 64 + fr + 16 * fq2) * 8 + j0;
        *reinterpret_cast<u32x2*>(dst) = (u32x2){pack2(o[0], o[1]), pack2(o[2], o[3])};
      }
    }
  }
}

__global__ __launch_bounds__(256) void k4_gemm2(const u16* __restrict__ hmf, const u16* __restrict__ wdf,
                                                const u16* __restrict__ h, u16* __restrict__ out) {
  __shared__ __attribute__((aligned(16))) float sred[4 * 64 * 84];
  const int tid = threadIdx.x, lane = tid & 63;
  const int wk = __builtin_amdgcn_readfirstlane(tid >> 6);
  const int fr = lane & 15, fq = lane >> 4;
  const int bid = blockIdx.x;
  const int xcd = bid & 7, idx = bid >> 3;
  const int mt = (xcd >> 1) * 8 + (idx & 7);
  const int nb = (xcd & 1) * 4 + (idx >> 3);
  const int ks0 = (wk < 2) ? wk * 14 : 28 + (wk - 2) * 13;
  const int nks = (wk < 2) ? 14 : 13;
  const bf16x8* Ap = reinterpret_cast<const bf16x8*>(hmf) + (size_t)(mt * 4) * 54 * 64 + lane;
  const bf16x8* Bp = reinterpret_cast<const bf16x8*>(wdf) + (size_t)(nb * 5) * 54 * 64 + lane;
  const int rl = tid >> 2, c0 = (tid & 3) * 20;
  const int row = mt * 64 + rl;
  u32x2 hres[5];
  {
    const u16* hr = h + (size_t)row * D_ + nb * 80 + c0;
#pragma unroll
    for (int i = 0; i < 5; ++i) hres[i] = *reinterpret_cast<const u32x2*>(hr + 4 * i);
  }
  f32x4 acc[4][5];
#pragma unroll
  for (int m = 0; m < 4; ++m)
#pragma unroll
    for (int n = 0; n < 5; ++n) acc[m][n] = (f32x4){0, 0, 0, 0};
  bf16x8 xa[4][4], wb[4][5];
#define G2_LOAD(slot, kidx)                                                              \
  {                                                                                      \
    const int kq_ = ks0 + (((kidx) < nks) ? (kidx) : nks - 1);                           \
    _Pragma("unroll") for (int m = 0; m < 4; ++m) xa[slot][m] = Ap[(m * 54 + kq_) * 64];   \
    _Pragma("unroll") for (int n = 0; n < 5; ++n) wb[slot][n] = Bp[(n * 54 + kq_) * 64];   \
  }
#define G2_MMA(slot)                                                                     \
  {                                                                                      \
    _Pragma("unroll") for (int n = 0; n < 5; ++n)                                        \
    _Pragma("unroll") for (int m = 0; m < 4; ++m)                                        \
      acc[m][n] = __builtin_amdgcn_mfma_f32_16x16x32_bf16(wb[slot][n], xa[slot][m], acc[m][n], 0, 0, 0); \
  }
  G2_LOAD(0, 0);
  G2_LOAD(1, 1);
  G2_LOAD(2, 2);
  __builtin_amdgcn_sched_barrier(0);
#pragma unroll
  for (int i = 0; i < 13; ++i) {
    if (i + 3 < 14) G2_LOAD((i + 3) & 3, i + 3);
    __builtin_amdgcn_sched_barrier(0);
    G2_MMA(i & 3);
    __builtin_amdgcn_sched_barrier(0);
  }
  if (nks == 14) G2_MMA(13 & 3);
  float* sw = sred + wk * 64 * 84;
#pragma unroll
  for (int m = 0; m < 4; ++m)
#pragma unroll
    for (int n = 0; n < 5; ++n)
      *reinterpret_cast<f32x4*>(sw + (m * 16 + fr) * 84 + n * 16 + 4 * fq) = acc[m][n];
  __syncthreads();
  {
    u16* orow = out + (size_t)row * D_ + nb * 80 + c0;
#pragma unroll
    for (int i = 0; i < 5; ++i) {
      const float* p = sred + rl * 84 + c0 + 4 * i;
      const f32x4 a0 = *reinterpret_cast<const f32x4*>(p);
      const f32x4 a1 = *reinterpret_cast<const f32x4*>(p + 64 * 84);
      const f32x4 a2 = *reinterpret_cast<const f32x4*>(p + 2 * 64 * 84);
      const f32x4 a3 = *reinterpret_cast<const f32x4*>(p + 3 * 64 * 84);
      float o[4];
#pragma unroll
      for (int j = 0; j < 4; ++j) o[j] = rbf((a0[j] + a1[j]) + (a2[j] + a3[j]));
      const u32 r0 = pack2(lo_f(hres[i][0]) + o[0], hi_f(hres[i][0]) + o[1]);
      const u32 r1 = pack2(lo_f(hres[i][1]) + o[2], hi_f(hres[i][1]) + o[3]);
      *reinterpret_cast<u32x2*>(orow + 4 * i) = (u32x2){r0, r1};
    }
  }
}

extern "C" void kernel_launch(void* const* d_in, const int* in_sizes, int n_in,
                              void* d_out, int out_size, void* d_ws, size_t ws_size,
                              hipStream_t stream) {
  const u16* x = (const u16*)d_in[0];
  const u16* logA = (const u16*)d_in[1];
  const u16* Aim = (const u16*)d_in[2];
  const u16* Cr = (const u16*)d_in[3];
  const u16* Ci = (const u16*)d_in[4];
  const u16* logdt = (const u16*)d_in[5];
  const u16* Dskip = (const u16*)d_in[6];
  const u16* wg = (const u16*)d_in[8];
  const u16* wu = (const u16*)d_in[9];
  const u16* wd = (const u16*)d_in[10];
  u16* out = (u16*)d_out;

  char* ws = (char*)d_ws;
  size_t off = 0;
  auto alloc = [&](size_t bytes) { char* p = ws + off; off += (bytes + 255) & ~(size_t)255; return p; };
  u16* uT = (u16*)alloc((size_t)D_ * LT_ * 2);
  u16* sT = (u16*)alloc((size_t)D_ * LT_ * 2);
  u16* h = (u16*)alloc((size_t)L_ * D_ * 2);
  u16* xni = (u16*)alloc((size_t)L_ * D_ * 2);
  u16* hmf = (u16*)alloc((size_t)L_ * F_ * 2);
  u16* w1i = (u16*)alloc((size_t)2 * F_ * D_ * 2);
  u16* wdf = (u16*)alloc((size_t)D_ * F_ * 2);
  float* Kc = (float*)alloc((size_t)2 * D_ * KC_STRIDE * 4);

  K1Args a;
  a.x = x; a.wg = wg; a.wu = wu; a.wd = wd;
  a.logA = logA; a.Aim = Aim; a.Cr = Cr; a.Ci = Ci; a.logdt = logdt;
  a.uT = uT; a.w1i = w1i; a.wdf = wdf; a.Kc = Kc;
  k1_kernel<<<K1_BLOCKS, 256, 0, stream>>>(a);
  K2Args a2;
  a2.logA = logA; a2.Aim = Aim; a2.Cr = Cr; a2.Ci = Ci; a2.logdt = logdt;
  a2.Dskip = Dskip; a2.uT = uT; a2.sT = sT;
  k2_conv<<<D_, 256, 0, stream>>>(a2);
  LN2Args a3;
  a3.sT = sT; a3.x = x; a3.h = h; a3.xni = xni; a3.wg = wg; a3.wu = wu; a3.wd = wd; a3.w1i = w1i; a3.wdf = wdf;
  k_ln2<<<LN2_BLOCKS, 1024, 0, stream>>>(a3);
  k3_gemm1<<<256, 512, 0, stream>>>(xni, w1i, hmf);
  k4_gemm2<<<256, 256, 0, stream>>>(hmf, wdf, h, out);
}
```

```cpp
#include <hip/hip_runtime.h>
#include <hip/hip_bf16.h>
#include <stdint.h>

constexpr int L_ = 2048;
constexpr int D_ = 640;
constexpr int N_ = 128;
constexpr int F_ = 1728;
constexpr int LT_ = 2112;
constexpr int KC_STRIDE = 648;

using bf16x8 = __attribute__((ext_vector_type(8))) short;
using f32x4 = __attribute__((ext_vector_type(4))) float;
using u32x4 = __attribute__((ext_vector_type(4))) unsigned int;
using u32x2 = __attribute__((ext_vector_type(2))) unsigned int;
using f32x2 = __attribute__((ext_vector_type(2))) float;
typedef unsigned short u16;
typedef unsigned int u32;

__device__ __forceinline__ float bf2f(u16 v) { return __uint_as_float(((u32)v) << 16); }
__device__ __forceinline__ u16 f2bf(float f) {
  __hip_bfloat16 b = __float2bfloat16(f);
  return *reinterpret_cast<u16*>(&b);
}
__device__ __forceinline__ float rbf(float f) { return bf2f(f2bf(f)); }
__device__ __forceinline__ float lo_f(u32 v) { return __uint_as_float(v << 16); }
__device__ __forceinline__ float hi_f(u32 v) { return __uint_as_float(v & 0xffff0000u); }
__device__ __forceinline__ u32 pack2(float a, float b) { return (u32)f2bf(a) | ((u32)f2bf(b) << 16); }

template <int CTRL, int RMASK>
__device__ __forceinline__ float dpp_add(float v) {
  const int t = __builtin_amdgcn_update_dpp(0, __float_as_int(v), CTRL, RMASK, 0xf, false);
  return v + __int_as_float(t);
}
__device__ __forceinline__ float wave_sum(float v) {
  v = dpp_add<0x111, 0xf>(v);
  v = dpp_add<0x112, 0xf>(v);
  v = dpp_add<0x114, 0xf>(v);
  v = dpp_add<0x118, 0xf>(v);
  v = dpp_add<0x142, 0xa>(v);
  v = dpp_add<0x143, 0xc>(v);
  return __int_as_float(__builtin_amdgcn_readlane(__float_as_int(v), 63));
}

__device__ __forceinline__ void cexpp(float re2, float imr, float p, float& outr, float& outi) {
  const float mag = __builtin_amdgcn_exp2f(re2 * p);
  const float t = __builtin_amdgcn_fractf(imr * p);
  outr = mag * __builtin_amdgcn_cosf(t);
  outi = mag * __builtin_amdgcn_sinf(t);
}

__device__ __forceinline__ void ln_row(const float (&f)[10], float (&o)[10]) {
  float s = 0.f;
#pragma unroll
  for (int i = 0; i < 10; ++i) s += f[i];
  const float mean = wave_sum(s) * (1.f / D_);
  float q = 0.f;
#pragma unroll
  for (int i = 0; i < 10; ++i) { const float t = f[i] - mean; q += t * t; }
  const float var = wave_sum(q) * (1.f / D_);
  const float mb = rbf(mean);
  const float sd = rbf(sqrtf(rbf(rbf(var) + 1e-5f)));
  const float rs = 1.f / sd;
#pragma unroll
  for (int i = 0; i < 10; ++i) {
    const float t = rbf(f[i] - mb);
    const float q0 = t * rs;
    o[i] = fmaf(fmaf(-q0, sd, t), rs, q0);
  }
}

constexpr int K1_KG_BLOCKS = D_;
constexpr int K1_LN_BLOCKS = L_ / 16;
constexpr int K1_W1_TILES = (D_ / 64) * (F_ / 64);
constexpr int K1_WD_TILES = (F_ / 64) * (D_ / 64);
constexpr int K1_W_BLOCKS = (K1_W1_TILES + K1_WD_TILES);
constexpr int K1_BLOCKS = K1_KG_BLOCKS + K1_LN_BLOCKS + K1_W_BLOCKS;

__device__ __forceinline__ void wtile_load(int tile, int tid, u16* st, const u16* wg, const u16* wu, const u16* wd) {
  if (tile < K1_W1_TILES) {
    const int kt = tile / 27, cc0 = (tile % 27) * 64;
#pragma unroll
    for (int i = 0; i < 4; ++i) {
      const int idx = tid + 256 * i;
      const int mat = idx >> 9, r = (idx >> 3) & 63, c = (idx & 7) * 8;
      const u16* src = mat ? wu : wg;
      const u32x4 v = *reinterpret_cast<const u32x4*>(src + (size_t)(kt * 64 + r) * F_ + cc0 + c);
      *reinterpret_cast<u32x4*>(st + (mat * 64 + r) * 72 + c) = v;
    }
  } else {
    const int t2 = tile - K1_W1_TILES;
    const int k0 = (t2 / 10) * 64, n0 = (t2 % 10) * 64;
#pragma unroll
    for (int i = 0; i < 2; ++i) {
      const int idx = tid + 256 * i;
      const int r = idx >> 3, c = (idx & 7) * 8;
      const u32x4 v = *reinterpret_cast<const u32x4*>(wd + (size_t)(k0 + r) * D_ + n0 + c);
      *reinterpret_cast<u32x4*>(st + r * 72 + c) = v;
    }
  }
}
__device__ __forceinline__ void wtile_store(int tile, int tid, const u16* st, u16* w1i, u16* wdf) {
  if (tile < K1_W1_TILES) {
    const int kt = tile / 27, cc0 = (tile % 27) * 64;
#pragma unroll
    for (int i = 0; i < 4; ++i) {
      const int idx = tid + 256 * i;
      const int gl = idx >> 8, rr = (idx >> 3) & 31, pc = idx & 7;
      const int ch = pc ^ (rr & 7);
      const int mat = rr >> 4, col = gl * 16 + (rr & 15);
      u32 p[4];
#pragma unroll
      for (int j = 0; j < 4; ++j)
        p[j] = (u32)st[(mat * 64 + ch * 8 + 2 * j) * 72 + col] | ((u32)st[(mat * 64 + ch * 8 + 2 * j + 1) * 72 + col] << 16);
      const int grp = (cc0 >> 4) + gl;
      u16* dst = w1i + ((size_t)(kt * 108 + grp) * 2048) + rr * 64 + pc * 8;
      *reinterpret_cast<u32x4*>(dst) = (u32x4){p[0], p[1], p[2], p[3]};
    }
  } else {
    const int t2 = tile - K1_W1_TILES;
    const int k0 = (t2 / 10) * 64, n0 = (t2 % 10) * 64;
#pragma unroll
    for (int i = 0; i < 2; ++i) {
      const int idx = tid + 256 * i;
      const int ntl = idx >> 7, ksl = (idx >> 6) & 1, ln = idx & 63;
      const int fr = ln & 15, fq = ln >> 4;
      const int kb = ksl * 32 + fq * 8, col = ntl * 16 + fr;
      u32 p[4];
#pragma unroll
      for (int j = 0; j < 4; ++j)
        p[j] = (u32)st[(kb + 2 * j) * 72 + col] | ((u32)st[(kb + 2 * j + 1) * 72 + col] << 16);
      const int nt = (n0 >> 4) + ntl, ks = (k0 >> 5) + ksl;
      u16* dst = wdf + ((size_t)(nt * 54 + ks) * 64 + ln) * 8;
      *reinterpret_cast<u32x4*>(dst) = (u32x4){p[0], p[1], p[2], p[3]};
    }
  }
}

struct K1Args {
  const u16* x; const u16* wg; const u16* wu; const u16* wd;
  const u16* logA; const u16* Aim; const u16* Cr; const u16* Ci; const u16* logdt;
  u16* uT; u16* w1i; u16* wdf; float* Kc;
};

__global__ __launch_bounds__(256) void k1_kernel(K1Args a) {
  __shared__ __attribute__((aligned(16))) unsigned char smem_raw[16 * 648 * 2];
  const int tid = threadIdx.x, lane = tid & 63, wid = tid >> 6;
  int b = blockIdx.x;
  if (b < K1_LN_BLOCKS) {
    u16* su = reinterpret_cast<u16*>(smem_raw);
    const int row0 = b * 16;
    u32 v[4][5];
#pragma unroll
    for (int rr = 0; rr < 4; ++rr) {
      const u32* xr = reinterpret_cast<const u32*>(a.x + (size_t)(row0 + wid * 4 + rr) * D_);
#pragma unroll
      for (int i = 0; i < 5; ++i) v[rr][i] = xr[lane + 64 * i];
    }
#pragma unroll
    for (int rr = 0; rr < 4; ++rr) {
      float f[10], o[10];
#pragma unroll
      for (int i = 0; i < 5; ++i) { f[2 * i] = lo_f(v[rr][i]); f[2 * i + 1] = hi_f(v[rr][i]); }
      ln_row(f, o);
#pragma unroll
      for (int i = 0; i < 5; ++i)
        *reinterpret_cast<u32*>(su + (wid * 4 + rr) * 648 + 2 * (lane + 64 * i)) = pack2(o[2 * i], o[2 * i + 1]);
    }
    __syncthreads();
#pragma unroll
    for (int it = 0; it < 3; ++it) {
      const int d = tid + 256 * it;
      if (d < D_) {
        u32 pu[8];
#pragma unroll
        for (int r = 0; r < 8; ++r)
          pu[r] = (u32)su[(2 * r) * 648 + d] | ((u32)su[(2 * r + 1) * 648 + d] << 16);
        u32x4* du = reinterpret_cast<u32x4*>(a.uT + (size_t)d * LT_ + row0);
        du[0] = (u32x4){pu[0], pu[1], pu[2], pu[3]};
        du[1] = (u32x4){pu[4], pu[5], pu[6], pu[7]};
      }
    }
    return;
  }
  b -= K1_LN_BLOCKS;
  if (b < K1_KG_BLOCKS) {
    const int d = b;
    float* sp = reinterpret_cast<float*>(smem_raw);
    float* sr = sp + 512;
    const float dt = rbf(expf(bf2f(a.logdt[d])));
    if (tid < N_) {
      const int n = tid;
      const float la = bf2f(a.logA[d * N_ + n]);
      f32x4 pv;
      pv[0] = (-rbf(expf(la)) * dt) * 1.4426950408889634f;
      pv[1] = (bf2f(a.Aim[d * N_ + n]) * dt) * 0.15915494309189535f;
      pv[2] = bf2f(a.Cr[d * N_ + n]) * dt;
      pv[3] = bf2f(a.Ci[d * N_ + n]) * dt;
      *reinterpret_cast<f32x4*>(sp + 4 * tid) = pv;
    }
    __syncthreads();
    const int rc = lane & 15;
    const int kk = lane >> 4;
    f32x4 acc0 = {0, 0, 0, 0}, acc1 = {0, 0, 0, 0}, acc2 = {0, 0, 0, 0}, acc3 = {0, 0, 0, 0};
    const float pA = (float)(16 * rc);
    const float pB = (float)(rc + 1);
#pragma unroll 2
    for (int i = 0; i < 4; ++i) {
      const f32x4 p0 = *reinterpret_cast<const f32x4*>(sp + 4 * (wid * 32 + 8 * i + kk));
      const f32x4 p1 = *reinterpret_cast<const f32x4*>(sp + 4 * (wid * 32 + 8 * i + 4 + kk));
      const f32x2 cr = {p0[2], p1[2]}, ci = {p0[3], p1[3]};
      f32x2 Ar, Ai, Br, Bi;
      {
        float r0, i0, r1, i1;
        cexpp(p0[0], p0[1], pA, r0, i0); cexpp(p1[0], p1[1], pA, r1, i1);
        Ar = (f32x2){r0, r1}; Ai = (f32x2){i0, i1};
        cexpp(p0[0], p0[1], pB, r0, i0); cexpp(p1[0], p1[1], pB, r1, i1);
        Br = (f32x2){r0, r1}; Bi = (f32x2){i0, i1};
      }
#define KG_STEP(ACC)                                                              \
      {                                                                             \
        const f32x2 tr = __builtin_elementwise_fma(Ar, cr, -(Ai * ci));             \
        const f32x2 ti = __builtin_elementwise_fma(-Ar, ci, -(Ai * cr));            \
        ACC = __builtin_amdgcn_mfma_f32_16x16x4f32(tr[0], Br[0], ACC, 0, 0, 0);     \
        ACC = __builtin_amdgcn_mfma_f32_16x16x4f32(ti[0], Bi[0], ACC, 0, 0, 0);     \
        ACC = __builtin_amdgcn_mfma_f32_16x16x4f32(tr[1], Br[1], ACC, 0, 0, 0);     \
        ACC = __builtin_amdgcn_mfma_f32_16x16x4f32(ti[1], Bi[1], ACC, 0, 0, 0);     \
      }
#define KG_SQ()                                                                   \
      {                                                                             \
        const f32x2 t0 = __builtin_elementwise_fma(Ar, Ar, -(Ai * Ai));             \
        Ai = (Ar + Ar) * Ai; Ar = t0;                                               \
        const f32x2 t1 = __builtin_elementwise_fma(Br, Br, -(Bi * Bi));             \
        Bi = (Br + Br) * Bi; Br = t1;                                               \
      }
      KG_STEP(acc0); KG_SQ();
      KG_STEP(acc1); KG_SQ();
      KG_STEP(acc2); KG_SQ();
      KG_STEP(acc3);
    }
    {
      float* w = sr + wid * 1024;
#pragma unroll
      for (int j = 0; j < 4; ++j) {
        const int e = (4 * kk + j) * 16 + rc;
        w[e] = acc0[j]; w[256 + e] = acc1[j]; w[512 + e] = acc2[j]; w[768 + e] = acc3[j];
      }
    }
    __syncthreads();
    float* Kd = a.Kc + (size_t)d * KC_STRIDE;
    {
      Kd[tid + 1] = 2.f * ((sr[tid] + sr[1024 + tid]) + (sr[2048 + tid] + sr[3072 + tid]));
      if (tid >= 128) {
#pragma unroll
        for (int gI = 1; gI < 4; ++gI) {
          const int e = gI * 256 + tid;
          Kd[gI * 128 + tid + 1] = 2.f * ((sr[e] + sr[1024 + e]) + (sr[2048 + e] + sr[3072 + e]));
        }
      }
      if (wid == 0) {
        const float s0 = 2.f * wave_sum(sp[4 * lane + 2] + sp[4 * (lane + 64) + 2]);
        if (lane == 0) Kd[0] = s0;
      }
    }
    return;
  }
  b -= K1_KG_BLOCKS;
  {
    u16* st = reinterpret_cast<u16*>(smem_raw);
    wtile_load(b, tid, st, a.wg, a.wu, a.wd);
    __syncthreads();
    wtile_store(b, tid, st, a.w1i, a.wdf);
  }
}

constexpr int CV_KLEN = 2208;
constexpr int CV_USTRIDE = 2320;
constexpr int CV_OFF_U = 2 * CV_KLEN * 2;
constexpr int CV_LDS_BYTES = CV_OFF_U + 8 * CV_USTRIDE * 2;

template <int TMIN>
__device__ __forceinline__ void conv_steps(int s0, int s1, const u16* kh, const u16* kl, const u16* ub,
                                           f32x4 (&acc)[8]) {
  bf16x8 ah = *reinterpret_cast<const bf16x8*>(kh + 32 * s0);
  bf16x8 al = *reinterpret_cast<const bf16x8*>(kl + 32 * s0);
  bf16x8 bv[8];
#pragma unroll
  for (int t = TMIN; t < 8; ++t) bv[t] = *reinterpret_cast<const bf16x8*>(ub + 32 * s0 - 256 * t);
  for (int s = s0; s < s1; ++s) {
    const bf16x8 ah2 = *reinterpret_cast<const bf16x8*>(kh + 32 * s + 32);
    const bf16x8 al2 = *reinterpret_cast<const bf16x8*>(kl + 32 * s + 32);
    bf16x8 bv2[8];
#pragma unroll
    for (int t = TMIN; t < 8; ++t) bv2[t] = *reinterpret_cast<const bf16x8*>(ub + 32 * s + 32 - 256 * t);
    __builtin_amdgcn_sched_barrier(0);
#pragma unroll
    for (int t = TMIN; t < 8; ++t) {
      acc[t] = __builtin_amdgcn_mfma_f32_16x16x32_bf16(ah, bv[t], acc[t], 0, 0, 0);
      acc[t] = __builtin_amdgcn_mfma_f32_16x16x32_bf16(al, bv[t], acc[t], 0, 0, 0);
    }
    __builtin_amdgcn_sched_barrier(0);
    ah = ah2; al = al2;
#pragma unroll
    for (int t = TMIN; t < 8; ++t) bv[t] = bv2[t];
  }
}

struct K2Args {
  const float* Kc; const u16* Dskip; const u16* uT; u16* sT;
};

__device__ __forceinline__ void build_u_chunk(const u32 (&W)[8], u16* dst) {
#pragma unroll
  for (int rho = 0; rho < 8; ++rho) {
    u32 o[4];
#pragma unroll
    for (int k = 0; k < 4; ++k) {
      if ((rho & 1) == 0) {
        const int aI = (rho + 8 - 2 * k) >> 1;
        o[k] = (W[aI] & 0xffffu) | (W[aI - 1] & 0xffff0000u);
      } else {
        const int aI = (rho + 7 - 2 * k) >> 1;
        o[k] = (W[aI] >> 16) | (W[aI] << 16);
      }
    }
    *reinterpret_cast<u32x4*>(dst + rho * CV_USTRIDE) = (u32x4){o[0], o[1], o[2], o[3]};
  }
}

__global__ __launch_bounds__(256, 3) void k2_conv(K2Args a) {
  __shared__ __attribute__((aligned(16))) unsigned char smem[CV_LDS_BYTES];
  u16* sKh = reinterpret_cast<u16*>(smem);
  u16* sKl = sKh + CV_KLEN;
  u16* sU = reinterpret_cast<u16*>(smem + CV_OFF_U);
  const int tid = threadIdx.x, lane = tid & 63, wid = tid >> 6;
  const int d = blockIdx.x;
  const u16* ud = a.uT + (size_t)d * LT_;
  const float* Kd = a.Kc + (size_t)d * KC_STRIDE;
  const int q = 255 - tid;
  const u32x4 w0 = *reinterpret_cast<const u32x4*>(ud + 8 * q);
  u32x4 w1 = {0, 0, 0, 0};
  if (q < 255) w1 = *reinterpret_cast<const u32x4*>(ud + 8 * q + 8);
  float kv[8];
  {
    const int j0 = 8 * tid;
    const int e = (j0 < 256) ? 0 : (j0 < 512) ? 1 : (j0 < 1024) ? 2 : 3;
    const int base = 128 * e;
    const int hm1 = ((1 << e) >> 1) - 1;
#pragma unroll
    for (int jj = 0; jj < 8; ++jj) {
      const int j = j0 + jj;
      const int pq = (e == 0) ? j : ((j + hm1 + ((j >> e) & 1)) >> e);
      kv[jj] = Kd[base + pq];
    }
  }
  {
    const u32 W[8] = {w0[0], w0[1], w0[2], w0[3], w1[0], w1[1], w1[2], w1[3]};
    build_u_chunk(W, sU + 8 * tid);
    if (tid == 255) {
      const u32 W2[8] = {0, 0, 0, 0, w0[0], w0[1], w0[2], w0[3]};
      build_u_chunk(W2, sU + 8 * 256);
    }
    if (tid < 8 * 19) {
      const int rho = tid / 19, m = 257 + tid % 19;
      *reinterpret_cast<u32x4*>(sU + rho * CV_USTRIDE + 8 * m) = (u32x4){0, 0, 0, 0};
    } else if (tid >= 160 && tid < 200) {
      const int t = tid - 160;
      const int m = (t % 20 < 16) ? t % 20 : 256 + t % 20;
      u16* dst = (t < 20) ? sKh : sKl;
      *reinterpret_cast<u32x4*>(dst + 8 * m) = (u32x4){0, 0, 0, 0};
    }
    u32x4 ph, pl;
#pragma unroll
    for (int k = 0; k < 4; ++k) {
      const u32 hh = pack2(kv[2 * k], kv[2 * k + 1]);
      ph[k] = hh;
      pl[k] = pack2(kv[2 * k] - lo_f(hh), kv[2 * k + 1] - hi_f(hh));
    }
    *reinterpret_cast<u32x4*>(sKh + 128 + 8 * tid) = ph;
    *reinterpret_cast<u32x4*>(sKl + 128 + 8 * tid) = pl;
  }
  __syncthreads();
  const int r = lane & 15, g = lane >> 4;
  const int ap = (lane & 15) >> 3, rho = lane & 7;
  f32x4 acc[8];
#pragma unroll
  for (int t = 0; t < 8; ++t) acc[t] = (f32x4){0, 0, 0, 0};
  const u16* kh = sKh + 128 + 8 * r + 8 * g;
  const u16* kl = sKl + 128 + 8 * r + 8 * g;
  const u16* ub = sU + rho * CV_USTRIDE + 8 * g - 128 * ap + 2048;
  if (wid == 0) {
    conv_steps<0>(-4, 5, kh, kl, ub, acc);
  } else if (wid == 1) {
    conv_steps<1>(5, 13, kh, kl, ub, acc);
    conv_steps<2>(13, 16, kh, kl, ub, acc);
  } else if (wid == 2) {
    conv_steps<2>(16, 21, kh, kl, ub, acc);
    conv_steps<3>(21, 29, kh, kl, ub, acc);
    conv_steps<4>(29, 30, kh, kl, ub, acc);
  } else {
    conv_steps<4>(30, 37, kh, kl, ub, acc);
    conv_steps<5>(37, 45, kh, kl, ub, acc);
    conv_steps<6>(45, 53, kh, kl, ub, acc);
    conv_steps<7>(53, 61, kh, kl, ub, acc);
  }
  __syncthreads();
  float* sy = reinterpret_cast<float*>(smem);
#pragma unroll
  for (int t = 0; t < 8; ++t) {
#pragma unroll
    for (int j = 0; j < 4; ++j) {
      const int l = 256 * t + 128 * ap + rho + 8 * (4 * g + j);
      sy[wid * L_ + l] = acc[t][j];
    }
  }
  __syncthreads();
  const float dsk = bf2f(a.Dskip[d]);
  {
    const int l0 = q * 8;
    float y[8];
#pragma unroll
    for (int hh = 0; hh < 2; ++hh) {
      f32x4 a0 = *reinterpret_cast<const f32x4*>(sy + l0 + 4 * hh);
      f32x4 a1 = *reinterpret_cast<const f32x4*>(sy + L_ + l0 + 4 * hh);
      f32x4 a2 = *reinterpret_cast<const f32x4*>(sy + 2 * L_ + l0 + 4 * hh);
      f32x4 a3 = *reinterpret_cast<const f32x4*>(sy + 3 * L_ + l0 + 4 * hh);
#pragma unroll
      for (int j = 0; j < 4; ++j) y[4 * hh + j] = (a0[j] + a1[j]) + (a2[j] + a3[j]);
    }
    u32 o[4];
#pragma unroll
    for (int k = 0; k < 4; ++k) {
      const float t0 = rbf(lo_f(w0[k]) * dsk), t1 = rbf(hi_f(w0[k]) * dsk);
      o[k] = pack2(rbf(y[2 * k]) + t0, rbf(y[2 * k + 1]) + t1);
    }
    *reinterpret_cast<u32x4*>(a.sT + (size_t)d * LT_ + l0) = (u32x4){o[0], o[1], o[2], o[3]};
  }
}

struct LN2Args {
  const u16* sT; const u16* x; u16* h; u16* xni;
};
constexpr int LN2_BLOCKS = L_ / 16;

__global__ __launch_bounds__(1024) void k_ln2(LN2Args a) {
  __shared__ __attribute__((aligned(16))) u16 ss[16 * 648];
  const int tid = threadIdx.x, lane = tid & 63, wid = tid >> 6;
  const u16* __restrict__ sT = a.sT; const u16* __restrict__ x = a.x;
  u16* __restrict__ h = a.h; u16* __restrict__ xni = a.xni;
  const int row0 = blockIdx.x * 16;
  const int row = row0 + wid;
  u32 xv[5];
  {
    const u32* xr = reinterpret_cast<const u32*>(x + (size_t)row * D_);
#pragma unroll
    for (int i = 0; i < 5; ++i) xv[i] = xr[lane + 64 * i];
  }
  if (tid < D_) {
    const int d = tid;
    const u32x4* src = reinterpret_cast<const u32x4*>(sT + (size_t)d * LT_ + row0);
    const u32x4 a0 = src[0], a1 = src[1];
#pragma unroll
    for (int k = 0; k < 4; ++k) {
      ss[(2 * k) * 648 + d] = (u16)(a0[k] & 0xffffu);
      ss[(2 * k + 1) * 648 + d] = (u16)(a0[k] >> 16);
      ss[(8 + 2 * k) * 648 + d] = (u16)(a1[k] & 0xffffu);
      ss[(8 + 2 * k + 1) * 648 + d] = (u16)(a1[k] >> 16);
    }
  }
  __syncthreads();
  {
    float f[10], o[10];
    u32* hrow = reinterpret_cast<u32*>(h + (size_t)row * D_);
#pragma unroll
    for (int i = 0; i < 5; ++i) {
      const u32 sv = *reinterpret_cast<const u32*>(ss + wid * 648 + 2 * (lane + 64 * i));
      const u32 hv = pack2(lo_f(xv[i]) + lo_f(sv), hi_f(xv[i]) + hi_f(sv));
      hrow[lane + 64 * i] = hv;
      f[2 * i] = lo_f(hv); f[2 * i + 1] = hi_f(hv);
    }
    ln_row(f, o);
    const int mt = row >> 7, r = row & 127;
#pragma unroll
    for (int i = 0; i < 5; ++i) {
      const int c = 2 * (lane + 64 * i);
      const int kt = c >> 6, cc = c & 63;
      const int off = ((mt * 10 + kt) * 128 + r) * 64 + (((cc >> 3) ^ (r & 7)) << 3) + (cc & 7);
      *reinterpret_cast<u32*>(xni + off) = pack2(o[2 * i], o[2 * i + 1]);
    }
  }
}

__device__ __forceinline__ float gelu_tanh(float x) {
  const float c0 = -2.f * 0.7978845608028654f * 1.4426950408889634f;
  const float c1 = c0 * 0.044715f;
  const float e = __builtin_amdgcn_exp2f(x * fmaf(x * x, c1, c0));
  return x * __builtin_amdgcn_rcpf(1.f + e);
}

constexpr int G1_A_BYTES = 16384;
constexpr int G1_B_BYTES = 7 * 4096;
constexpr int G1_STAGE = G1_A_BYTES + G1_B_BYTES;
constexpr int G1_NSTG = 3;

template <int NG>
__device__ __forceinline__ void g1_ldfrag(const unsigned char* sb, int arow, int brow, int cpos,
                                          bf16x8 (&xf)[2], bf16x8 (&wf)[8]) {
#pragma unroll
  for (int m = 0; m < 2; ++m)
    xf[m] = *reinterpret_cast<const bf16x8*>(sb + (arow + m * 16) * 128 + cpos);
#pragma unroll
  for (int n = 0; n < 2 * NG; ++n)
    wf[n] = *reinterpret_cast<const bf16x8*>(sb + G1_A_BYTES + (brow + n * 16) * 128 + cpos);
}
template <int NG>
__device__ __forceinline__ void g1_mma(const bf16x8 (&xf)[2], const bf16x8 (&wf)[8], f32x4 (&acc)[2][8]) {
#pragma unroll
  for (int n = 0; n < 2 * NG; ++n)
#pragma unroll
    for (int m = 0; m < 2; ++m)
      acc[m][n] = __builtin_amdgcn_mfma_f32_16x16x32_bf16(wf[n], xf[m], acc[m][n], 0, 0, 0);
}

template <int NG>
__device__ __forceinline__ void g1_main(unsigned char* smem, const unsigned char* Ag, const unsigned char* Bg,
                                        int g0, int wid, int lane, int arow, int brow, int cpos0, int cpos1,
                                        f32x4 (&acc)[2][8]) {
#define G1_ISSUE(kt, buf)                                                                              \
  {                                                                                                    \
    unsigned char* sb_ = smem + (buf) * G1_STAGE;                                                      \
    _Pragma("unroll") for (int i = 0; i < 2; ++i) {                                                    \
      const int piece = wid + 8 * i;                                                                   \
      __builtin_amdgcn_global_load_lds(                                                                \
          (const __attribute__((address_space(1))) void*)(Ag + (size_t)(kt) * G1_A_BYTES + piece * 1024 + lane * 16), \
          (__attribute__((address_space(3))) void*)(sb_ + piece * 1024), 16, 0, 0);                    \
    }                                                                                                  \
    _Pragma("unroll") for (int i = 0; i < 4; ++i) {                                                    \
      int pb = wid + 8 * i; if (pb >= 28) pb -= 4;                                                     \
      const int gi = pb >> 2;                                                                          \
      const int grp = (g0 + gi < 108) ? g0 + gi : 107;                                                 \
      __builtin_amdgcn_global_load_lds(                                                                \
          (const __attribute__((address_space(1))) void*)(Bg + ((size_t)((kt) * 108 + grp) * 4096) + (pb & 3) * 1024 + lane * 16), \
          (__attribute__((address_space(3))) void*)(sb_ + G1_A_BYTES + pb * 1024), 16, 0, 0);          \
    }                                                                                                  \
  }
  bf16x8 xf0[2], wf0[8], xf1[2], wf1[8];
  G1_ISSUE(0, 0);
  G1_ISSUE(1, 1);
  G1_ISSUE(2, 2);
  asm volatile("s_waitcnt vmcnt(12)" ::: "memory");
  __builtin_amdgcn_s_barrier();
  asm volatile("" ::: "memory");
  g1_ldfrag<NG>(smem, arow, brow, cpos0, xf0, wf0);
#pragma unroll
  for (int kt = 0; kt < 10; ++kt) {
    const int buf = kt % 3;
    const unsigned char* sb = smem + buf * G1_STAGE;
    const int nbuf = (buf == 2) ? 0 : buf + 1;
    g1_ldfrag<NG>(sb, arow, brow, cpos1, xf1, wf1);
    g1_mma<NG>(xf0, wf0, acc);
    if (kt < 9) {
      if (kt < 8) asm volatile("s_waitcnt vmcnt(6) lgkmcnt(0)" ::: "memory");
      else asm volatile("s_waitcnt vmcnt(0) lgkmcnt(0)" ::: "memory");
      __builtin_amdgcn_s_barrier();
      asm volatile("" ::: "memory");
      g1_ldfrag<NG>(smem + nbuf * G1_STAGE, arow, brow, cpos0, xf0, wf0);
      __builtin_amdgcn_sched_barrier(0);
      if (kt + 3 < 10) G1_ISSUE(kt + 3, buf);
    }
    g1_mma<NG>(xf1, wf1, acc);
  }
}

__global__ __launch_bounds__(512) void k3_gemm1(const u16* __restrict__ xni, const u16* __restrict__ w1i,
                                                u16* __restrict__ hmf) {
  __shared__ __attribute__((aligned(16))) unsigned char smem[G1_NSTG * G1_STAGE];
  const int tid = threadIdx.x, lane = tid & 63;
  const int wid = __builtin_amdgcn_readfirstlane(tid >> 6);
  const int wm = wid & 3, wn = wid >> 2;
  const int fr = lane & 15, fq = lane >> 4;
  const int bid = blockIdx.x;
  const int xcd = bid & 7, idx = bid >> 3;
  const int mt = (xcd & 1) * 8 + (idx & 7);
  const int nb = (xcd >> 1) * 4 + (idx >> 3);
  const int g0 = 27 * (nb >> 2) + 7 * (nb & 3);
  const int ng = ((nb & 3) == 3) ? 6 : 7;
  const int gw0 = wn * 4;
  const int gwn = wn ? ng - 4 : 4;
  const unsigned char* Ag = reinterpret_cast<const unsigned char*>(xni) + (size_t)mt * 10 * G1_A_BYTES;
  const unsigned char* Bg = reinterpret_cast<const unsigned char*>(w1i);
  f32x4 acc[2][8];
#pragma unroll
  for (int m = 0; m < 2; ++m)
#pragma unroll
    for (int n = 0; n < 8; ++n) acc[m][n] = (f32x4){0, 0, 0, 0};
  const int swz = fr & 7;
  const int cpos0 = ((0 + fq) ^ swz) << 4, cpos1 = ((4 + fq) ^ swz) << 4;
  const int arow = wm * 32 + fr, brow = gw0 * 32 + fr;
  if (wn == 0) g1_main<4>(smem, Ag, Bg, g0, wid, lane, arow, brow, cpos0, cpos1, acc);
  else g1_main<3>(smem, Ag, Bg, g0, wid, lane, arow, brow, cpos0, cpos1, acc);
#pragma unroll
  for (int m = 0; m < 2; ++m) {
    const int mt16 = mt * 8 + wm * 2 + m;
#pragma unroll
    for (int gI = 0; gI < 4; ++gI) {
      if (gI < gwn) {
        float o[4];
#pragma unroll
        for (int j = 0; j < 4; ++j) {
          const float gv = rbf(acc[m][2 * gI][j]);
          const float uv = rbf(acc[m][2 * gI + 1][j]);
          o[j] = rbf(gelu_tanh(gv)) * uv;
        }
        const int f = 16 * (g0 + gw0 + gI) + 4 * fq;
        const int ks = f >> 5, fq2 = (f & 31) >> 3, j0 = f & 7;
        u16* dst = hmf + ((size_t)(mt16 * 54 + ks) * 64 + fr + 16 * fq2) * 8 + j0;
        *reinterpret_cast<u32x2*>(dst) = (u32x2){pack2(o[0], o[1]), pack2(o[2], o[3])};
      }
    }
  }
}

__global__ __launch_bounds__(256) void k4_gemm2(const u16* __restrict__ hmf, const u16* __restrict__ wdf,
                                                const u16* __restrict__ h, u16* __restrict__ out) {
  __shared__ __attribute__((aligned(16))) float sred[4 * 64 * 84];
  const int tid = threadIdx.x, lane = tid & 63;
  const int wk = __builtin_amdgcn_readfirstlane(tid >> 6);
  const int fr = lane & 15, fq = lane >> 4;
  const int bid = blockIdx.x;
  const int xcd = bid & 7, idx = bid >> 3;
  const int mt = (xcd >> 1) * 8 + (idx & 7);
  const int nb = (xcd & 1) * 4 + (idx >> 3);
  const int ks0 = (wk < 2) ? wk * 14 : 28 + (wk - 2) * 13;
  const int nks = (wk < 2) ? 14 : 13;
  const bf16x8* Ap = reinterpret_cast<const bf16x8*>(hmf) + (size_t)(mt * 4) * 54 * 64 + lane;
  const bf16x8* Bp = reinterpret_cast<const bf16x8*>(wdf) + (size_t)(nb * 5) * 54 * 64 + lane;
  const int rl = tid >> 2, c0 = (tid & 3) * 20;
  const int row = mt * 64 + rl;
  u32x2 hres[5];
  {
    const u16* hr = h + (size_t)row * D_ + nb * 80 + c0;
#pragma unroll
    for (int i = 0; i < 5; ++i) hres[i] = *reinterpret_cast<const u32x2*>(hr + 4 * i);
  }
  f32x4 acc[4][5];
#pragma unroll
  for (int m = 0; m < 4; ++m)
#pragma unroll
    for (int n = 0; n < 5; ++n) acc[m][n] = (f32x4){0, 0, 0, 0};
  bf16x8 xa[4][4], wb[4][5];
#define G2_LOAD(slot, kidx)                                                              \
  {                                                                                      \
    const int kq_ = ks0 + (((kidx) < nks) ? (kidx) : nks - 1);                           \
    _Pragma("unroll") for (int m = 0; m < 4; ++m) xa[slot][m] = Ap[(m * 54 + kq_) * 64];   \
    _Pragma("unroll") for (int n = 0; n < 5; ++n) wb[slot][n] = Bp[(n * 54 + kq_) * 64];   \
  }
#define G2_MMA(slot)                                                                     \
  {                                                                                      \
    _Pragma("unroll") for (int n = 0; n < 5; ++n)                                        \
    _Pragma("unroll") for (int m = 0; m < 4; ++m)                                        \
      acc[m][n] = __builtin_amdgcn_mfma_f32_16x16x32_bf16(wb[slot][n], xa[slot][m], acc[m][n], 0, 0, 0); \
  }
  G2_LOAD(0, 0);
  G2_LOAD(1, 1);
  G2_LOAD(2, 2);
  __builtin_amdgcn_sched_barrier(0);
#pragma unroll
  for (int i = 0; i < 13; ++i) {
    if (i + 3 < 14) G2_LOAD((i + 3) & 3, i + 3);
    __builtin_amdgcn_sched_barrier(0);
    G2_MMA(i & 3);
    __builtin_amdgcn_sched_barrier(0);
  }
  if (nks == 14) G2_MMA(13 & 3);
  float* sw = sred + wk * 64 * 84;
#pragma unroll
  for (int m = 0; m < 4; ++m)
#pragma unroll
    for (int n = 0; n < 5; ++n)
      *reinterpret_cast<f32x4*>(sw + (m * 16 + fr) * 84 + n * 16 + 4 * fq) = acc[m][n];
  __syncthreads();
  {
    u16* orow = out + (size_t)row * D_ + nb * 80 + c0;
#pragma unroll
    for (int i = 0; i < 5; ++i) {
      const float* p = sred + rl * 84 + c0 + 4 * i;
      const f32x4 a0 = *reinterpret_cast<const f32x4*>(p);
      const f32x4 a1 = *reinterpret_cast<const f32x4*>(p + 64 * 84);
      const f32x4 a2 = *reinterpret_cast<const f32x4*>(p + 2 * 64 * 84);
      const f32x4 a3 = *reinterpret_cast<const f32x4*>(p + 3 * 64 * 84);
      float o[4];
#pragma unroll
      for (int j = 0; j < 4; ++j) o[j] = rbf((a0[j] + a1[j]) + (a2[j] + a3[j]));
      const u32 r0 = pack2(lo_f(hres[i][0]) + o[0], hi_f(hres[i][0]) + o[1]);
      const u32 r1 = pack2(lo_f(hres[i][1]) + o[2], hi_f(hres[i][1]) + o[3]);
      *reinterpret_cast<u32x2*>(orow + 4 * i) = (u32x2){r0, r1};
    }
  }
}

extern "C" void kernel_launch(void* const* d_in, const int* in_sizes, int n_in,
                              void* d_out, int out_size, void* d_ws, size_t ws_size,
                              hipStream_t stream) {
  const u16* x = (const u16*)d_in[0];
  const u16* logA = (const u16*)d_in[1];
  const u16* Aim = (const u16*)d_in[2];
  const u16* Cr = (const u16*)d_in[3];
  const u16* Ci = (const u16*)d_in[4];
  const u16* logdt = (const u16*)d_in[5];
  const u16* Dskip = (const u16*)d_in[6];
  const u16* wg = (const u16*)d_in[8];
  const u16* wu = (const u16*)d_in[9];
  const u16* wd = (const u16*)d_in[10];
  u16* out = (u16*)d_out;

  char* ws = (char*)d_ws;
  size_t off = 0;
  auto alloc = [&](size_t bytes) { char* p = ws + off; off += (bytes + 255) & ~(size_t)255; return p; };
  u16* uT = (u16*)alloc((size_t)D_ * LT_ * 2);
  u16* sT = (u16*)alloc((size_t)D_ * LT_ * 2);
  u16* h = (u16*)alloc((size_t)L_ * D_ * 2);
  u16* xni = (u16*)alloc((size_t)L_ * D_ * 2);
  u16* hmf = (u16*)alloc((size_t)L_ * F_ * 2);
  u16* w1i = (u16*)alloc((size_t)2 * F_ * D_ * 2);
  u16* wdf = (u16*)alloc((size_t)D_ * F_ * 2);
  float* Kc = (float*)alloc((size_t)D_ * KC_STRIDE * 4);

  K1Args a;
  a.x = x; a.wg = wg; a.wu = wu; a.wd = wd;
  a.logA = logA; a.Aim = Aim; a.Cr = Cr; a.Ci = Ci; a.logdt = logdt;
  a.uT = uT; a.w1i = w1i; a.wdf = wdf; a.Kc = Kc;
  k1_kernel<<<K1_BLOCKS, 256, 0, stream>>>(a);
  K2Args a2;
  a2.Kc = Kc; a2.Dskip = Dskip; a2.uT = uT; a2.sT = sT;
  k2_conv<<<D_, 256, 0, stream>>>(a2);
  LN2Args a3;
  a3.sT = sT; a3.x = x; a3.h = h; a3.xni = xni;
  k_ln2<<<LN2_BLOCKS, 1024, 0, stream>>>(a3);
  k3_gemm1<<<256, 512, 0, stream>>>(xni, w1i, hmf);
  k4_gemm2<<<256, 256, 0, stream>>>(hmf, wdf, h, out);
}
```
